```python
import torch

def _softmax(x, axis=-1):
    return torch.softmax(x, dim=axis)

def _gelu(x):
    return torch.nn.functional.gelu(x, approximate='tanh')

def _silu(x):
    return torch.nn.functional.silu(x)

def _sigmoid(x):
    return torch.sigmoid(x)

def _layernorm(x, axis=-1, eps=1e-05):
    m = x.mean(dim=axis, keepdim=True)
    v = x.var(dim=axis, keepdim=True, unbiased=False)
    return (x - m) / torch.sqrt(v + eps)

def _scatter_add(tgt, idx, src, axis=0):
    return tgt.clone().index_add_(axis, idx, src)

def _segment_sum(src, seg, n):
    out = torch.zeros((n,) + src.shape[1:], dtype=src.dtype)
    return out.index_add_(0, seg, src)

def _pad(x, pw):
    flat = tuple((v for pair in reversed(pw) for v in pair))
    return torch.nn.functional.pad(x, flat)
kernel_name = 'sequential_preLN__ssm_conv_s4d__mixer_gmlp__prompt'

def setup_inputs():

    _w = lambda a: (lambda t: t.to(torch.bfloat16) if t.is_floating_point() else t)(torch.as_tensor(a))

    def _A_setup():
        Bsz, L, D, N = (1, 2048, 640, 128)
        return {'u': _w((lambda *s: torch.randn(*s, dtype=torch.bfloat16) * 0.05)(Bsz, L, D)), 'log_A_real': _w((lambda *s: torch.randn(*s, dtype=torch.bfloat16) * 6.4)(D, N) * 0.5), 'A_imag': _w((lambda *s: torch.randn(*s, dtype=torch.bfloat16) * 0.05)(D, N) * 3.14), 'C_real': _w((lambda *s: torch.randn(*s, dtype=torch.bfloat16) * 0.05)(D, N) / N ** 0.5), 'C_imag': _w((lambda *s: torch.randn(*s, dtype=torch.bfloat16) * 0.05)(D, N) / N ** 0.5), 'log_dt': _w((lambda *s: torch.randn(*s, dtype=torch.bfloat16) * 0.05)(D) * 0.5 - 4.0), 'D_skip': _w((lambda *s: torch.randn(*s, dtype=torch.bfloat16) * 0.05)(D))}
    _A_in = _A_setup()

    def _B_setup():
        B, S, D, F = (1, 2048, 640, 1728)
        Fh = F // 2
        return {'x': _w((lambda *s: torch.randn(*s, dtype=torch.bfloat16) * 0.05)(B, S, D)), 'w_in': _w((lambda *s: torch.randn(*s, dtype=torch.bfloat16) * 0.4)(D, 2 * Fh)), 'w_sp': _w(0.01 * (lambda *s: torch.randn(*s, dtype=torch.bfloat16) * 0.4)(S, S)), 'b_sp': _w(1.0 + 0.01 * (lambda *s: torch.randn(*s, dtype=torch.bfloat16) * 0.05)(S)), 'w_out': _w((lambda *s: torch.randn(*s, dtype=torch.bfloat16) * 0.05)(Fh, D))}
    _B_in = _B_setup()
    return {'A_u': _A_in['u'], 'A_log_A_real': _A_in['log_A_real'], 'A_A_imag': _A_in['A_imag'], 'A_C_real': _A_in['C_real'], 'A_C_imag': _A_in['C_imag'], 'A_log_dt': _A_in['log_dt'], 'A_D_skip': _A_in['D_skip'], 'B_x': _B_in['x'], 'B_w_in': _B_in['w_in'], 'B_w_sp': _B_in['w_sp'], 'B_b_sp': _B_in['b_sp'], 'B_w_out': _B_in['w_out']}

def reference(A_u, A_log_A_real, A_A_imag, A_C_real, A_C_imag, A_log_dt, A_D_skip, B_x, B_w_in, B_w_sp, B_b_sp, B_w_out):

    def _A_ref(u, log_A_real, A_imag, C_real, C_imag, log_dt, D_skip):
        L = u.shape[1]
        dt = torch.exp(log_dt)
        A = -torch.exp(log_A_real) + 1j * A_imag
        C = (C_real + 1j * C_imag) * dt[:, None]
        pos = (lambda x, dt: x.to(dt))(torch.arange(L), log_dt.dtype)
        P = torch.exp((A * dt[:, None])[:, :, None] * pos[None, None, :])
        K = 2.0 * torch.einsum('dn,dnl->dl', C, P).real
        L2 = 2 * L
        u_t = (lambda x, *d: x.permute(*d))(u, 0, 2, 1)
        Kf = (lambda x, *a, **k: torch.fft.rfft(x.float(), *a, **k))(K, L2)
        Uf = (lambda x, *a, **k: torch.fft.rfft(x.float(), *a, **k))(u_t, L2)
        y = (lambda x, *a, **k: torch.fft.irfft(x, *a, **k).to(torch.bfloat16))(Uf * Kf[None, :, :], L2)[:, :, :L]
        y = (lambda x, *d: x.permute(*d))(y, 0, 2, 1)
        return y + u * D_skip[None, None, :]

    def _B_ref(x, w_in, w_sp, b_sp, w_out):
        B, S, D = x.shape
        F = w_in.shape[-1]
        Fh = F // 2
        z = _gelu(torch.einsum('bsd,df->bsf', x, w_in))
        z1 = z[..., :Fh].clone()
        z2 = _layernorm(z[..., Fh:], axis=-1)
        g = torch.einsum('ts,bsf->btf', w_sp, z2) + b_sp[None, :, None]
        return torch.einsum('bsf,fd->bsd', z1 * g, w_out)
    x = A_u
    h = x + _A_ref(_layernorm(x, axis=-1), A_log_A_real, A_A_imag, A_C_real, A_C_imag, A_log_dt, A_D_skip)
    h2 = h + _B_ref(_layernorm(h, axis=-1), B_w_in, B_w_sp, B_b_sp, B_w_out)
    return h2
```

```cpp
#include <hip/hip_runtime.h>
#include <hip/hip_bf16.h>
typedef unsigned short u16;
__device__ __forceinline__ float b2f(u16 v){ return __uint_as_float(((unsigned)v)<<16); }
__device__ __forceinline__ u16 f2b(float f){ __hip_bfloat16 h = __float2bfloat16(f); return *(u16*)&h; }
__device__ __forceinline__ float rb(float f){ return b2f(f2b(f)); }
constexpr int L=2048, D=640, N=128, F=1728, FH=864;

__global__ void ln_rows(const u16* x, int ld, int n, u16* o, int ldo){
  int r=blockIdx.x; int lane=threadIdx.x;
  float s=0; for(int i=lane;i<n;i+=64) s+=b2f(x[r*ld+i]);
  for(int o2=32;o2;o2>>=1) s+=__shfl_xor(s,o2);
  float m=s/n; float q=0; for(int i=lane;i<n;i+=64){float d=b2f(x[r*ld+i])-m; q+=d*d;}
  for(int o2=32;o2;o2>>=1) q+=__shfl_xor(q,o2);
  float mb=rb(m), vb=rb(q/n), sb=rb(sqrtf(rb(vb+1e-5f)));
  for(int i=lane;i<n;i+=64) o[r*ldo+i]=f2b(rb(b2f(x[r*ld+i])-mb)/sb);
}
__global__ void kgen(const u16* la,const u16* ai,const u16* cr,const u16* ci,const u16* ldt,float* K){
  int d=blockIdx.y; int l=blockIdx.x*256+threadIdx.x;
  float dt=rb(expf(b2f(ldt[d]))); float p=rb((float)l); float acc=0;
  for(int n=0;n<N;n++){ float a=-rb(expf(b2f(la[d*N+n])))*dt, b=b2f(ai[d*N+n])*dt; float c1=b2f(cr[d*N+n])*dt,c2=b2f(ci[d*N+n])*dt;
    float e=expf(a*p); float sn,cs; sincosf(b*p,&sn,&cs); acc+=e*(c1*cs-c2*sn);}
  K[d*L+l]=2.f*acc;
}
__global__ void conv(const u16* x,const u16* u,const float* K,const u16* Ds,u16* h){
  int t=blockIdx.y; int d=blockIdx.x*64+threadIdx.x; float acc=0;
  for(int s=0;s<=t;s++) acc+=K[d*L+(t-s)]*b2f(u[s*D+d]);
  float y=rb(acc); float a=rb(y+rb(b2f(u[t*D+d])*b2f(Ds[d])));
  h[t*D+d]=f2b(b2f(x[t*D+d])+a);
}
template<int MODE> __global__ void gemm(const u16* A,int lda,const u16* B,int ldb,int K,u16* C,int ldc,const u16* e1,const u16* e2){
  int m=blockIdx.y; int n=blockIdx.x*32+threadIdx.x; float acc=0;
  for(int k=0;k<K;k++) acc+=b2f(A[m*lda+k])*b2f(B[k*ldb+n]);
  float v=rb(acc);
  if(MODE==0){ float g=0.5f*v*(1.f+tanhf(0.7978845608f*(v+0.044715f*v*v*v))); C[m*ldc+n]=f2b(g);}
  if(MODE==1){ float g=rb(v+b2f(e1[m])); C[m*ldc+n]=f2b(g*b2f(e2[m*F+n])); }
  if(MODE==2){ C[m*ldc+n]=f2b(v+b2f(e1[m*D+n])); }
}
extern "C" void kernel_launch(void* const* d_in, const int* in_sizes, int n_in, void* d_out, int out_size, void* d_ws, size_t ws_size, hipStream_t stream) {
  const u16* x=(const u16*)d_in[0];
  char* w=(char*)d_ws;
  u16* u=(u16*)w; w+=L*D*2; float* K=(float*)w; w+=D*L*4; u16* h=(u16*)w; w+=L*D*2; u16* hn=(u16*)w; w+=L*D*2; u16* z=(u16*)w; w+=L*F*2; u16* z2=(u16*)w; w+=L*FH*2; u16* v=(u16*)w;
  ln_rows<<<L,64,0,stream>>>(x,D,D,u,D);
  kgen<<<dim3(L/256,D),256,0,stream>>>((const u16*)d_in[1],(const u16*)d_in[2],(const u16*)d_in[3],(const u16*)d_in[4],(const u16*)d_in[5],K);
  conv<<<dim3(D/64,L),64,0,stream>>>(x,u,K,(const u16*)d_in[6],h);
  ln_rows<<<L,64,0,stream>>>(h,D,D,hn,D);
  gemm<0><<<dim3(F/32,L),32,0,stream>>>(hn,D,(const u16*)d_in[8],F,D,z,F,nullptr,nullptr);
  ln_rows<<<L,64,0,stream>>>(z+FH,F,FH,z2,FH);
  gemm<1><<<dim3(FH/32,L),32,0,stream>>>((const u16*)d_in[9],L,z2,FH,L,v,FH,(const u16*)d_in[10],z);
  gemm<2><<<dim3(D/32,L),32,0,stream>>>(v,FH,(const u16*)d_in[11],D,FH,(u16*)d_out,D,h,nullptr);
}
```

```cpp
#include <hip/hip_runtime.h>

typedef unsigned short u16;
typedef unsigned long long u64;
typedef __attribute__((ext_vector_type(8))) __bf16 bf8;
typedef __attribute__((ext_vector_type(2))) __bf16 bf2;
typedef __attribute__((ext_vector_type(8))) _Float16 h8;
typedef __attribute__((ext_vector_type(2))) _Float16 h2;
typedef __attribute__((ext_vector_type(4))) float f4;
typedef __attribute__((ext_vector_type(2))) float f2;
typedef __attribute__((ext_vector_type(4))) short s4;
typedef __attribute__((ext_vector_type(4))) unsigned u4;
typedef __attribute__((ext_vector_type(2))) unsigned u2;
#define LDSP __attribute__((address_space(3)))
#define DEVI __device__ __forceinline__

constexpr int L = 2048, D = 640, NM = 128, F = 1728, FH = 864;
constexpr int NGB = 54;
constexpr int NKS1 = 20;
constexpr int NKS3 = 27;

constexpr size_t OFF_UT = 0;
constexpr size_t OFF_XT = OFF_UT + (size_t)D * L * 2;
constexpr size_t OFF_KR = OFF_XT + (size_t)D * L * 2;
constexpr size_t OFF_HT = OFF_KR + (size_t)D * L * 4;
constexpr size_t OFF_WF = OFF_HT + (size_t)D * L * 2;
constexpr size_t OFF_WO = OFF_WF + (size_t)2 * NGB * NKS1 * 1024;
constexpr size_t OFF_Z1 = OFF_WO + (size_t)40 * NKS3 * 1024;
constexpr size_t OFF_Z2 = OFF_Z1 + (size_t)128 * NGB * 512;
constexpr size_t OFF_V = OFF_Z2 + (size_t)64 * NGB * 1024;
constexpr size_t OFF_ST = OFF_V + (size_t)128 * NKS3 * 1024;
constexpr size_t OFF_CSP = OFF_ST + (size_t)32 * 8 * 64 * 8;
constexpr size_t OFF_CS = OFF_CSP + (size_t)5 * F * 4;
constexpr size_t OFF_KSC = OFF_CS + (size_t)F * 4;

DEVI float bflo(unsigned v) { return __uint_as_float(v << 16); }
DEVI float bfhi(unsigned v) { return __uint_as_float(v & 0xffff0000u); }
DEVI unsigned pk_bf(float a, float b) { f2 p = {a, b}; return __builtin_bit_cast(unsigned, __builtin_convertvector(p, bf2)); }
DEVI unsigned pk_h(float a, float b) { f2 p = {a, b}; return __builtin_bit_cast(unsigned, __builtin_convertvector(p, h2)); }
DEVI float rbf(float a) { return bflo(pk_bf(a, 0.f)); }
DEVI float hlo(unsigned v) { return (float)__builtin_bit_cast(h2, v).x; }
DEVI float hhi(unsigned v) { return (float)__builtin_bit_cast(h2, v).y; }
DEVI f4 mfma_bf(u4 a, u4 b, f4 c) { return __builtin_amdgcn_mfma_f32_16x16x32_bf16(__builtin_bit_cast(bf8, a), __builtin_bit_cast(bf8, b), c, 0, 0, 0); }
DEVI f4 mfma_h(u4 a, u4 b, f4 c) { return __builtin_amdgcn_mfma_f32_16x16x32_f16(__builtin_bit_cast(h8, a), __builtin_bit_cast(h8, b), c, 0, 0, 0); }
template <int CTRL> DEVI float dpp(float v) { return __int_as_float(__builtin_amdgcn_update_dpp(0, __float_as_int(v), CTRL, 0xf, 0xf, true)); }
constexpr int DPP_ROR1 = 0x121, DPP_ROR2 = 0x122, DPP_ROR4 = 0x124, DPP_ROR8 = 0x128, DPP_MIRROR = 0x140, DPP_HMIRROR = 0x141, DPP_Q3210 = 0x1B, DPP_Q1032 = 0xB1;

DEVI f2 ln_stats(float s, float q, float inv_n) {
  float m = s * inv_n;
  float v = fmaxf(q * inv_n - m * m, 0.f);
  float sd = rbf(__builtin_sqrtf(rbf(rbf(v) + 1e-5f)));
  f2 r = {rbf(m), 1.0f / sd};
  return r;
}
DEVI float gelu_t(float x) {
  float t = __builtin_fmaf(x * x, -0.1029432397f, -2.3022081985f);
  float e = __builtin_amdgcn_exp2f(x * t);
  return x * __builtin_amdgcn_rcpf(1.0f + e);
}

template <int IMM> DEVI void gld16(u4& d, unsigned voff, const void* sb) { asm volatile("global_load_dwordx4 %0, %1, %2 offset:%3" : "=v"(d) : "v"(voff), "s"(sb), "i"(IMM)); }
template <int IMM> DEVI void gld8(u2& d, unsigned voff, const void* sb) { asm volatile("global_load_dwordx2 %0, %1, %2 offset:%3" : "=v"(d) : "v"(voff), "s"(sb), "i"(IMM)); }
template <int IMM> DEVI void gld4(unsigned& d, unsigned voff, const void* sb) { asm volatile("global_load_dword %0, %1, %2 offset:%3" : "=v"(d) : "v"(voff), "s"(sb), "i"(IMM)); }
DEVI void gld2(unsigned& d, unsigned voff, const void* sb) { asm volatile("global_load_ushort %0, %1, %2" : "=v"(d) : "v"(voff), "s"(sb)); }
DEVI void dma16(unsigned ldsdst, unsigned voff, const void* sb) {
  asm volatile("s_mov_b32 m0, %0\n\ts_nop 0\n\tglobal_load_lds_dwordx4 %1, %2" ::"s"(ldsdst), "v"(voff), "s"(sb) : "memory");
}
template <int N> DEVI void waitv() { asm volatile("s_waitcnt vmcnt(%0)" ::"i"(N) : "memory"); }
template <int N> DEVI void waitv(u4& a) { asm volatile("s_waitcnt vmcnt(%1)" : "+v"(a) : "i"(N) : "memory"); }
DEVI void pin(u4& a) { asm volatile("" : "+v"(a)); }
DEVI void pin(u2& a) { asm volatile("" : "+v"(a)); }
DEVI void pin(unsigned& a) { asm volatile("" : "+v"(a)); }
DEVI void bar() { asm volatile("s_waitcnt lgkmcnt(0)\n\ts_barrier" ::: "memory"); }
DEVI unsigned lds_addr(const void* p) { return (unsigned)(size_t)(LDSP const char*)p; }

DEVI void tr8x8(const u4 (&r)[8], u4 (&o)[8]) {
#pragma unroll
  for (int c2 = 0; c2 < 4; ++c2)
#pragma unroll
    for (int m = 0; m < 4; ++m) {
      o[2 * c2][m] = __builtin_amdgcn_perm(r[2 * m + 1][c2], r[2 * m][c2], 0x05040100u);
      o[2 * c2 + 1][m] = __builtin_amdgcn_perm(r[2 * m + 1][c2], r[2 * m][c2], 0x07060302u);
    }
}

constexpr int RO = 2;
constexpr int NB_LN = L / (8 * RO);
constexpr int NB_KG = D;
constexpr int NB_WI = 14 * 5;
constexpr int NB_WO = 34;
constexpr int KG_PITCH = 528;
constexpr int K1_LDS = 64 * KG_PITCH + 1312;

struct cf { float x, y; };
DEVI cf cmul(cf a, cf b) { cf r; r.x = a.x * b.x - a.y * b.y; r.y = a.x * b.y + a.y * b.x; return r; }
DEVI cf cexpp(float a2, float br, float p) {
  float e = __builtin_amdgcn_exp2f(a2 * p);
  float ph = __builtin_amdgcn_fractf(br * p);
  cf r; r.x = e * __builtin_amdgcn_cosf(ph); r.y = e * __builtin_amdgcn_sinf(ph); return r;
}

__global__ __launch_bounds__(256, 4) void k1(const u16* __restrict__ x, const u16* __restrict__ logA, const u16* __restrict__ Aim,
                                          const u16* __restrict__ Cre, const u16* __restrict__ Cim, const u16* __restrict__ logdt,
                                          const u16* __restrict__ w_in, const u16* __restrict__ w_out, char* __restrict__ ws) {
  __shared__ __attribute__((aligned(16))) char sm[K1_LDS];
  const int tid = threadIdx.x, lane = tid & 63;
  const int w = __builtin_amdgcn_readfirstlane(tid >> 6);
  int b = blockIdx.x;
  if (b < NB_LN) {
    const int rg = (b & 7) * (NB_LN / 8) + (b >> 3);
    const int ro = tid % RO, co = tid / RO;
    const bool valid = co < 80;
    const int t0 = rg * 8 * RO + ro * 8;
    u4 r[8];
#pragma unroll
    for (int e = 0; e < 8; ++e) r[e] = *(const u4*)(x + (size_t)(t0 + e) * D + (valid ? co : 79) * 8);
    if (valid) {
      u4 o[8];
      tr8x8(r, o);
      u16* xt = (u16*)(ws + OFF_XT) + (size_t)co * 8 * L + t0;
#pragma unroll
      for (int c = 0; c < 8; ++c) *(u4*)(xt + (size_t)c * L) = o[c];
    }
    float* part = (float*)sm;
    f2* stat = (f2*)(sm + 4096);
    float sv[16];
#pragma unroll
    for (int e = 0; e < 8; ++e) {
      float s = 0.f, q = 0.f;
#pragma unroll
      for (int c = 0; c < 4; ++c) { float a = bflo(r[e][c]), bb = bfhi(r[e][c]); s += a + bb; q = __builtin_fmaf(a, a, __builtin_fmaf(bb, bb, q)); }
      sv[2 * e] = valid ? s : 0.f; sv[2 * e + 1] = valid ? q : 0.f;
    }
#pragma unroll
    for (int e = 0; e < 16; ++e) {
      float v = sv[e];
      v += dpp<DPP_ROR8>(v); v += dpp<DPP_ROR4>(v);
      if (RO <= 2) v += dpp<DPP_ROR2>(v);
      if (RO <= 1) v += dpp<DPP_ROR1>(v);
      sv[e] = v;
    }
    if ((tid & 15) < RO) {
      f4* p = (f4*)(part + ((tid >> 4) * RO + ro) * 16);
#pragma unroll
      for (int e = 0; e < 4; ++e) p[e] = (f4){sv[4 * e], sv[4 * e + 1], sv[4 * e + 2], sv[4 * e + 3]};
    }
    bar();
    if (tid < 8 * RO) {
      float s = 0.f, q = 0.f;
#pragma unroll
      for (int gi = 0; gi < 5 * RO; ++gi) { f2 v = *(const f2*)(part + (gi * RO + (tid >> 3)) * 16 + (tid & 7) * 2); s += v.x; q += v.y; }
      stat[tid] = ln_stats(s, q, 1.0f / D);
    }
    bar();
    if (valid) {
      u4 o[8];
#pragma unroll
      for (int e = 0; e < 8; ++e) {
        f2 st = stat[ro * 8 + e];
#pragma unroll
        for (int c = 0; c < 4; ++c) r[e][c] = pk_h((bflo(r[e][c]) - st.x) * st.y, (bfhi(r[e][c]) - st.x) * st.y);
      }
      tr8x8(r, o);
      u16* ut = (u16*)(ws + OFF_UT) + (size_t)co * 8 * L + t0;
#pragma unroll
      for (int c = 0; c < 8; ++c) *(u4*)(ut + (size_t)c * L) = o[c];
    }
    return;
  }
  b -= NB_LN;
  if (b < NB_KG) {
    const int d = b;
    const unsigned la = ((const unsigned*)logA)[d * 64 + lane], ai = ((const unsigned*)Aim)[d * 64 + lane];
    const float dt = rbf(__expf(bflo(logdt[d])));
    float a2[2], br[2];
    a2[0] = -rbf(__expf(bflo(la))) * dt * 1.4426950408889634f; a2[1] = -rbf(__expf(bfhi(la))) * dt * 1.4426950408889634f;
    br[0] = bflo(ai) * dt * 0.15915494309189535f; br[1] = bfhi(ai) * dt * 0.15915494309189535f;
    char* base = sm + lane * 8;
    auto put = [&](int row, cf v0, cf v1, float sg) { *(u2*)(base + row * KG_PITCH) = (u2){pk_h(v0.x, sg * v0.y), pk_h(v1.x, sg * v1.y)}; };
    if (w == 0) {
      const unsigned cr = ((const unsigned*)Cre)[d * 64 + lane], ci = ((const unsigned*)Cim)[d * 64 + lane];
      float mx = fmaxf(fmaxf(fabsf(bflo(cr)), fabsf(bfhi(cr))), fmaxf(fabsf(bflo(ci)), fabsf(bfhi(ci))));
#pragma unroll
      for (int o = 32; o; o >>= 1) mx = fmaxf(mx, __shfl_xor(mx, o));
      if (!(mx > 1e-30f)) mx = 1.0f;
      const float im = 1.0f / mx;
      if (lane == 0) ((float*)(ws + OFF_KSC))[d] = 2.0f * dt * mx;
      cf v0 = {bflo(cr) * im, bflo(ci) * im}, v1 = {bfhi(cr) * im, bfhi(ci) * im};
      const cf s0 = cexpp(a2[0], br[0], 8.f), s1 = cexpp(a2[1], br[1], 8.f);
#pragma unroll
      for (int j = 0; j < 16; ++j) { put(48 + j, v0, v1, 1.f); v0 = cmul(v0, s0); v1 = cmul(v1, s1); }
    } else {
      const cf h0 = cexpp(a2[0], br[0], 128.f), h1 = cexpp(a2[1], br[1], 128.f);
      if (w == 1) {
        const cf s0 = cexpp(a2[0], br[0], 1.f), s1 = cexpp(a2[1], br[1], 1.f);
        cf v0 = {1.f, 0.f}, v1 = {1.f, 0.f};
#pragma unroll
        for (int c = 0; c < 8; ++c) { put(c, v0, v1, -1.f); put(8 + c, cmul(v0, h0), cmul(v1, h1), -1.f); v0 = cmul(v0, s0); v1 = cmul(v1, s1); }
      } else if (w == 2) {
        const cf s0 = cexpp(a2[0], br[0], 2.f), s1 = cexpp(a2[1], br[1], 2.f);
        const cf q0 = cmul(s0, s0), q1 = cmul(s1, s1);
        cf v0 = cmul(h0, h0), v1 = cmul(h1, h1);
        cf y0 = cmul(v0, v0), y1 = cmul(v1, v1);
#pragma unroll
        for (int c = 0; c < 4; ++c) { put(16 + c, v0, v1, -1.f); put(20 + c, cmul(v0, h0), cmul(v1, h1), -1.f); v0 = cmul(v0, s0); v1 = cmul(v1, s1); }
#pragma unroll
        for (int i = 0; i < 4; ++i) { put(24 + 2 * i, y0, y1, -1.f); put(25 + 2 * i, cmul(y0, q0), cmul(y1, q1), -1.f); y0 = cmul(y0, h0); y1 = cmul(y1, h1); }
      } else {
        cf v0 = cexpp(a2[0], br[0], 1024.f), v1 = cexpp(a2[1], br[1], 1024.f);
#pragma unroll
        for (int i = 0; i < 9; ++i) { put(32 + i, v0, v1, -1.f); v0 = cmul(v0, h0); v1 = cmul(v1, h1); }
      }
    }
    __syncthreads();
    u16* tbl = (u16*)(sm + 64 * KG_PITCH);
    if (w > 0) {
      const int mt = w - 1, c = lane & 15, q = lane >> 4;
      const char* pa = sm + (16 * mt + c) * KG_PITCH + q * 16;
      const char* pb = sm + (48 + c) * KG_PITCH + q * 16;
      f4 acc = {0.f, 0.f, 0.f, 0.f};
#pragma unroll
      for (int ks = 0; ks < 8; ++ks) acc = mfma_h(*(const u4*)(pa + ks * 64), *(const u4*)(pb + ks * 64), acc);
#pragma unroll
      for (int rr = 0; rr < 4; ++rr) {
        const int row = 4 * q + rr;
        int idx; bool ok = true;
        if (mt == 0) idx = 128 * (row >> 3) + (row & 7) + 8 * c;
        else if (mt == 1) idx = row < 8 ? 256 + 64 * (row >> 2) + (row & 3) + 4 * c : 384 + 32 * ((row - 8) >> 1) + (row & 1) + 2 * c;
        else { idx = 512 + 16 * row + c; ok = idx <= 640; }
        if (ok) tbl[idx] = (u16)(pk_h(acc[rr], 0.f) & 0xffffu);
      }
    }
    __syncthreads();
    unsigned v[9];
#pragma unroll
    for (int e = 0; e < 9; ++e) {
      const int lag = 2047 - 8 * tid - e;
      unsigned u = __float_as_uint((float)lag);
      u = (u + 0x7fffu + ((u >> 16) & 1u)) >> 16;
      const int idx = lag < 128 ? (lag < 0 ? 0 : lag) : (int)u - 0x4300 + 128;
      v[e] = tbl[idx];
    }
    if (tid == 255) v[8] = 0;
    u4 o0, o1;
#pragma unroll
    for (int m = 0; m < 4; ++m) { o0[m] = v[2 * m] | (v[2 * m + 1] << 16); o1[m] = v[2 * m + 1] | (v[2 * m + 2] << 16); }
    u4* kr = (u4*)(ws + OFF_KR + (size_t)d * 8192);
    kr[tid] = o0; kr[256 + tid] = o1;
    return;
  }
  b -= NB_KG;
  if (b < NB_WI) {
    const int kp = b / 14, c0 = (b % 14) * 128 + (tid & 15) * 8, k0 = kp * 128 + (tid >> 4) * 8;
    const bool valid = c0 < F;
    u4 r[8], o[8];
#pragma unroll
    for (int e = 0; e < 8; ++e) r[e] = *(const u4*)(w_in + (size_t)(k0 + e) * F + (valid ? c0 : 0));
    float cs[8];
#pragma unroll
    for (int c = 0; c < 4; ++c) {
      float s0 = 0.f, s1 = 0.f;
#pragma unroll
      for (int e = 0; e < 8; ++e) { s0 += bflo(r[e][c]); s1 += bfhi(r[e][c]); }
      cs[2 * c] = s0; cs[2 * c + 1] = s1;
    }
    float* part = (float*)sm;
    *(f4*)(part + (tid >> 4) * 128 + (tid & 15) * 8) = (f4){cs[0], cs[1], cs[2], cs[3]};
    *(f4*)(part + (tid >> 4) * 128 + (tid & 15) * 8 + 4) = (f4){cs[4], cs[5], cs[6], cs[7]};
    if (valid) {
      tr8x8(r, o);
      const int half = c0 >= FH ? 0 : 1, cc = c0 >= FH ? c0 - FH : c0;
      u4* dst = (u4*)(ws + OFF_WF + ((size_t)(half * NGB + (cc >> 4)) * NKS1 + (k0 >> 5)) * 1024) + ((k0 >> 3) & 3) * 16 + (cc & 15);
#pragma unroll
      for (int c = 0; c < 8; ++c) dst[c] = o[c];
    }
    __syncthreads();
    if (tid < 128 && (b % 14) * 128 + tid < F) {
      float s = 0.f;
#pragma unroll
      for (int e = 0; e < 16; ++e) s += part[e * 128 + tid];
      ((float*)(ws + OFF_CSP))[kp * F + (b % 14) * 128 + tid] = s;
    }
    return;
  }
  b -= NB_WI;
  {
    const int item = b * 256 + tid;
    if (item < 8192) ((u4*)(ws + OFF_ST))[item] = (u4){0, 0, 0, 0};
    if (item < NKS3 * 4 * 80) {
      const int d0 = (item % 80) * 8, kq = item / 80, kap = kq >> 2, q = kq & 3;
      u4 r[8], o[8];
#pragma unroll
      for (int e = 0; e < 8; ++e) r[e] = *(const u4*)(w_out + (size_t)(32 * kap + 16 * (e >> 2) + 4 * q + (e & 3)) * D + d0);
      tr8x8(r, o);
      u4* dst = (u4*)(ws + OFF_WO + ((size_t)(d0 >> 4) * NKS3 + kap) * 1024) + q * 16 + (d0 & 15);
#pragma unroll
      for (int c = 0; c < 8; ++c) dst[c] = o[c];
    }
  }
}

constexpr int K2_K1 = 4160, K2_U = 8320, K2_RED = 12928, K2_LDS = K2_RED + 4 * 6 * 1024;
__global__ __launch_bounds__(256) void k2(const unsigned* __restrict__ dskip, const float* __restrict__ kscale, char* __restrict__ ws) {
  __shared__ __attribute__((aligned(16))) char sm[K2_LDS];
  const int tid = threadIdx.x, lane = tid & 63;
  const int w = __builtin_amdgcn_readfirstlane(tid >> 6);
  const int d = blockIdx.x;
  if (d >= D) {
    const int c = (d - D) * 256 + tid;
    if (c < F) {
      const float* p = (const float*)(ws + OFF_CSP) + c;
      ((float*)(ws + OFF_CS))[c] = (((p[0] + p[F]) + p[2 * F]) + p[3 * F]) + p[4 * F];
    }
    return;
  }
  const float ksc = kscale[d];
  const unsigned dsw = dskip[d >> 1];
  const float dsk = (d & 1) ? bfhi(dsw) : bflo(dsw);
  const unsigned sb = lds_addr(sm);
  const unsigned vo = w * 1024 + lane * 16;
  dma16(sb + w * 1024, vo, ws + OFF_KR + (size_t)d * 8192);
  dma16(sb + K2_K1 + w * 1024, vo, ws + OFF_KR + (size_t)d * 8192 + 4096);
  dma16(sb + K2_U + 480 + w * 1024, vo, ws + OFF_UT + (size_t)d * 4096);
  const int col = lane & 15, q = lane >> 4;
  u2 xv[2];
  const unsigned xo = (512 * w + 16 * col + 4 * q) * 2;
  gld8<0>(xv[0], xo, ws + OFF_XT + (size_t)d * 4096);
  gld8<512>(xv[1], xo, ws + OFF_XT + (size_t)d * 4096);
  if (tid < 30) *(u4*)(sm + K2_U + tid * 16) = (u4){0, 0, 0, 0};
  else if (tid < 34) *(u4*)(sm + ((tid & 2) ? K2_K1 : 0) + 4096 + (tid & 1) * 16) = (u4){0, 0, 0, 0};
  waitv<2>();
  bar();
  f4 acc[8];
#pragma unroll
  for (int n = 0; n < 8; ++n) acc[n] = (f4){0.f, 0.f, 0.f, 0.f};
  const char* pa = sm + ((col & 1) ? 0 : K2_K1) + 4 * (1015 - (col >> 1) + 4 * q) - 128 * w;
  const char* pb = sm + K2_U + 448 + 32 * col + 16 * q - 128 * w;
#pragma unroll
  for (int rr = 0; rr < 2; ++rr) {
    u4 A[8], B[8];
#pragma unroll
    for (int p = 0; p < 8; ++p) {
      const unsigned* ap = (const unsigned*)(pa - 64 * rr - 512 * p);
      A[p] = (u4){ap[0], ap[1], ap[2], ap[3]};
      B[p] = *(const u4*)(pb - 64 * rr + 512 * p);
    }
#pragma unroll
    for (int s = 0; s < 8; ++s)
#pragma unroll
      for (int p = 0; p <= s; ++p) acc[s] = mfma_h(A[p], B[s - p], acc[s]);
  }
  f4 own[2];
  {
    f4* red = (f4*)(sm + K2_RED + w * 6144) + lane;
#pragma unroll
    for (int v = 0; v < 4; ++v) {
      if (v == w) { own[0] = acc[2 * v]; own[1] = acc[2 * v + 1]; }
      else { const int sl = v > w ? v - 1 : v; red[sl * 128] = acc[2 * v]; red[sl * 128 + 64] = acc[2 * v + 1]; }
    }
  }
  bar();
  f4 tot[2] = {{0.f, 0.f, 0.f, 0.f}, {0.f, 0.f, 0.f, 0.f}};
#pragma unroll
  for (int v = 0; v < 4; ++v) {
    if (v == w) { tot[0] += own[0]; tot[1] += own[1]; }
    else { const f4* rp = (const f4*)(sm + K2_RED + v * 6144) + (w > v ? w - 1 : w) * 128 + lane; tot[0] += rp[0]; tot[1] += rp[64]; }
  }
  asm volatile("s_waitcnt vmcnt(0)" : "+v"(xv[0]), "+v"(xv[1])::"memory");
#pragma unroll
  for (int nn = 0; nn < 2; ++nn) {
    const int t = 512 * w + 256 * nn + 16 * col + 4 * q;
    const u2 uv = *(const u2*)(sm + K2_U + 480 + 2 * t);
    float h[4];
    h[0] = bflo(xv[nn].x) + rbf(rbf(ksc * tot[nn][0]) + rbf(hlo(uv.x) * dsk));
    h[1] = bfhi(xv[nn].x) + rbf(rbf(ksc * tot[nn][1]) + rbf(hhi(uv.x) * dsk));
    h[2] = bflo(xv[nn].y) + rbf(rbf(ksc * tot[nn][2]) + rbf(hlo(uv.y) * dsk));
    h[3] = bfhi(xv[nn].y) + rbf(rbf(ksc * tot[nn][3]) + rbf(hhi(uv.y) * dsk));
    const int pos = (t & ~31) + 16 * ((t >> 2) & 1) + 4 * ((t >> 3) & 3);
    *(u2*)(ws + OFF_HT + (size_t)d * 4096 + pos * 2) = (u2){pk_bf(h[0], h[1]), pk_bf(h[2], h[3])};
  }
}

DEVI void tile_of(int b, int& i, int& j) { const int g = b & 7, r = b >> 3; i = (g >> 1) * 8 + (r >> 2); j = (g & 1) * 4 + (r & 3); }
DEVI int grp_start(int j) { return j < 7 ? 7 * j : 48; }

constexpr int K3_STAT = 81920, K3_PART = K3_STAT + 512, K3_LDS = K3_PART + 8 * 512;
template <int KS> DEVI void k3_ldb(u4 (&b)[20], unsigned vo, const char* base) {
  if constexpr (KS < 20) { gld16<(KS & 3) * 1024>(b[KS], vo, base + (KS >> 2) * 4096); k3_ldb<KS + 1>(b, vo, base); }
}
DEVI u4 k3_afrag(const char* p) {
  const s4 lo = __builtin_amdgcn_ds_read_tr16_b64_v4i16((LDSP s4*)p);
  const s4 hi = __builtin_amdgcn_ds_read_tr16_b64_v4i16((LDSP s4*)(p + 512));
  const u2 a = __builtin_bit_cast(u2, lo), b = __builtin_bit_cast(u2, hi);
  return (u4){a.x, a.y, b.x, b.y};
}
template <int KS, int BASEW, bool SWAP> DEVI void k3_phase(u4 (&b)[20], f4 (&acc)[4], const char* const (&pa)[4]) {
  if constexpr (KS < 20) {
    u4 a[4];
#pragma unroll
    for (int mt = 0; mt < 4; ++mt) a[mt] = k3_afrag(pa[mt] + KS * 4096);
    waitv<BASEW - KS>(b[KS]);
#pragma unroll
    for (int mt = 0; mt < 4; ++mt) acc[mt] = SWAP ? mfma_bf(b[KS], a[mt], acc[mt]) : mfma_bf(a[mt], b[KS], acc[mt]);
    k3_phase<KS + 1, BASEW, SWAP>(b, acc, pa);
  }
}
template <int CTRL, int H> DEVI void bfly(float (&v)[16], bool up) {
#pragma unroll
  for (int k = 0; k < H; ++k) { const float send = up ? v[k] : v[k + H], keep = up ? v[k + H] : v[k]; v[k] = keep + dpp<CTRL>(send); }
}

__global__ __launch_bounds__(512, 2) void k3(char* __restrict__ ws) {
  __shared__ __attribute__((aligned(16))) char sm[K3_LDS];
  const int tid = threadIdx.x, lane = tid & 63;
  const int w = __builtin_amdgcn_readfirstlane(tid >> 6);
  int i, j; tile_of(blockIdx.x, i, j);
  const int c = lane & 15, g4 = lane >> 4;
  const int gbr = grp_start(j) + w;
  const bool live = w < (j < 6 ? 7 : 6);
  const int gb = gbr > NGB - 1 ? NGB - 1 : gbr;
  const unsigned sb = lds_addr(sm);
  unsigned cs1; u4 cs2;
  gld4<0>(cs1, (FH + 16 * gb + c) * 4, ws + OFF_CS);
  gld16<0>(cs2, (16 * gb + 4 * g4) * 4, ws + OFF_CS);
  {
    const int r8 = lane >> 3;
    const int sg = 2 * (((r8 >> 1) & 1) | ((w & 1) << 1));
    const unsigned vo = (unsigned)(8 * w + r8) * 4096 + (((lane & 7) ^ sg) * 16);
    const char* src = ws + OFF_HT + (size_t)i * 128;
#pragma unroll
    for (int m = 0; m < 10; ++m) dma16(sb + (w + 8 * m) * 1024, vo, src + (size_t)m * 64 * 4096);
  }
  const char* pa[4];
  {
    const int qq = c >> 2, p = c & 3, sh = (qq >> 1) | ((g4 & 1) << 1);
#pragma unroll
    for (int mt = 0; mt < 4; ++mt) pa[mt] = sm + (8 * g4 + qq) * 128 + (p >> 1) * 16 + 8 * (p & 1) + (mt ^ sh) * 32;
  }
  f2* stat = (f2*)(sm + K3_STAT);
  f2* part = (f2*)(sm + K3_PART);
  u64* st = (u64*)(ws + OFF_ST) + (size_t)i * 512;
  if (w == 7) {
    waitv<0>();
    bar();
    float s[4] = {0.f, 0.f, 0.f, 0.f}, q[4] = {0.f, 0.f, 0.f, 0.f};
    const bf2 one = __builtin_bit_cast(bf2, 0x3f803f80u);
#pragma unroll 4
    for (int ks = 0; ks < 20; ++ks)
#pragma unroll
      for (int mt = 0; mt < 4; ++mt) {
        const u4 a = k3_afrag(pa[mt] + ks * 4096);
#pragma unroll
        for (int e = 0; e < 4; ++e) {
          const unsigned ae = a[e];
          const bf2 v = __builtin_bit_cast(bf2, ae);
          s[mt] = __builtin_amdgcn_fdot2_f32_bf16(v, one, s[mt], false);
          q[mt] = __builtin_amdgcn_fdot2_f32_bf16(v, v, q[mt], false);
        }
      }
#pragma unroll
    for (int mt = 0; mt < 4; ++mt) {
      s[mt] += __shfl_xor(s[mt], 16); s[mt] += __shfl_xor(s[mt], 32);
      q[mt] += __shfl_xor(q[mt], 16); q[mt] += __shfl_xor(q[mt], 32);
    }
    const float ss = g4 == 0 ? s[0] : g4 == 1 ? s[1] : g4 == 2 ? s[2] : s[3];
    const float qq = g4 == 0 ? q[0] : g4 == 1 ? q[1] : g4 == 2 ? q[2] : q[3];
    stat[lane] = ln_stats(ss, qq, 1.0f / D);
    bar();
    bar();
    float ps = 0.f, pq = 0.f;
#pragma unroll
    for (int v = 0; v < 7; ++v) { const f2 t = part[v * 64 + lane]; ps += t.x; pq += t.y; }
    __hip_atomic_store(st + j * 64 + lane, ((u64)(__float_as_uint(pq) | 0x80000000u) << 32) | __float_as_uint(ps), __ATOMIC_RELAXED, __HIP_MEMORY_SCOPE_AGENT);
    u64 gv[8];
    for (int spin = 0; spin < (1 << 22); ++spin) {
      bool ok = true;
#pragma unroll
      for (int v = 0; v < 8; ++v) { gv[v] = __hip_atomic_load(st + v * 64 + lane, __ATOMIC_RELAXED, __HIP_MEMORY_SCOPE_AGENT); ok = ok && (gv[v] >> 63); }
      if (__all(ok)) break;
      __builtin_amdgcn_s_sleep(2);
    }
    float ts = 0.f, tq = 0.f;
#pragma unroll
    for (int v = 0; v < 8; ++v) { ts += __uint_as_float((unsigned)gv[v]); tq += __uint_as_float((unsigned)(gv[v] >> 32) & 0x7fffffffu); }
    part[lane] = ln_stats(ts, tq, 1.0f / FH);
    bar();
    return;
  }
  u4 b1[20], b2[20];
  {
    const unsigned vo = (unsigned)gb * (NKS1 * 1024) + lane * 16;
    k3_ldb<0>(b1, vo, ws + OFF_WF);
    k3_ldb<0>(b2, vo, ws + OFF_WF + (size_t)NGB * NKS1 * 1024);
  }
  waitv<40>();
  bar();
  f4 acc[4];
#pragma unroll
  for (int mt = 0; mt < 4; ++mt) acc[mt] = (f4){0.f, 0.f, 0.f, 0.f};
  k3_phase<0, 39, false>(b1, acc, pa);
  pin(cs1); pin(cs2);
  bar();
  float z[16];
  {
    float sv[16], qv[16];
    const float csf = __uint_as_float(cs1);
#pragma unroll
    for (int mt = 0; mt < 4; ++mt)
#pragma unroll
      for (int r2 = 0; r2 < 2; ++r2) {
        const f4 s2 = *(const f4*)(stat + 16 * mt + 4 * g4 + 2 * r2);
        const unsigned pz = pk_bf(gelu_t((acc[mt][2 * r2] - s2.x * csf) * s2.y), gelu_t((acc[mt][2 * r2 + 1] - s2.z * csf) * s2.w));
        z[4 * mt + 2 * r2] = bflo(pz); z[4 * mt + 2 * r2 + 1] = bfhi(pz);
      }
#pragma unroll
    for (int k = 0; k < 16; ++k) { sv[k] = live ? z[k] : 0.f; qv[k] = sv[k] * sv[k]; }
    bfly<DPP_MIRROR, 8>(sv, c & 8); bfly<DPP_HMIRROR, 4>(sv, c & 4); bfly<DPP_Q3210, 2>(sv, c & 2); bfly<DPP_Q1032, 1>(sv, c & 1);
    bfly<DPP_MIRROR, 8>(qv, c & 8); bfly<DPP_HMIRROR, 4>(qv, c & 4); bfly<DPP_Q3210, 2>(qv, c & 2); bfly<DPP_Q1032, 1>(qv, c & 1);
    part[w * 64 + 16 * (c >> 2) + 4 * g4 + (c & 3)] = (f2){sv[0], qv[0]};
  }
  bar();
#pragma unroll
  for (int mt = 0; mt < 4; ++mt) acc[mt] = (f4){0.f, 0.f, 0.f, 0.f};
  k3_phase<0, 19, true>(b2, acc, pa);
  if (live) {
#pragma unroll
    for (int mt = 0; mt < 4; ++mt) {
      const f2 s2 = stat[16 * mt + c];
      float g[4];
#pragma unroll
      for (int r = 0; r < 4; ++r) g[r] = gelu_t((acc[mt][r] - s2.x * __uint_as_float(cs2[r])) * s2.y);
      *(u2*)(ws + OFF_Z1 + ((size_t)(4 * i + mt) * NGB + gb) * 512 + lane * 8) = (u2){pk_bf(g[0], g[1]), pk_bf(g[2], g[3])};
    }
  }
  bar();
  if (live) {
#pragma unroll
    for (int a = 0; a < 2; ++a) {
      u4 o;
#pragma unroll
      for (int hh = 0; hh < 4; ++hh) {
        const int mt = 2 * a + (hh >> 1), r2 = hh & 1;
        const f4 s2 = *(const f4*)(part + 16 * mt + 4 * g4 + 2 * r2);
        o[hh] = pk_bf((z[4 * mt + 2 * r2] - s2.x) * s2.y, (z[4 * mt + 2 * r2 + 1] - s2.z) * s2.w);
      }
      *(u4*)(ws + OFF_Z2 + ((size_t)(2 * i + a) * NGB + gb) * 1024 + lane * 16) = o;
    }
  }
}

constexpr int K4_RING = 3 * 8192, K4_LDS = 4 * K4_RING;
struct K4Ctx { unsigned vw[2], vz[7], ring; const char* wsrc; const char* zsrc; const char* rd[2]; };
template <int CH> DEVI void k4_issue(const K4Ctx& x, u4 (&zf)[3][14]) {
  if constexpr (CH < 8) {
#pragma unroll
    for (int n = 0; n < 8; ++n)
      dma16(x.ring + (CH % 3) * 8192 + n * 1024, x.vw[n & 1], x.wsrc + (size_t)(32 * (n >> 2) + 4 * ((n >> 1) & 1) + 16 * (n & 1)) * 4096 + CH * 128);
#pragma unroll
    for (int kh = 0; kh < 2; ++kh)
#pragma unroll
      for (int fb = 0; fb < 7; ++fb) gld16<0>(zf[CH % 3][kh * 7 + fb], x.vz[fb], x.zsrc + (size_t)(2 * CH + kh) * NGB * 1024);
  }
}
template <int CH> DEVI void k4_loop(const K4Ctx& x, u4 (&zf)[3][14], f4 (&acc)[7][4]) {
  if constexpr (CH < 8) {
    if constexpr (CH < 7) waitv<22>(); else waitv<0>();
#pragma unroll
    for (int k = 0; k < 14; ++k) pin(zf[CH % 3][k]);
    u4 wf[2][4];
#pragma unroll
    for (int kh = 0; kh < 2; ++kh)
#pragma unroll
      for (int mt = 0; mt < 4; ++mt) wf[kh][mt] = *(const u4*)(x.rd[kh] + (CH % 3) * 8192 + mt * 2048);
    k4_issue<CH + 2>(x, zf);
    __builtin_amdgcn_sched_barrier(0);
#pragma unroll
    for (int kh = 0; kh < 2; ++kh)
#pragma unroll
      for (int fb = 0; fb < 7; ++fb)
#pragma unroll
        for (int mt = 0; mt < 4; ++mt) acc[fb][mt] = mfma_bf(zf[CH % 3][kh * 7 + fb], wf[kh][mt], acc[fb][mt]);
    __builtin_amdgcn_sched_barrier(0);
    k4_loop<CH + 1>(x, zf, acc);
  }
}
__global__ __launch_bounds__(256, 1) void k4(const u16* __restrict__ wsp, const u16* __restrict__ bsp, char* __restrict__ ws) {
  __shared__ __attribute__((aligned(16))) char sm[K4_LDS];
  const int tid = threadIdx.x, lane = tid & 63;
  const int w = __builtin_amdgcn_readfirstlane(tid >> 6);
  int i, j; tile_of(blockIdx.x, i, j);
  const int c = lane & 15, g4 = lane >> 4;
  const int gs = grp_start(j), cnt = j < 6 ? 7 : 6;
  u2 z1[7]; unsigned bias;
  const int trow = 64 * i + 32 * (w >> 1) + 8 * (c >> 2) + 4 * (w & 1) + (c & 3);
  K4Ctx x;
#pragma unroll
  for (int fb = 0; fb < 7; ++fb) {
    const int gb = gs + fb > NGB - 1 ? NGB - 1 : gs + fb;
    gld8<0>(z1[fb], (unsigned)(w * NGB + gb) * 512 + lane * 8, ws + OFF_Z1 + (size_t)4 * i * NGB * 512);
    x.vz[fb] = (unsigned)(16 * w * NGB + gb) * 1024 + lane * 16;
  }
  gld2(bias, trow * 2, bsp);
#pragma unroll
  for (int o = 0; o < 2; ++o)
    x.vw[o] = (unsigned)(8 * (lane >> 5) + ((lane >> 3) & 3)) * 4096 + w * 1024 + (((lane & 7) ^ ((4 * o + (lane >> 4)) & 7)) * 16);
  x.ring = lds_addr(sm) + w * K4_RING;
  x.wsrc = (const char*)wsp + (size_t)i * 64 * 4096;
  x.zsrc = ws + OFF_Z2;
#pragma unroll
  for (int kh = 0; kh < 2; ++kh) x.rd[kh] = sm + w * K4_RING + c * 128 + (((4 * kh + g4) ^ ((c >> 1) & 7)) * 16);
  u4 zf[3][14];
  f4 acc[7][4];
#pragma unroll
  for (int fb = 0; fb < 7; ++fb)
#pragma unroll
    for (int mt = 0; mt < 4; ++mt) acc[fb][mt] = (f4){0.f, 0.f, 0.f, 0.f};
  k4_issue<0>(x, zf);
  k4_issue<1>(x, zf);
  k4_loop<0>(x, zf, acc);
  f4 tot[7];
  {
    f4* red = (f4*)(sm + w * K4_RING) + lane;
#pragma unroll
    for (int v = 0; v < 4; ++v) {
      if (v == w) {
#pragma unroll
        for (int fb = 0; fb < 7; ++fb) tot[fb] = acc[fb][v];
      } else {
        const int sl = v > w ? v - 1 : v;
#pragma unroll
        for (int fb = 0; fb < 7; ++fb) red[(sl * 7 + fb) * 64] = acc[fb][v];
      }
    }
  }
  bar();
  {
    f4 sum[7];
#pragma unroll
    for (int fb = 0; fb < 7; ++fb) sum[fb] = (f4){0.f, 0.f, 0.f, 0.f};
#pragma unroll
    for (int v = 0; v < 4; ++v) {
      if (v == w) {
#pragma unroll
        for (int fb = 0; fb < 7; ++fb) sum[fb] += tot[fb];
      } else {
        const f4* rp = (const f4*)(sm + v * K4_RING) + (w > v ? w - 1 : w) * 7 * 64 + lane;
#pragma unroll
        for (int fb = 0; fb < 7; ++fb) sum[fb] += rp[fb * 64];
      }
    }
    pin(bias);
    const float bb = bflo(bias);
#pragma unroll
    for (int fb = 0; fb < 7; ++fb) {
      pin(z1[fb]);
      if (fb < cnt) {
        const int gb = gs + fb;
        const float g0 = rbf(rbf(sum[fb][0]) + bb), g1 = rbf(rbf(sum[fb][1]) + bb), g2 = rbf(rbf(sum[fb][2]) + bb), g3 = rbf(rbf(sum[fb][3]) + bb);
        *(u2*)(ws + OFF_V + ((size_t)(4 * i + w) * NKS3 + (gb >> 1)) * 1024 + lane * 16 + (gb & 1) * 8) =
            (u2){pk_bf(g0 * bflo(z1[fb].x), g1 * bfhi(z1[fb].x)), pk_bf(g2 * bflo(z1[fb].y), g3 * bfhi(z1[fb].y))};
      }
    }
  }
}

constexpr int K5_H = 4 * 15 * 1024, K5_LDS = K5_H + 80 * 128;
constexpr int K5_WIN = 5;
template <int S> DEVI void k5_issue(u4 (&fr)[K5_WIN][9], unsigned vv, unsigned vo, const char* vsrc, const char* osrc, bool on) {
  if constexpr (S < 7) {
    if (S < 6 || on) {
#pragma unroll
      for (int mt = 0; mt < 4; ++mt) gld16<0>(fr[S % K5_WIN][mt], vv, vsrc + (size_t)(mt * NKS3 + S) * 1024);
#pragma unroll
      for (int db = 0; db < 5; ++db) gld16<0>(fr[S % K5_WIN][4 + db], vo, osrc + (size_t)(db * NKS3 + S) * 1024);
    }
  }
}
template <int S> DEVI void k5_loop(u4 (&fr)[K5_WIN][9], f4 (&acc)[5][4], unsigned vv, unsigned vo, const char* vsrc, const char* osrc, bool on) {
  if constexpr (S < 7) {
    if (S < 6 || on) {
      constexpr int younger = (S + K5_WIN - 1 < 5 ? K5_WIN - 1 : (5 - S > 0 ? 5 - S : 0));
      waitv<9 * younger>();
#pragma unroll
      for (int k = 0; k < 9; ++k) pin(fr[S % K5_WIN][k]);
#pragma unroll
      for (int db = 0; db < 5; ++db)
#pragma unroll
        for (int mt = 0; mt < 4; ++mt) acc[db][mt] = mfma_bf(fr[S % K5_WIN][4 + db], fr[S % K5_WIN][mt], acc[db][mt]);
      __builtin_amdgcn_sched_barrier(0);
      k5_issue<S + K5_WIN>(fr, vv, vo, vsrc, osrc, on);
    }
    k5_loop<S + 1>(fr, acc, vv, vo, vsrc, osrc, on);
  }
}
template <int S> DEVI void k5_pro(u4 (&fr)[K5_WIN][9], unsigned vv, unsigned vo, const char* vsrc, const char* osrc) {
  if constexpr (S < K5_WIN) { k5_issue<S>(fr, vv, vo, vsrc, osrc, true); k5_pro<S + 1>(fr, vv, vo, vsrc, osrc); }
}
__global__ __launch_bounds__(256, 1) void k5(char* __restrict__ ws, u16* __restrict__ out) {
  __shared__ __attribute__((aligned(16))) char sm[K5_LDS];
  const int tid = threadIdx.x, lane = tid & 63;
  const int w = __builtin_amdgcn_readfirstlane(tid >> 6);
  int i, j; tile_of(blockIdx.x, i, j);
  const int c = lane & 15, g4 = lane >> 4;
  {
    const unsigned vo = (unsigned)(8 * w + (lane >> 3)) * 4096 + (lane & 7) * 16;
    const char* src = ws + OFF_HT + (size_t)j * 80 * 4096 + (size_t)i * 128;
    const unsigned dst = lds_addr(sm) + K5_H + w * 1024;
    dma16(dst, vo, src);
    dma16(dst + 4096, vo, src + 32 * 4096);
    if (w < 2) dma16(dst + 8192, vo, src + 64 * 4096);
  }
  const unsigned vv = (unsigned)(7 * w) * 1024 + lane * 16;
  const char* vsrc = ws + OFF_V + (size_t)4 * i * NKS3 * 1024;
  const char* osrc = ws + OFF_WO + (size_t)5 * j * NKS3 * 1024;
  u4 fr[K5_WIN][9];
  f4 acc[5][4];
#pragma unroll
  for (int db = 0; db < 5; ++db)
#pragma unroll
    for (int mt = 0; mt < 4; ++mt) acc[db][mt] = (f4){0.f, 0.f, 0.f, 0.f};
  k5_pro<0>(fr, vv, vv, vsrc, osrc);
  k5_loop<0>(fr, acc, vv, vv, vsrc, osrc, w < 3);
  f4 tot[5];
  {
    f4* red = (f4*)(sm + w * 15360) + lane;
#pragma unroll
    for (int v = 0; v < 4; ++v) {
      if (v == w) {
#pragma unroll
        for (int db = 0; db < 5; ++db) tot[db] = acc[db][v];
      } else {
        const int sl = v > w ? v - 1 : v;
#pragma unroll
        for (int db = 0; db < 5; ++db) red[(sl * 5 + db) * 64] = acc[db][v];
      }
    }
  }
  bar();
  f4 sum[5];
#pragma unroll
  for (int db = 0; db < 5; ++db) sum[db] = (f4){0.f, 0.f, 0.f, 0.f};
#pragma unroll
  for (int v = 0; v < 4; ++v) {
    if (v == w) {
#pragma unroll
      for (int db = 0; db < 5; ++db) sum[db] += tot[db];
    } else {
      const f4* rp = (const f4*)(sm + v * 15360) + (w > v ? w - 1 : w) * 5 * 64 + lane;
#pragma unroll
      for (int db = 0; db < 5; ++db) sum[db] += rp[db * 64];
    }
  }
  const int trow = 64 * i + 32 * (w >> 1) + 8 * (c >> 2) + 4 * (w & 1) + (c & 3);
  const char* hp = sm + K5_H + (4 * g4 + (c >> 2)) * 128 + w * 32 + (c & 3) * 8;
#pragma unroll
  for (int db = 0; db < 5; ++db) {
    const u2 hv = __builtin_bit_cast(u2, __builtin_amdgcn_ds_read_tr16_b64_v4i16((LDSP s4*)(hp + db * 2048)));
    *(u2*)(out + (size_t)trow * D + 80 * j + 16 * db + 4 * g4) =
        (u2){pk_bf(bflo(hv.x) + rbf(sum[db][0]), bfhi(hv.x) + rbf(sum[db][1])), pk_bf(bflo(hv.y) + rbf(sum[db][2]), bfhi(hv.y) + rbf(sum[db][3]))};
  }
}

extern "C" void kernel_launch(void* const* d_in, const int* in_sizes, int n_in, void* d_out, int out_size, void* d_ws, size_t ws_size,
                              hipStream_t stream) {
  char* ws = (char*)d_ws;
  k1<<<NB_LN + NB_KG + NB_WI + NB_WO, 256, 0, stream>>>((const u16*)d_in[0], (const u16*)d_in[1], (const u16*)d_in[2], (const u16*)d_in[3],
                                                         (const u16*)d_in[4], (const u16*)d_in[5], (const u16*)d_in[8], (const u16*)d_in[11], ws);
  k2<<<D + 7, 256, 0, stream>>>((const unsigned*)d_in[6], (const float*)(ws + OFF_KSC), ws);
  k3<<<256, 512, 0, stream>>>(ws);
  k4<<<256, 256, 0, stream>>>((const u16*)d_in[9], (const u16*)d_in[10], ws);
  k5<<<256, 256, 0, stream>>>(ws, (u16*)d_out);
}
```

```cpp
#include <hip/hip_runtime.h>

typedef unsigned short u16;
typedef unsigned long long u64;
typedef __attribute__((ext_vector_type(8))) __bf16 bf8;
typedef __attribute__((ext_vector_type(2))) __bf16 bf2;
typedef __attribute__((ext_vector_type(8))) _Float16 h8;
typedef __attribute__((ext_vector_type(2))) _Float16 h2;
typedef __attribute__((ext_vector_type(4))) float f4;
typedef __attribute__((ext_vector_type(2))) float f2;
typedef __attribute__((ext_vector_type(4))) short s4;
typedef __attribute__((ext_vector_type(4))) unsigned u4;
typedef __attribute__((ext_vector_type(2))) unsigned u2;
#define LDSP __attribute__((address_space(3)))
#define DEVI __device__ __forceinline__
#define ST_DECL u64* st_ = nullptr;
#define ST(i)
#define ST_END(kid)

constexpr int L = 2048, D = 640, NM = 128, F = 1728, FH = 864;
constexpr int NGB = 54;
constexpr int NKS1 = 20;
constexpr int NKS3 = 27;

constexpr size_t OFF_UT = 0;
constexpr size_t OFF_XT = OFF_UT + (size_t)D * L * 2;
constexpr size_t OFF_KR = OFF_XT + (size_t)D * L * 2;
constexpr size_t OFF_HT = OFF_KR + (size_t)D * L * 4;
constexpr size_t OFF_WF = OFF_HT + (size_t)D * L * 2;
constexpr size_t OFF_WO = OFF_WF + (size_t)2 * NGB * NKS1 * 1024;
constexpr size_t OFF_Z1 = OFF_WO + (size_t)40 * NKS3 * 1024;
constexpr size_t OFF_Z2 = OFF_Z1 + (size_t)128 * NGB * 512;
constexpr size_t OFF_V = OFF_Z2 + (size_t)64 * NGB * 1024;
constexpr size_t OFF_ST = OFF_V + (size_t)128 * NKS3 * 1024;
constexpr size_t OFF_CSP = OFF_ST + (size_t)32 * 8 * 64 * 8;
constexpr size_t OFF_CS = OFF_CSP + (size_t)5 * F * 4;
constexpr size_t OFF_KSC = OFF_CS + (size_t)F * 4;
constexpr size_t OFF_G1 = 32u << 20;
constexpr size_t OFF_F1 = OFF_G1 + 32768;
constexpr size_t OFF_EP = OFF_G1 + 65536;

DEVI float bflo(unsigned v) { return __uint_as_float(v << 16); }
DEVI float bfhi(unsigned v) { return __uint_as_float(v & 0xffff0000u); }
DEVI unsigned pk_bf(float a, float b) { f2 p = {a, b}; return __builtin_bit_cast(unsigned, __builtin_convertvector(p, bf2)); }
DEVI unsigned pk_h(float a, float b) { f2 p = {a, b}; return __builtin_bit_cast(unsigned, __builtin_convertvector(p, h2)); }
DEVI float rbf(float a) { return bflo(pk_bf(a, 0.f)); }
DEVI float hlo(unsigned v) { return (float)__builtin_bit_cast(h2, v).x; }
DEVI float hhi(unsigned v) { return (float)__builtin_bit_cast(h2, v).y; }
DEVI f4 mfma_bf(u4 a, u4 b, f4 c) { return __builtin_amdgcn_mfma_f32_16x16x32_bf16(__builtin_bit_cast(bf8, a), __builtin_bit_cast(bf8, b), c, 0, 0, 0); }
DEVI f4 mfma_h(u4 a, u4 b, f4 c) { return __builtin_amdgcn_mfma_f32_16x16x32_f16(__builtin_bit_cast(h8, a), __builtin_bit_cast(h8, b), c, 0, 0, 0); }
template <int CTRL> DEVI float dpp(float v) { return __int_as_float(__builtin_amdgcn_update_dpp(0, __float_as_int(v), CTRL, 0xf, 0xf, true)); }
constexpr int DPP_ROR1 = 0x121, DPP_ROR2 = 0x122, DPP_ROR4 = 0x124, DPP_ROR8 = 0x128, DPP_MIRROR = 0x140, DPP_HMIRROR = 0x141, DPP_Q3210 = 0x1B, DPP_Q1032 = 0xB1;

DEVI f2 ln_stats(float s, float q, float inv_n) {
  float m = s * inv_n;
  float v = fmaxf(q * inv_n - m * m, 0.f);
  float sd = rbf(__builtin_sqrtf(rbf(rbf(v) + 1e-5f)));
  f2 r = {rbf(m), 1.0f / sd};
  return r;
}
DEVI float gelu_t(float x) {
  float t = __builtin_fmaf(x * x, -0.1029432397f, -2.3022081985f);
  float e = __builtin_amdgcn_exp2f(x * t);
  return x * __builtin_amdgcn_rcpf(1.0f + e);
}

template <int IMM> DEVI void gld16(u4& d, unsigned voff, const void* sb) { asm volatile("global_load_dwordx4 %0, %1, %2 offset:%3" : "=v"(d) : "v"(voff), "s"(sb), "i"(IMM)); }
template <int IMM> DEVI void gld8(u2& d, unsigned voff, const void* sb) { asm volatile("global_load_dwordx2 %0, %1, %2 offset:%3" : "=v"(d) : "v"(voff), "s"(sb), "i"(IMM)); }
template <int IMM> DEVI void gld4(unsigned& d, unsigned voff, const void* sb) { asm volatile("global_load_dword %0, %1, %2 offset:%3" : "=v"(d) : "v"(voff), "s"(sb), "i"(IMM)); }
DEVI void gld2(unsigned& d, unsigned voff, const void* sb) { asm volatile("global_load_ushort %0, %1, %2" : "=v"(d) : "v"(voff), "s"(sb)); }
DEVI void dma16(unsigned ldsdst, unsigned voff, const void* sb) {
  asm volatile("s_mov_b32 m0, %0\n\ts_nop 0\n\tglobal_load_lds_dwordx4 %1, %2" ::"s"(ldsdst), "v"(voff), "s"(sb) : "memory");
}
template <int N> DEVI void waitv() { asm volatile("s_waitcnt vmcnt(%0)" ::"i"(N) : "memory"); }
template <int N> DEVI void waitv(u4& a) { asm volatile("s_waitcnt vmcnt(%1)" : "+v"(a) : "i"(N) : "memory"); }
DEVI void pin(u4& a) { asm volatile("" : "+v"(a)); }
DEVI void pin(u2& a) { asm volatile("" : "+v"(a)); }
DEVI void pin(unsigned& a) { asm volatile("" : "+v"(a)); }
DEVI void bar() { asm volatile("s_waitcnt lgkmcnt(0)\n\ts_barrier" ::: "memory"); }
DEVI unsigned lds_addr(const void* p) { return (unsigned)(size_t)(LDSP const char*)p; }

DEVI void tr8x8(const u4 (&r)[8], u4 (&o)[8]) {
#pragma unroll
  for (int c2 = 0; c2 < 4; ++c2)
#pragma unroll
    for (int m = 0; m < 4; ++m) {
      o[2 * c2][m] = __builtin_amdgcn_perm(r[2 * m + 1][c2], r[2 * m][c2], 0x05040100u);
      o[2 * c2 + 1][m] = __builtin_amdgcn_perm(r[2 * m + 1][c2], r[2 * m][c2], 0x07060302u);
    }
}

constexpr int KG_PITCH = 528;
constexpr int KC_PITCH = 4160;
constexpr int RC_SZ = 30208;
constexpr int KA_K = 3 * RC_SZ, KA_U = KA_K + 12 * KC_PITCH, U_PITCH = 4608, KA_T = KA_U + 3 * U_PITCH, T_PITCH = 1536;
constexpr int KA_S = KA_T + 3 * T_PITCH, KA_LDS = KA_S + 96;
constexpr int PFN = 2;
constexpr unsigned PFL = 8192;

struct c2 { f2 x, y; };
DEVI c2 cmul(c2 a, c2 b) { c2 r; r.x = a.x * b.x - a.y * b.y; r.y = a.x * b.y + a.y * b.x; return r; }
DEVI c2 cexpp(f2 a2, f2 br, float p) {
  const f2 ea = a2 * p, ph = br * p;
  const f2 e = {__builtin_amdgcn_exp2f(ea.x), __builtin_amdgcn_exp2f(ea.y)};
  const float p0 = __builtin_amdgcn_fractf(ph.x), p1 = __builtin_amdgcn_fractf(ph.y);
  c2 r;
  r.x = e * (f2){__builtin_amdgcn_cosf(p0), __builtin_amdgcn_cosf(p1)};
  r.y = e * (f2){__builtin_amdgcn_sinf(p0), __builtin_amdgcn_sinf(p1)};
  return r;
}
DEVI unsigned pk_h2(f2 p) { return __builtin_bit_cast(unsigned, __builtin_convertvector(p, h2)); }
template <int CTRL, int RM> DEVI float dppm(float v) { return __int_as_float(__builtin_amdgcn_update_dpp(0, __float_as_int(v), CTRL, RM, 0xf, false)); }
DEVI float wave_sum63(float v) {
  v += dpp<DPP_ROR8>(v); v += dpp<DPP_ROR4>(v); v += dpp<DPP_ROR2>(v); v += dpp<DPP_ROR1>(v);
  v += dppm<0x142, 0xa>(v); v += dppm<0x143, 0xc>(v);
  return v;
}
template <int IMM> DEVI void lds8(u2& d, unsigned a) { asm volatile("ds_read_b64 %0, %1 offset:%2" : "=v"(d) : "v"(a), "i"(IMM)); }
template <int IMM> DEVI void lds16(u4& d, unsigned a) { asm volatile("ds_read_b128 %0, %1 offset:%2" : "=v"(d) : "v"(a), "i"(IMM)); }
template <int N> DEVI void waitl(u2& a, u2& b, u4& c) { asm volatile("s_waitcnt lgkmcnt(%3)" : "+v"(a), "+v"(b), "+v"(c) : "i"(N) : "memory"); }
template <int IMM> DEVI void gld16s(u4& d, unsigned voff, const void* sb) { asm volatile("global_load_dwordx4 %0, %1, %2 offset:%3 sc1" : "=v"(d) : "v"(voff), "s"(sb), "i"(IMM)); }
DEVI void gld4s(unsigned& d, unsigned voff, const void* sb) { asm volatile("global_load_dword %0, %1, %2 sc1" : "=v"(d) : "v"(voff), "s"(sb)); }
DEVI unsigned perm(unsigned hi, unsigned lo, unsigned sel) { return __builtin_amdgcn_perm(hi, lo, sel); }

template <int RR, int P> DEVI void conv_ld(u2 (&al)[8], u2 (&ah)[8], u4 (&bb)[8], unsigned pa, unsigned pb) {
  if constexpr (P < 8) {
    lds8<64 * (1 - RR) + 512 * (7 - P)>(al[P], pa);
    lds8<64 * (1 - RR) + 512 * (7 - P) + 8>(ah[P], pa);
    lds16<64 * (1 - RR) + 512 * P>(bb[P], pb);
    conv_ld<RR, P + 1>(al, ah, bb, pa, pb);
  }
}
template <int S> DEVI void conv_mm(u2 (&al)[8], u2 (&ah)[8], u4 (&bb)[8], f4 (&acc)[8]) {
  if constexpr (S < 8) {
    waitl<(3 * (7 - S) > 15 ? 15 : 3 * (7 - S))>(al[S], ah[S], bb[S]);
#pragma unroll
    for (int p = 0; p <= S; ++p) acc[S] = mfma_h((u4){al[p].x, al[p].y, ah[p].x, ah[p].y}, bb[S - p], acc[S]);
    __builtin_amdgcn_sched_barrier(0);
    conv_mm<S + 1>(al, ah, bb, acc);
  }
}
template <int IMM> DEVI void gld8s(u2& d, unsigned voff, const void* sb) { asm volatile("global_load_dwordx2 %0, %1, %2 offset:%3 sc1" : "=v"(d) : "v"(voff), "s"(sb), "i"(IMM)); }
DEVI void gst16s(unsigned voff, u4 v, const void* sb) { asm volatile("global_store_dwordx4 %0, %1, %2 sc1" ::"v"(voff), "v"(v), "s"(sb) : "memory"); }
DEVI u4 tr2(const char* p, int step) {
  const u2 a = __builtin_bit_cast(u2, __builtin_amdgcn_ds_read_tr16_b64_v4i16((LDSP s4*)p));
  const u2 b = __builtin_bit_cast(u2, __builtin_amdgcn_ds_read_tr16_b64_v4i16((LDSP s4*)(p + step)));
  return (u4){a.x, a.y, b.x, b.y};
}
template <bool TWO, int K> DEVI void tbl_ld(u4 (&a)[8], u4 (&c)[8], u4 (&e)[8], unsigned pa, unsigned pb) {
  if constexpr (K < 8) {
    lds16<64 * K>(a[K], pa); lds16<64 * K>(c[K], pb);
    if constexpr (TWO) lds16<64 * K + 16 * KG_PITCH>(e[K], pb);
    tbl_ld<TWO, K + 1>(a, c, e, pa, pb);
  }
}
template <bool TWO, int K> DEVI void tbl_mm(u4 (&a)[8], u4 (&c)[8], u4 (&e)[8], f4& acc, f4& ac0) {
  if constexpr (K < 8) {
    constexpr int N = (TWO ? 3 : 2) * (7 - K);
    if constexpr (TWO) asm volatile("s_waitcnt lgkmcnt(%3)" : "+v"(a[K]), "+v"(c[K]), "+v"(e[K]) : "i"(N > 15 ? 15 : N) : "memory");
    else asm volatile("s_waitcnt lgkmcnt(%2)" : "+v"(a[K]), "+v"(c[K]) : "i"(N) : "memory");
    acc = mfma_h(a[K], c[K], acc);
    if constexpr (TWO) ac0 = mfma_h(e[K], c[K], ac0);
    __builtin_amdgcn_sched_barrier(0);
    tbl_mm<TWO, K + 1>(a, c, e, acc, ac0);
  }
}
template <bool TWO> DEVI void tbl_tiles(unsigned pa, unsigned pb, f4& acc, f4& ac0) {
  u4 a[8], c[8], e[8];
  tbl_ld<TWO, 0>(a, c, e, pa, pb);
  tbl_mm<TWO, 0>(a, c, e, acc, ac0);
}
DEVI void chsync(unsigned fl, int wq, unsigned phase) {
  asm volatile("s_waitcnt lgkmcnt(0)\n\tds_write_b32 %0, %1" ::"v"(fl + 4 * wq), "v"(phase) : "memory");
  for (;;) {
    u4 v;
    asm volatile("ds_read_b128 %0, %1\n\ts_waitcnt lgkmcnt(0)" : "=v"(v) : "v"(fl) : "memory");
    if (__builtin_amdgcn_readfirstlane(min(min(v.x, v.y), min(v.z, v.w))) >= phase) break;
    __builtin_amdgcn_s_sleep(1);
  }
}
DEVI void rawbar() { asm volatile("s_barrier" ::: "memory"); }
template <int I> DEVI void poll_ld(u4 (&g)[4], unsigned go, const char* base) {
  if constexpr (I < 4) { gld16s<16 * I>(g[I], go, base); poll_ld<I + 1>(g, go, base); }
}
DEVI void wait4(u4 (&g)[4]) { asm volatile("s_waitcnt vmcnt(0)" : "+v"(g[0]), "+v"(g[1]), "+v"(g[2]), "+v"(g[3])::"memory"); }

__global__ __launch_bounds__(1024, 4) void ka(const u16* __restrict__ x, const u16* __restrict__ logA, const u16* __restrict__ Aim,
                                          const u16* __restrict__ Cre, const u16* __restrict__ Cim, const unsigned* __restrict__ logdt,
                                          const unsigned* __restrict__ dskip, const u16* __restrict__ w_in, const u16* __restrict__ wsp,
                                          const u16* __restrict__ w_out, char* __restrict__ ws) {
  __shared__ __attribute__((aligned(16))) char sm[KA_LDS];
  const int tid = threadIdx.x, lane = tid & 63;
  const int w = __builtin_amdgcn_readfirstlane(tid >> 6);
  const int b = blockIdx.x;
  ST_DECL
  const int g = b & 7, m = b >> 3;
  const bool isconv = m < 27;
  const int d0 = 80 * g + (3 * m < 77 ? 3 * m : 77);
  if (tid < 16) ((int*)(sm + KA_S + 32))[tid] = 0;
  unsigned tag = 0;
  if (w < 4) {
    u4 r0, r1; u2 r2;
    unsigned ep;
    const int row = 256 * g + 8 * m + 2 * w + (lane >> 5), tm = 4 * g + (m >> 3), tg = m & 7;
    const unsigned ro = (unsigned)row * 1280 + (lane & 31) * 16;
    gld4s(ep, 0, ws + OFF_EP);
    gld16<0>(r0, ro, x); gld16<512>(r1, ro, x); gld8<1024>(r2, ro - (lane & 31) * 8, x);
    bar();
    {
      const char* tsrc = (const char*)x + (size_t)tm * 64 * 1280 + tg * 160;
      const unsigned dst = lds_addr(sm) + KA_U + w * 1024;
#pragma unroll
      for (int i = 0; i < 3; ++i) { const unsigned pc = tid + 256 * i < 640 ? tid + 256 * i : 639; dma16(dst + i * 4096, (pc / 10) * 1280 + (pc % 10) * 16, tsrc); }
    }
    __builtin_amdgcn_s_setprio(2);
    asm volatile("s_waitcnt vmcnt(3)" : "+v"(ep), "+v"(r0), "+v"(r1), "+v"(r2)::"memory");
    ST(1);
    tag = ep + 0x9E3779B9u;
    f2 s0 = {bflo(r2.x), bfhi(r2.x)}, s1 = {bflo(r2.y), bfhi(r2.y)}, q0 = s0 * s0, q1 = s1 * s1;
#pragma unroll
    for (int c = 0; c < 4; ++c) {
      const unsigned v = r0[c], v2 = r1[c];
      const f2 a = {bflo(v), bfhi(v)}, a2 = {bflo(v2), bfhi(v2)};
      s0 += a; q0 += a * a; s1 += a2; q1 += a2 * a2;
    }
    s0 += s1; q0 += q1;
    float ss = s0.x + s0.y, qq = q0.x + q0.y;
    ss += dpp<DPP_ROR8>(ss); qq += dpp<DPP_ROR8>(qq); ss += dpp<DPP_ROR4>(ss); qq += dpp<DPP_ROR4>(qq);
    ss += dpp<DPP_ROR2>(ss); qq += dpp<DPP_ROR2>(qq); ss += dpp<DPP_ROR1>(ss); qq += dpp<DPP_ROR1>(qq);
    ss += dppm<0x142, 0xa>(ss); qq += dppm<0x142, 0xa>(qq);
    const float mean = ss * (1.0f / D);
    const float var = fmaxf(qq * (1.0f / D) - mean * mean, 0.f);
    unsigned pay = pk_bf(mean, __builtin_amdgcn_sqrtf(rbf(rbf(var) + 1e-5f)));
    pin(pay);
    u4 gv = {(unsigned)__builtin_amdgcn_readlane(pay, 31), tag, pay, tag};
    pin(gv);
    waitv<0>();
    if (lane == 63) gst16s((row - 1) * 8, gv, ws + OFF_G1);
    chsync(lds_addr(sm) + KA_S + 80, w, 1);
    ST(2);
#pragma unroll 1
    for (int bl = w; bl < 5; bl += 4) {
      const int l = lane & 15, gm = lane >> 4;
      const char* tp = sm + KA_U + (8 * gm + (l >> 2)) * 160 + bl * 32 + (l & 3) * 8;
      const u4 A = tr2(tp, 640), B = tr2(tp + 32 * 160, 640);
      u4 s1v, s2v;
#pragma unroll
      for (int e = 0; e < 4; ++e) {
        const unsigned ae = A[e], be = B[e];
        s1v[e] = __builtin_amdgcn_update_dpp(ae, be, DPP_ROR8, 0xf, 0xc, false);
        s2v[e] = __builtin_amdgcn_update_dpp(be, ae, DPP_ROR8, 0xf, 0x3, false);
      }
      const unsigned vo = (unsigned)(80 * tg + 16 * bl + (l & 7)) * 4096 + tm * 128 + (gm + 4 * (l >> 3)) * 16;
      gst16s(vo, s1v, ws + OFF_XT);
      gst16s(vo + 8 * 4096, s2v, ws + OFF_XT);
    }
    waitv<0>();
    chsync(lds_addr(sm) + KA_S + 80, w, 2);
    if (tid == 0) __hip_atomic_store((unsigned*)(ws + OFF_F1) + (32 * tg + tm) * 32, tag, __ATOMIC_RELAXED, __HIP_MEMORY_SCOPE_AGENT);
    ST(3);
    if (isconv) {
      u4 gr[4], xr[3];
      poll_ld<0>(gr, tid * 64, ws + OFF_G1);
      if (w == 0) {
#pragma unroll 1
        for (int spin = 0; spin < (1 << 16); ++spin) {
          unsigned v;
          asm volatile("global_load_dword %0, %1, %2 sc1\n\ts_waitcnt vmcnt(0)" : "=v"(v) : "v"((lane & 31) * 128), "s"(ws + OFF_F1 + g * 4096) : "memory");
          if (__all(v == tag)) break;
          __builtin_amdgcn_s_sleep(2);
        }
      }
      wait4(gr);
      chsync(lds_addr(sm) + KA_S + 80, w, 3);
      ST(4);
      const char* xsrc = ws + OFF_XT + (size_t)d0 * 4096;
      gld16s<0>(xr[0], tid * 16, xsrc); gld16s<0>(xr[1], tid * 16, xsrc + 4096); gld16s<0>(xr[2], tid * 16, xsrc + 8192);
#pragma unroll 1
      for (int spin = 0; spin < (1 << 16); ++spin) {
        bool ok = true;
#pragma unroll
        for (int i = 0; i < 4; ++i) ok = ok && gr[i].y == tag && gr[i].w == tag;
        if (__all(ok)) break;
        poll_ld<0>(gr, tid * 64, ws + OFF_G1);
        wait4(gr);
      }
      f2 mm[4], rs[4];
#pragma unroll
      for (int i = 0; i < 4; ++i) { mm[i] = (f2){bflo(gr[i].x), bflo(gr[i].z)}; rs[i] = (f2){__builtin_amdgcn_rcpf(bfhi(gr[i].x)), __builtin_amdgcn_rcpf(bfhi(gr[i].z))}; }
      if (tid < 90) *(u4*)(sm + KA_U + (tid / 30) * U_PITCH + (tid % 30) * 16) = (u4){0, 0, 0, 0};
      ST(7);
      asm volatile("s_waitcnt vmcnt(0)" : "+v"(xr[0]), "+v"(xr[1]), "+v"(xr[2])::"memory");
      ST(8);
#pragma unroll
      for (int c = 0; c < 3; ++c) {
        u4 o;
#pragma unroll
        for (int i = 0; i < 4; ++i) { const unsigned v = xr[c][i]; o[i] = pk_h2(((f2){bflo(v), bfhi(v)} - mm[i]) * rs[i]); }
        *(u4*)(sm + KA_U + c * U_PITCH + 480 + tid * 16) = o;
      }
    }
    __builtin_amdgcn_s_setprio(0);
  }
  if (!isconv) {
    if (w >= 4) rawbar();
    rawbar();
    const int si = b - 216;
    if (si < 27) {
      const int tt = tid - 384, co = tt & 7, ko = tt >> 3, c0 = 64 * si + 8 * co, k0 = 8 * ko;
      float* part = (float*)(sm + 98304);
      if (w >= 6) {
        u4 r[8], o[8];
#pragma unroll
        for (int e = 0; e < 8; ++e) r[e] = *(const u4*)(w_in + (size_t)(k0 + e) * F + c0);
        float cs[8];
#pragma unroll
        for (int c = 0; c < 4; ++c) {
          f2 s = {0.f, 0.f};
#pragma unroll
          for (int e = 0; e < 8; ++e) { const unsigned v = r[e][c]; s += (f2){bflo(v), bfhi(v)}; }
          cs[2 * c] = s.x + dpp<DPP_ROR8>(s.x); cs[2 * c + 1] = s.y + dpp<DPP_ROR8>(s.y);
        }
        if (!(lane & 8)) {
          f4* p = (f4*)(part + ((w - 6) * 4 + (lane >> 4)) * 64 + co * 8);
          p[0] = (f4){cs[0], cs[1], cs[2], cs[3]}; p[1] = (f4){cs[4], cs[5], cs[6], cs[7]};
        }
        tr8x8(r, o);
#pragma unroll
        for (int c = 0; c < 8; ++c) *(u4*)(sm + tt * 144 + c * 16) = o[c];
      }
      __syncthreads();
      if (w < 8) {
        float s = 0.f;
#pragma unroll
        for (int e = 0; e < 5; ++e) s += part[(w * 5 + e) * 64 + lane];
        part[2560 + w * 64 + lane] = s;
      }
#pragma unroll
      for (int i = 0; i < 5; ++i) {
        const int f = w * 5 + i, blk = f / 20, ks = f % 20, gc = 4 * si + blk;
        const u4 v = *(const u4*)(sm + ((4 * ks + (lane >> 4)) * 8 + 2 * blk + ((lane >> 3) & 1)) * 144 + (lane & 7) * 16);
        *(u4*)(ws + OFF_WF + ((size_t)(gc >= NGB ? gc - NGB : gc + NGB) * NKS1 + ks) * 1024 + lane * 16) = v;
      }
      __syncthreads();
      if (w == 0) {
        float s = 0.f;
#pragma unroll
        for (int e = 0; e < 8; ++e) s += part[2560 + e * 64 + lane];
        ((float*)(ws + OFF_CS))[64 * si + lane] = s;
      }
    } else if (si < 35) {
      ((u4*)(ws + OFF_ST))[(si - 27) * 1024 + tid] = (u4){0, 0, 0, 0};
    }
    ST_END(0);
    return;
  }
  const int ch = w < 4 ? 0 : (w >> 2) - 1, role = (w + ch) & 3;
  const int d = d0 + ch;
  const int col = lane & 15, q = lane >> 4;
  char* rc = sm + ch * RC_SZ;
  float* slot = (float*)(sm + KA_S) + 2 * ch;
  const unsigned fl = lds_addr(sm) + KA_S + 32 + 16 * ch;
  if (w >= 4) {
    unsigned la, ai, cr, ci, ldw, dw;
    gld4<0>(ldw, 0, logdt + (d >> 1));
    gld4<0>(dw, 0, dskip + (d >> 1));
    gld4<0>(la, lane * 4, logA + d * NM);
    gld4<0>(ai, lane * 4, Aim + d * NM);
    gld4<0>(cr, lane * 4, Cre + d * NM);
    gld4<0>(ci, lane * 4, Cim + d * NM);
    rawbar();
    asm volatile("s_waitcnt vmcnt(0)" : "+v"(la), "+v"(ai), "+v"(cr), "+v"(ci), "+v"(ldw), "+v"(dw)::"memory");
    ST(1);
    const float dt = rbf(__expf((d & 1) ? bfhi(ldw) : bflo(ldw)));
    const unsigned ea = pk_bf(__expf(bflo(la)), __expf(bfhi(la)));
    const f2 a2 = (f2){bflo(ea), bfhi(ea)} * (-1.4426950408889634f * dt);
    f2 br = (f2){bflo(ai), bfhi(ai)} * (dt * 0.15915494309189535f);
    char* base = rc + lane * 8;
    auto put = [&](int row, c2 v) { *(u2*)(base + row * KG_PITCH) = (u2){pk_h2(v.x), pk_h2(v.y)}; };
    if (role == 0) {
      c2 v = {{bflo(cr), bfhi(cr)}, {bflo(ci), bfhi(ci)}};
      float mx = fmaxf(fmaxf(fabsf(v.x.x), fabsf(v.x.y)), fmaxf(fabsf(v.y.x), fabsf(v.y.y)));
      mx = fmaxf(mx, dpp<DPP_ROR8>(mx)); mx = fmaxf(mx, dpp<DPP_ROR4>(mx)); mx = fmaxf(mx, dpp<DPP_ROR2>(mx)); mx = fmaxf(mx, dpp<DPP_ROR1>(mx));
      mx = fmaxf(mx, dppm<0x142, 0xa>(mx)); mx = fmaxf(mx, dppm<0x143, 0xc>(mx));
      mx = __int_as_float(__builtin_amdgcn_readlane(__float_as_int(mx), 63));
      const float dsk = (d & 1) ? bfhi(dw) : bflo(dw);
      const float sc = fmaxf(fmaxf(2.0f * dt * mx, fabsf(dsk) * (1.0f / 16384.0f)), 1e-30f);
      const float isc = 1.0f / sc;
      if (lane == 0) { slot[0] = sc; slot[1] = dsk * isc; }
      v.x *= 2.0f * dt * isc; v.y *= 2.0f * dt * isc;
      const c2 s = cexpp(a2, br, 8.f);
#pragma unroll
      for (int j = 0; j < 16; ++j) { put(j, v); if (j < 15) v = cmul(v, s); }
    } else {
      br = -br;
      const c2 h = cexpp(a2, br, 128.f);
      if (role == 1) {
        const c2 s = cexpp(a2, br, 1.f);
        put(16, (c2){{1.f, 1.f}, {0.f, 0.f}}); put(24, h);
        c2 v = s;
#pragma unroll
        for (int c = 1; c < 8; ++c) { put(16 + c, v); put(24 + c, cmul(v, h)); if (c < 7) v = cmul(v, s); }
      } else if (role == 2) {
        const c2 s = cexpp(a2, br, 2.f);
        c2 v = cmul(h, h);
        const c2 q = cmul(s, s), y = cmul(v, v), z = cmul(y, h);
        put(40, y); put(41, cmul(y, q)); put(42, z); put(43, cmul(z, q));
#pragma unroll
        for (int c = 0; c < 4; ++c) { put(32 + c, v); put(36 + c, cmul(v, h)); if (c < 3) v = cmul(v, s); }
      } else {
        const c2 q = cexpp(a2, br, 4.f);
        c2 v = cexpp(a2, br, 768.f);
        put(44, v); put(45, cmul(v, q)); v = cmul(v, h);
        put(46, v); put(47, cmul(v, q));
#pragma unroll
        for (int i = 0; i < 9; ++i) { v = cmul(v, h); put(48 + i, v); }
      }
    }
  ST(2);
  chsync(fl, w & 3, 1);
  char* tb = sm + KA_T + ch * T_PITCH;
  {
    const int wq = w & 3;
    const unsigned pb = lds_addr(rc) + col * KG_PITCH + q * 16;
    f4 acc = {0.f, 0.f, 0.f, 0.f}, ac0 = {0.f, 0.f, 0.f, 0.f};
    const float dadd = slot[1];
    if (wq == 3) tbl_tiles<true>(pb + 32 * KG_PITCH, pb, acc, ac0); else tbl_tiles<false>(pb + (wq < 2 ? 48 : 40) * KG_PITCH, pb, acc, ac0);
    const unsigned p0 = pk_h(acc[0], acc[1]), p1 = pk_h(acc[2], acc[3]);
    if (wq < 2) {
      u16* t = (u16*)tb + 512 + 64 * q + col;
      t[0] = (u16)p0; t[16] = (u16)(p0 >> 16); t[32] = (u16)p1; t[48] = (u16)(p1 >> 16);
    } else if (wq == 2) {
      if (q < 2) { *(unsigned*)(tb + 2 * (384 + 64 * q + 2 * col)) = p0; *(unsigned*)(tb + 2 * (416 + 64 * q + 2 * col)) = p1; }
      else if (lane == 32) ((u16*)tb)[512] = (u16)p0;
    } else {
      if (q < 2) *(u2*)(tb + 2 * (256 + 64 * q + 4 * col)) = (u2){p0, p1};
      else if (lane == 32) ((u16*)tb)[384] = (u16)p0;
      if (lane == 0) ac0[0] += dadd;
      *(u2*)(tb + 2 * (128 * (q >> 1) + 4 * (q & 1) + 8 * col)) = (u2){pk_h(ac0[0], ac0[1]), pk_h(ac0[2], ac0[3])};
    }
  }
  asm volatile("s_waitcnt lgkmcnt(0)" ::: "memory");
  ST(3);
  {
    const int t8 = tid & 255, a = 255 - t8, wq = w & 3;
    u4 o0, o1;
    if (wq < 2) {
      const u16* t = (const u16*)tb + 384 + a;
      const unsigned lo = t[0], hi = t[1];
      const unsigned hh = perm(hi, hi, 0x01000100u), ll = perm(lo, lo, 0x01000100u), tie = (a & 1) ? hi : lo;
      o0 = (u4){hh, perm(tie, hi, 0x05040100u), ll, ll}; o1 = (u4){hh, perm(lo, tie, 0x05040100u), ll, ll};
    } else if (wq == 2) {
      const unsigned* t = (const unsigned*)(tb + 2 * (256 + 2 * a));
      const unsigned v01 = t[0], v2 = t[1];
      const unsigned t11 = perm(v01, v01, 0x03020302u), t00 = perm(v01, v01, 0x01000100u);
      o0 = (u4){perm(v2, v2, 0x01000100u), t11, perm(v01, v01, 0x01000302u), t00}; o1 = (u4){perm(v01, v2, 0x07060100u), t11, t00, t00};
    } else if (a >= 32) {
      const unsigned* t = (const unsigned*)(tb + 2 * (128 + 4 * a));
      const unsigned vm = t[-1], v01 = t[0], v23 = t[1], v4 = t[2];
      const unsigned t22 = perm(v23, v23, 0x01000100u);
      o0 = (u4){perm(v23, v4, 0x07060100u), t22, perm(v01, v23, 0x07060100u), perm(v01, v01, 0x01000100u)};
      o1 = (u4){perm(v23, v23, 0x01000302u), t22, perm(v01, v01, 0x01000302u), perm(vm, v01, a == 32 ? 0x07060100u : 0x01000100u)};
    } else {
      const u4 v = *(const u4*)(tb + 16 * a);
      const unsigned vm = a ? *(const unsigned*)(tb + 16 * a - 4) : 0u;
      o0 = (u4){perm(v.w, v.w, 0x01000302u), perm(v.z, v.z, 0x01000302u), perm(v.y, v.y, 0x01000302u), perm(v.x, v.x, 0x01000302u)};
      o1 = (u4){perm(v.z, v.w, 0x07060100u), perm(v.y, v.z, 0x07060100u), perm(v.x, v.y, 0x07060100u), perm(vm, v.x, 0x07060100u)};
    }
    char* kcb = sm + KA_K + ch * 4 * KC_PITCH;
    char* kc = kcb + 16 * t8;
    *(u4*)kc = o0; *(u4*)(kc + KC_PITCH) = o1;
    unsigned* k2 = (unsigned*)(kc + 2 * KC_PITCH - 4);
    unsigned* k3 = (unsigned*)(kc + 3 * KC_PITCH - 4);
    k2[0] = o0.x; k2[1] = o0.y; k2[2] = o0.z; k2[3] = o0.w;
    k3[0] = o1.x; k3[1] = o1.y; k3[2] = o1.z; k3[3] = o1.w;
    if (wq == 0 && lane < 36) { const int s = lane / 9; *(unsigned*)(kcb + s * KC_PITCH + 4096 - (s >> 1) * 4 + (lane - 9 * s) * 4) = 0; }
  }
  ST(4);
  }
  ST(5);
  bar();
  ST(6);
  if (b == 0 && tid == 0) __hip_atomic_store((unsigned*)(ws + OFF_EP), tag, __ATOMIC_RELAXED, __HIP_MEMORY_SCOPE_AGENT);
  if (w < 4) {
    __builtin_amdgcn_s_sleep(4);
    if (w == 0) {
      const int item = b * 40 + lane;
      if (lane < 40) {
        const int oc = item % 80, kq = item / 80, kap = kq >> 2, qq = kq & 3;
        u4 r[8], o[8];
#pragma unroll
        for (int e = 0; e < 8; ++e) r[e] = *(const u4*)(w_out + (size_t)(32 * kap + 16 * (e >> 2) + 4 * qq + (e & 3)) * D + oc * 8);
        tr8x8(r, o);
        u4* dst = (u4*)(ws + OFF_WO + ((size_t)(oc >> 1) * NKS3 + kap) * 1024) + qq * 16 + (oc & 1) * 8;
#pragma unroll
        for (int c = 0; c < 8; ++c) dst[c] = o[c];
      }
    } else {
      unsigned pw[PFN];
#pragma unroll
      for (int i = 0; i < PFN; ++i) {
        const unsigned ln = ((m * 3 + w - 1) * PFN + i) * 64 + lane;
        gld4<0>(pw[i], (ln < PFL ? ln : PFL - 1) * 128, (const char*)wsp + (size_t)(PFL == 16384 ? g >> 1 : g) * (PFL * 128));
      }
#pragma unroll
      for (int i = 0; i < PFN; ++i) asm volatile("s_waitcnt vmcnt(0)" : "+v"(pw[i])::"memory");
    }
    ST_END(0);
    return;
  }
  const int wq = w & 3;
  u2 xv[2];
  gld8s<0>(xv[0], (512 * wq + 16 * col + 4 * q) * 2, ws + OFF_XT + (size_t)d * 4096);
  gld8s<512>(xv[1], (512 * wq + 16 * col + 4 * q) * 2, ws + OFF_XT + (size_t)d * 4096);
  f4 acc[8];
#pragma unroll
  for (int n = 0; n < 8; ++n) acc[n] = (f4){0.f, 0.f, 0.f, 0.f};
  {
    const int s = (3 - col) & 3;
    const unsigned pa = lds_addr(sm) + KA_K + (4 * ch + s) * KC_PITCH + 2 * (2031 - s - col + 8 * q) - 64 * (2 * wq + 1) - 512 * 7;
    const unsigned pb = lds_addr(sm) + KA_U + ch * U_PITCH + 448 + 32 * col + 16 * q - 64 * (2 * wq + 1);
    u2 al[8], ah[8]; u4 bb[8];
    conv_ld<0, 0>(al, ah, bb, pa, pb);
    conv_mm<0>(al, ah, bb, acc);
    __builtin_amdgcn_sched_barrier(0);
    conv_ld<1, 0>(al, ah, bb, pa, pb);
    conv_mm<0>(al, ah, bb, acc);
  }
  ST(7);
  f4 tot[2];
  {
    f4* red = (f4*)(rc + wq * 6144) + lane;
#pragma unroll
    for (int v = 0; v < 4; ++v) {
      if (v == wq) { tot[0] = acc[2 * v]; tot[1] = acc[2 * v + 1]; }
      else { const int sl = v > wq ? v - 1 : v; red[sl * 128] = acc[2 * v]; red[sl * 128 + 64] = acc[2 * v + 1]; }
    }
  }
  chsync(fl, w & 3, 2);
  ST(8);
#pragma unroll
  for (int v = 0; v < 3; ++v) {
    const int src = v < wq ? v : v + 1;
    const f4* rp = (const f4*)(rc + src * 6144) + (wq > src ? wq - 1 : wq) * 128 + lane;
    tot[0] += rp[0]; tot[1] += rp[64];
  }
  const float sc = slot[0];
  asm volatile("s_waitcnt vmcnt(0)" : "+v"(xv[0]), "+v"(xv[1])::"memory");
#pragma unroll
  for (int nn = 0; nn < 2; ++nn) {
    const int t = 512 * wq + 256 * nn + 16 * col + 4 * q;
    const int pos = (t & ~31) + 16 * ((t >> 2) & 1) + 4 * ((t >> 3) & 3);
    const float h[4] = {__builtin_fmaf(tot[nn][0], sc, bflo(xv[nn].x)), __builtin_fmaf(tot[nn][1], sc, bfhi(xv[nn].x)),
                        __builtin_fmaf(tot[nn][2], sc, bflo(xv[nn].y)), __builtin_fmaf(tot[nn][3], sc, bfhi(xv[nn].y))};
    *(u2*)(ws + OFF_HT + (size_t)d * 4096 + pos * 2) = (u2){pk_bf(h[0], h[1]), pk_bf(h[2], h[3])};
  }
  ST_END(0);
}

DEVI void tile_of(int b, int& i, int& j) { const int g = b & 7, r = b >> 3; i = (g >> 1) * 8 + (r >> 2); j = (g & 1) * 4 + (r & 3); }
DEVI int grp_start(int j) { return j < 7 ? 7 * j : 48; }

constexpr int K3_STAT = 81920, K3_PART = K3_STAT + 512, K3_LDS = K3_PART + 8 * 512;
template <int KS> DEVI void k3_ldb(u4 (&b)[20], unsigned vo, const char* base) {
  if constexpr (KS < 20) { gld16<(KS & 3) * 1024>(b[KS], vo, base + (KS >> 2) * 4096); k3_ldb<KS + 1>(b, vo, base); }
}
DEVI u4 k3_afrag(const char* p) {
  const s4 lo = __builtin_amdgcn_ds_read_tr16_b64_v4i16((LDSP s4*)p);
  const s4 hi = __builtin_amdgcn_ds_read_tr16_b64_v4i16((LDSP s4*)(p + 512));
  const u2 a = __builtin_bit_cast(u2, lo), b = __builtin_bit_cast(u2, hi);
  return (u4){a.x, a.y, b.x, b.y};
}
template <int KS, int BASEW, bool SWAP> DEVI void k3_phase(u4 (&b)[20], f4 (&acc)[4], const char* const (&pa)[4]) {
  if constexpr (KS < 20) {
    u4 a[4];
#pragma unroll
    for (int mt = 0; mt < 4; ++mt) a[mt] = k3_afrag(pa[mt] + KS * 4096);
    waitv<BASEW - KS>(b[KS]);
#pragma unroll
    for (int mt = 0; mt < 4; ++mt) acc[mt] = SWAP ? mfma_bf(b[KS], a[mt], acc[mt]) : mfma_bf(a[mt], b[KS], acc[mt]);
    k3_phase<KS + 1, BASEW, SWAP>(b, acc, pa);
  }
}
template <int CTRL, int H> DEVI void bfly(float (&v)[16], bool up) {
#pragma unroll
  for (int k = 0; k < H; ++k) { const float send = up ? v[k] : v[k + H], keep = up ? v[k + H] : v[k]; v[k] = keep + dpp<CTRL>(send); }
}

__global__ __launch_bounds__(512, 2) void k3(char* __restrict__ ws) {
  __shared__ __attribute__((aligned(16))) char sm[K3_LDS];
  const int tid = threadIdx.x, lane = tid & 63;
  const int w = __builtin_amdgcn_readfirstlane(tid >> 6);
  int i, j; tile_of(blockIdx.x, i, j);
  const int c = lane & 15, g4 = lane >> 4;
  const int gbr = grp_start(j) + w;
  const bool live = w < (j < 6 ? 7 : 6);
  const int gb = gbr > NGB - 1 ? NGB - 1 : gbr;
  const unsigned sb = lds_addr(sm);
  ST_DECL
  unsigned cs1; u4 cs2;
  gld4<0>(cs1, (FH + 16 * gb + c) * 4, ws + OFF_CS);
  gld16<0>(cs2, (16 * gb + 4 * g4) * 4, ws + OFF_CS);
  {
    const int r8 = lane >> 3;
    const int sg = 2 * (((r8 >> 1) & 1) | ((w & 1) << 1));
    const unsigned vo = (unsigned)(8 * w + r8) * 4096 + (((lane & 7) ^ sg) * 16);
    const char* src = ws + OFF_HT + (size_t)i * 128;
#pragma unroll
    for (int m = 0; m < 10; ++m) dma16(sb + (w + 8 * m) * 1024, vo, src + (size_t)m * 64 * 4096);
  }
  const char* pa[4];
  {
    const int qq = c >> 2, p = c & 3, sh = (qq >> 1) | ((g4 & 1) << 1);
#pragma unroll
    for (int mt = 0; mt < 4; ++mt) pa[mt] = sm + (8 * g4 + qq) * 128 + (p >> 1) * 16 + 8 * (p & 1) + (mt ^ sh) * 32;
  }
  f2* stat = (f2*)(sm + K3_STAT);
  f2* part = (f2*)(sm + K3_PART);
  u64* st = (u64*)(ws + OFF_ST) + (size_t)i * 512;
  if (w == 7) {
    waitv<0>();
    ST(1);
    bar();
    ST(2);
    float s[4] = {0.f, 0.f, 0.f, 0.f}, q[4] = {0.f, 0.f, 0.f, 0.f};
    const bf2 one = __builtin_bit_cast(bf2, 0x3f803f80u);
#pragma unroll 4
    for (int ks = 0; ks < 20; ++ks)
#pragma unroll
      for (int mt = 0; mt < 4; ++mt) {
        const u4 a = k3_afrag(pa[mt] + ks * 4096);
#pragma unroll
        for (int e = 0; e < 4; ++e) {
          const unsigned ae = a[e];
          const bf2 v = __builtin_bit_cast(bf2, ae);
          s[mt] = __builtin_amdgcn_fdot2_f32_bf16(v, one, s[mt], false);
          q[mt] = __builtin_amdgcn_fdot2_f32_bf16(v, v, q[mt], false);
        }
      }
#pragma unroll
    for (int mt = 0; mt < 4; ++mt) {
      s[mt] += __shfl_xor(s[mt], 16); s[mt] += __shfl_xor(s[mt], 32);
      q[mt] += __shfl_xor(q[mt], 16); q[mt] += __shfl_xor(q[mt], 32);
    }
    const float ss = g4 == 0 ? s[0] : g4 == 1 ? s[1] : g4 == 2 ? s[2] : s[3];
    const float qq = g4 == 0 ? q[0] : g4 == 1 ? q[1] : g4 == 2 ? q[2] : q[3];
    stat[lane] = ln_stats(ss, qq, 1.0f / D);
    ST(3);
    bar();
    ST(4);
    bar();
    ST(5);
    float ps = 0.f, pq = 0.f;
#pragma unroll
    for (int v = 0; v < 7; ++v) { const f2 t = part[v * 64 + lane]; ps += t.x; pq += t.y; }
    __hip_atomic_store(st + j * 64 + lane, ((u64)(__float_as_uint(pq) | 0x80000000u) << 32) | __float_as_uint(ps), __ATOMIC_RELAXED, __HIP_MEMORY_SCOPE_AGENT);
    u64 gv[8];
    for (int spin = 0; spin < (1 << 22); ++spin) {
      bool ok = true;
#pragma unroll
      for (int v = 0; v < 8; ++v) { gv[v] = __hip_atomic_load(st + v * 64 + lane, __ATOMIC_RELAXED, __HIP_MEMORY_SCOPE_AGENT); ok = ok && (gv[v] >> 63); }
      if (__all(ok)) break;
      __builtin_amdgcn_s_sleep(2);
    }
    ST(6);
    float ts = 0.f, tq = 0.f;
#pragma unroll
    for (int v = 0; v < 8; ++v) { ts += __uint_as_float((unsigned)gv[v]); tq += __uint_as_float((unsigned)(gv[v] >> 32) & 0x7fffffffu); }
    part[lane] = ln_stats(ts, tq, 1.0f / FH);
    bar();
    ST_END(2);
    return;
  }
  u4 b1[20], b2[20];
  {
    const unsigned vo = (unsigned)gb * (NKS1 * 1024) + lane * 16;
    k3_ldb<0>(b1, vo, ws + OFF_WF);
    k3_ldb<0>(b2, vo, ws + OFF_WF + (size_t)NGB * NKS1 * 1024);
  }
  waitv<40>();
  ST(1);
  bar();
  ST(2);
  f4 acc[4];
#pragma unroll
  for (int mt = 0; mt < 4; ++mt) acc[mt] = (f4){0.f, 0.f, 0.f, 0.f};
  k3_phase<0, 39, false>(b1, acc, pa);
  pin(cs1); pin(cs2);
  ST(3);
  bar();
  ST(4);
  float z[16];
  {
    float sv[16], qv[16];
    const float csf = __uint_as_float(cs1);
#pragma unroll
    for (int mt = 0; mt < 4; ++mt)
#pragma unroll
      for (int r2 = 0; r2 < 2; ++r2) {
        const f4 s2 = *(const f4*)(stat + 16 * mt + 4 * g4 + 2 * r2);
        const unsigned pz = pk_bf(gelu_t((acc[mt][2 * r2] - s2.x * csf) * s2.y), gelu_t((acc[mt][2 * r2 + 1] - s2.z * csf) * s2.w));
        z[4 * mt + 2 * r2] = bflo(pz); z[4 * mt + 2 * r2 + 1] = bfhi(pz);
      }
#pragma unroll
    for (int k = 0; k < 16; ++k) { sv[k] = live ? z[k] : 0.f; qv[k] = sv[k] * sv[k]; }
    bfly<DPP_MIRROR, 8>(sv, c & 8); bfly<DPP_HMIRROR, 4>(sv, c & 4); bfly<DPP_Q3210, 2>(sv, c & 2); bfly<DPP_Q1032, 1>(sv, c & 1);
    bfly<DPP_MIRROR, 8>(qv, c & 8); bfly<DPP_HMIRROR, 4>(qv, c & 4); bfly<DPP_Q3210, 2>(qv, c & 2); bfly<DPP_Q1032, 1>(qv, c & 1);
    part[w * 64 + 16 * (c >> 2) + 4 * g4 + (c & 3)] = (f2){sv[0], qv[0]};
  }
  bar();
  ST(5);
#pragma unroll
  for (int mt = 0; mt < 4; ++mt) acc[mt] = (f4){0.f, 0.f, 0.f, 0.f};
  k3_phase<0, 19, true>(b2, acc, pa);
  ST(6);
  if (live) {
#pragma unroll
    for (int mt = 0; mt < 4; ++mt) {
      const f2 s2 = stat[16 * mt + c];
      float g[4];
#pragma unroll
      for (int r = 0; r < 4; ++r) g[r] = gelu_t((acc[mt][r] - s2.x * __uint_as_float(cs2[r])) * s2.y);
      *(u2*)(ws + OFF_Z1 + ((size_t)(4 * i + mt) * NGB + gb) * 512 + lane * 8) = (u2){pk_bf(g[0], g[1]), pk_bf(g[2], g[3])};
    }
  }
  ST(7);
  bar();
  ST(8);
  if (live) {
#pragma unroll
    for (int a = 0; a < 2; ++a) {
      u4 o;
#pragma unroll
      for (int hh = 0; hh < 4; ++hh) {
        const int mt = 2 * a + (hh >> 1), r2 = hh & 1;
        const f4 s2 = *(const f4*)(part + 16 * mt + 4 * g4 + 2 * r2);
        o[hh] = pk_bf((z[4 * mt + 2 * r2] - s2.x) * s2.y, (z[4 * mt + 2 * r2 + 1] - s2.z) * s2.w);
      }
      *(u4*)(ws + OFF_Z2 + ((size_t)(2 * i + a) * NGB + gb) * 1024 + lane * 16) = o;
    }
  }
  ST_END(2);
}

constexpr int K4_RING = 3 * 8192, K4_LDS = 4 * K4_RING;
struct K4Ctx { unsigned vw[2], vz[7], ring; const char* wsrc; const char* zsrc; const char* rd[2]; };
template <int CH> DEVI void k4_issue(const K4Ctx& x, u4 (&zf)[3][14]) {
  if constexpr (CH < 8) {
#pragma unroll
    for (int n = 0; n < 8; ++n)
      dma16(x.ring + (CH % 3) * 8192 + n * 1024, x.vw[n & 1], x.wsrc + (size_t)(32 * (n >> 2) + 4 * ((n >> 1) & 1) + 16 * (n & 1)) * 4096 + CH * 128);
#pragma unroll
    for (int kh = 0; kh < 2; ++kh)
#pragma unroll
      for (int fb = 0; fb < 7; ++fb) gld16<0>(zf[CH % 3][kh * 7 + fb], x.vz[fb], x.zsrc + (size_t)(2 * CH + kh) * NGB * 1024);
  }
}
template <int CH> DEVI void k4_loop(const K4Ctx& x, u4 (&zf)[3][14], f4 (&acc)[7][4], u64* st_) {
  if constexpr (CH < 8) {
    if constexpr (CH < 7) waitv<22>(); else waitv<0>();
    if constexpr (CH == 0) ST(2); if constexpr (CH == 1) ST(3); if constexpr (CH == 4) ST(4); if constexpr (CH == 7) ST(5);
#pragma unroll
    for (int k = 0; k < 14; ++k) pin(zf[CH % 3][k]);
    u4 wf[2][4];
#pragma unroll
    for (int kh = 0; kh < 2; ++kh)
#pragma unroll
      for (int mt = 0; mt < 4; ++mt) wf[kh][mt] = *(const u4*)(x.rd[kh] + (CH % 3) * 8192 + mt * 2048);
    k4_issue<CH + 2>(x, zf);
    __builtin_amdgcn_sched_barrier(0);
#pragma unroll
    for (int kh = 0; kh < 2; ++kh)
#pragma unroll
      for (int fb = 0; fb < 7; ++fb)
#pragma unroll
        for (int mt = 0; mt < 4; ++mt) acc[fb][mt] = mfma_bf(zf[CH % 3][kh * 7 + fb], wf[kh][mt], acc[fb][mt]);
    __builtin_amdgcn_sched_barrier(0);
    k4_loop<CH + 1>(x, zf, acc, st_);
  }
}
__global__ __launch_bounds__(256, 1) void k4(const u16* __restrict__ wsp, const u16* __restrict__ bsp, char* __restrict__ ws) {
  __shared__ __attribute__((aligned(16))) char sm[K4_LDS];
  const int tid = threadIdx.x, lane = tid & 63;
  const int w = __builtin_amdgcn_readfirstlane(tid >> 6);
  int i, j; tile_of(blockIdx.x, i, j);
  const int c = lane & 15, g4 = lane >> 4;
  const int gs = grp_start(j), cnt = j < 6 ? 7 : 6;
  ST_DECL
  u2 z1[7]; unsigned bias;
  const int trow = 64 * i + 32 * (w >> 1) + 8 * (c >> 2) + 4 * (w & 1) + (c & 3);
  K4Ctx x;
#pragma unroll
  for (int fb = 0; fb < 7; ++fb) {
    const int gb = gs + fb > NGB - 1 ? NGB - 1 : gs + fb;
    gld8<0>(z1[fb], (unsigned)(w * NGB + gb) * 512 + lane * 8, ws + OFF_Z1 + (size_t)4 * i * NGB * 512);
    x.vz[fb] = (unsigned)(16 * w * NGB + gb) * 1024 + lane * 16;
  }
  gld2(bias, trow * 2, bsp);
#pragma unroll
  for (int o = 0; o < 2; ++o)
    x.vw[o] = (unsigned)(8 * (lane >> 5) + ((lane >> 3) & 3)) * 4096 + w * 1024 + (((lane & 7) ^ ((4 * o + (lane >> 4)) & 7)) * 16);
  x.ring = lds_addr(sm) + w * K4_RING;
  x.wsrc = (const char*)wsp + (size_t)i * 64 * 4096;
  x.zsrc = ws + OFF_Z2;
#pragma unroll
  for (int kh = 0; kh < 2; ++kh) x.rd[kh] = sm + w * K4_RING + c * 128 + (((4 * kh + g4) ^ ((c >> 1) & 7)) * 16);
  u4 zf[3][14];
  f4 acc[7][4];
#pragma unroll
  for (int fb = 0; fb < 7; ++fb)
#pragma unroll
    for (int mt = 0; mt < 4; ++mt) acc[fb][mt] = (f4){0.f, 0.f, 0.f, 0.f};
  k4_issue<0>(x, zf);
  k4_issue<1>(x, zf);
  ST(1);
  k4_loop<0>(x, zf, acc, st_);
  ST(6);
  f4 tot[7];
  {
    f4* red = (f4*)(sm + w * K4_RING) + lane;
#pragma unroll
    for (int v = 0; v < 4; ++v) {
      if (v == w) {
#pragma unroll
        for (int fb = 0; fb < 7; ++fb) tot[fb] = acc[fb][v];
      } else {
        const int sl = v > w ? v - 1 : v;
#pragma unroll
        for (int fb = 0; fb < 7; ++fb) red[(sl * 7 + fb) * 64] = acc[fb][v];
      }
    }
  }
  ST(7);
  bar();
  ST(8);
  {
    f4 sum[7];
#pragma unroll
    for (int fb = 0; fb < 7; ++fb) sum[fb] = (f4){0.f, 0.f, 0.f, 0.f};
#pragma unroll
    for (int v = 0; v < 4; ++v) {
      if (v == w) {
#pragma unroll
        for (int fb = 0; fb < 7; ++fb) sum[fb] += tot[fb];
      } else {
        const f4* rp = (const f4*)(sm + v * K4_RING) + (w > v ? w - 1 : w) * 7 * 64 + lane;
#pragma unroll
        for (int fb = 0; fb < 7; ++fb) sum[fb] += rp[fb * 64];
      }
    }
    pin(bias);
    const float bb = bflo(bias);
#pragma unroll
    for (int fb = 0; fb < 7; ++fb) {
      pin(z1[fb]);
      if (fb < cnt) {
        const int gb = gs + fb;
        const float g0 = rbf(rbf(sum[fb][0]) + bb), g1 = rbf(rbf(sum[fb][1]) + bb), g2 = rbf(rbf(sum[fb][2]) + bb), g3 = rbf(rbf(sum[fb][3]) + bb);
        *(u2*)(ws + OFF_V + ((size_t)(4 * i + w) * NKS3 + (gb >> 1)) * 1024 + lane * 16 + (gb & 1) * 8) =
            (u2){pk_bf(g0 * bflo(z1[fb].x), g1 * bfhi(z1[fb].x)), pk_bf(g2 * bflo(z1[fb].y), g3 * bfhi(z1[fb].y))};
      }
    }
  }
  ST_END(3);
}

constexpr int K5_H = 4 * 15 * 1024, K5_LDS = K5_H + 80 * 128;
constexpr int K5_WIN = 5;
template <int S> DEVI void k5_issue(u4 (&fr)[K5_WIN][9], unsigned vv, unsigned vo, const char* vsrc, const char* osrc, bool on) {
  if constexpr (S < 7) {
    if (S < 6 || on) {
#pragma unroll
      for (int mt = 0; mt < 4; ++mt) gld16<0>(fr[S % K5_WIN][mt], vv, vsrc + (size_t)(mt * NKS3 + S) * 1024);
#pragma unroll
      for (int db = 0; db < 5; ++db) gld16<0>(fr[S % K5_WIN][4 + db], vo, osrc + (size_t)(db * NKS3 + S) * 1024);
    }
  }
}
template <int S> DEVI void k5_loop(u4 (&fr)[K5_WIN][9], f4 (&acc)[5][4], unsigned vv, unsigned vo, const char* vsrc, const char* osrc, bool on, u64* st_) {
  if constexpr (S < 7) {
    if (S < 6 || on) {
      constexpr int younger = (S + K5_WIN - 1 < 5 ? K5_WIN - 1 : (5 - S > 0 ? 5 - S : 0));
      waitv<9 * younger>();
      if constexpr (S == 0) ST(2); if constexpr (S == 2) ST(3); if constexpr (S == 5) ST(4);
#pragma unroll
      for (int k = 0; k < 9; ++k) pin(fr[S % K5_WIN][k]);
#pragma unroll
      for (int db = 0; db < 5; ++db)
#pragma unroll
        for (int mt = 0; mt < 4; ++mt) acc[db][mt] = mfma_bf(fr[S % K5_WIN][4 + db], fr[S % K5_WIN][mt], acc[db][mt]);
      __builtin_amdgcn_sched_barrier(0);
      k5_issue<S + K5_WIN>(fr, vv, vo, vsrc, osrc, on);
    }
    k5_loop<S + 1>(fr, acc, vv, vo, vsrc, osrc, on, st_);
  }
}
template <int S> DEVI void k5_pro(u4 (&fr)[K5_WIN][9], unsigned vv, unsigned vo, const char* vsrc, const char* osrc) {
  if constexpr (S < K5_WIN) { k5_issue<S>(fr, vv, vo, vsrc, osrc, true); k5_pro<S + 1>(fr, vv, vo, vsrc, osrc); }
}
__global__ __launch_bounds__(256, 1) void k5(char* __restrict__ ws, u16* __restrict__ out) {
  __shared__ __attribute__((aligned(16))) char sm[K5_LDS];
  const int tid = threadIdx.x, lane = tid & 63;
  const int w = __builtin_amdgcn_readfirstlane(tid >> 6);
  int i, j; tile_of(blockIdx.x, i, j);
  const int c = lane & 15, g4 = lane >> 4;
  ST_DECL
  {
    const unsigned vo = (unsigned)(8 * w + (lane >> 3)) * 4096 + (lane & 7) * 16;
    const char* src = ws + OFF_HT + (size_t)j * 80 * 4096 + (size_t)i * 128;
    const unsigned dst = lds_addr(sm) + K5_H + w * 1024;
    dma16(dst, vo, src);
    dma16(dst + 4096, vo, src + 32 * 4096);
    if (w < 2) dma16(dst + 8192, vo, src + 64 * 4096);
  }
  const unsigned vv = (unsigned)(7 * w) * 1024 + lane * 16;
  const char* vsrc = ws + OFF_V + (size_t)4 * i * NKS3 * 1024;
  const char* osrc = ws + OFF_WO + (size_t)5 * j * NKS3 * 1024;
  u4 fr[K5_WIN][9];
  f4 acc[5][4];
#pragma unroll
  for (int db = 0; db < 5; ++db)
#pragma unroll
    for (int mt = 0; mt < 4; ++mt) acc[db][mt] = (f4){0.f, 0.f, 0.f, 0.f};
  k5_pro<0>(fr, vv, vv, vsrc, osrc);
  ST(1);
  k5_loop<0>(fr, acc, vv, vv, vsrc, osrc, w < 3, st_);
  ST(5);
  f4 tot[5];
  {
    f4* red = (f4*)(sm + w * 15360) + lane;
#pragma unroll
    for (int v = 0; v < 4; ++v) {
      if (v == w) {
#pragma unroll
        for (int db = 0; db < 5; ++db) tot[db] = acc[db][v];
      } else {
        const int sl = v > w ? v - 1 : v;
#pragma unroll
        for (int db = 0; db < 5; ++db) red[(sl * 5 + db) * 64] = acc[db][v];
      }
    }
  }
  ST(6);
  bar();
  ST(7);
  f4 sum[5];
#pragma unroll
  for (int db = 0; db < 5; ++db) sum[db] = (f4){0.f, 0.f, 0.f, 0.f};
#pragma unroll
  for (int v = 0; v < 4; ++v) {
    if (v == w) {
#pragma unroll
      for (int db = 0; db < 5; ++db) sum[db] += tot[db];
    } else {
      const f4* rp = (const f4*)(sm + v * 15360) + (w > v ? w - 1 : w) * 5 * 64 + lane;
#pragma unroll
      for (int db = 0; db < 5; ++db) sum[db] += rp[db * 64];
    }
  }
  const int trow = 64 * i + 32 * (w >> 1) + 8 * (c >> 2) + 4 * (w & 1) + (c & 3);
  const char* hp = sm + K5_H + (4 * g4 + (c >> 2)) * 128 + w * 32 + (c & 3) * 8;
#pragma unroll
  for (int db = 0; db < 5; ++db) {
    const u2 hv = __builtin_bit_cast(u2, __builtin_amdgcn_ds_read_tr16_b64_v4i16((LDSP s4*)(hp + db * 2048)));
    *(u2*)(out + (size_t)trow * D + 80 * j + 16 * db + 4 * g4) =
        (u2){pk_bf(bflo(hv.x) + rbf(sum[db][0]), bfhi(hv.x) + rbf(sum[db][1])), pk_bf(bflo(hv.y) + rbf(sum[db][2]), bfhi(hv.y) + rbf(sum[db][3]))};
  }
  ST_END(4);
}

extern "C" void kernel_launch(void* const* d_in, const int* in_sizes, int n_in, void* d_out, int out_size, void* d_ws, size_t ws_size,
                              hipStream_t stream) {
  char* ws = (char*)d_ws;
  ka<<<256, 1024, 0, stream>>>((const u16*)d_in[0], (const u16*)d_in[1], (const u16*)d_in[2], (const u16*)d_in[3], (const u16*)d_in[4],
                              (const unsigned*)d_in[5], (const unsigned*)d_in[6], (const u16*)d_in[8], (const u16*)d_in[9], (const u16*)d_in[11], ws);
  k3<<<256, 512, 0, stream>>>(ws);
  k4<<<256, 256, 0, stream>>>((const u16*)d_in[9], (const u16*)d_in[10], ws);
  k5<<<256, 256, 0, stream>>>(ws, (u16*)d_out);
}
```

```cpp
#include <hip/hip_runtime.h>

typedef unsigned short u16;
typedef unsigned long long u64;
typedef __attribute__((ext_vector_type(8))) __bf16 bf8;
typedef __attribute__((ext_vector_type(2))) __bf16 bf2;
typedef __attribute__((ext_vector_type(8))) _Float16 h8;
typedef __attribute__((ext_vector_type(2))) _Float16 h2;
typedef __attribute__((ext_vector_type(4))) float f4;
typedef __attribute__((ext_vector_type(2))) float f2;
typedef __attribute__((ext_vector_type(4))) short s4;
typedef __attribute__((ext_vector_type(4))) unsigned u4;
typedef __attribute__((ext_vector_type(2))) unsigned u2;
#define LDSP __attribute__((address_space(3)))
#define DEVI __device__ __forceinline__
#define ST_DECL u64* st_ = nullptr;
#define ST(i)
#define ST_END(kid)

constexpr int L = 2048, D = 640, NM = 128, F = 1728, FH = 864;
constexpr int NGB = 54;
constexpr int NKS1 = 20;
constexpr int NKS3 = 27;

constexpr size_t OFF_UT = 0;
constexpr size_t OFF_XT = OFF_UT + (size_t)D * L * 2;
constexpr size_t OFF_KR = OFF_XT + (size_t)D * L * 2;
constexpr size_t OFF_HT = OFF_KR + (size_t)D * L * 4;
constexpr size_t OFF_WF = OFF_HT + (size_t)D * L * 2;
constexpr size_t OFF_WO = OFF_WF + (size_t)2 * NGB * NKS1 * 1024;
constexpr size_t OFF_Z1 = OFF_WO + (size_t)40 * NKS3 * 1024;
constexpr size_t OFF_Z2 = OFF_Z1 + (size_t)128 * NGB * 512;
constexpr size_t OFF_V = OFF_Z2 + (size_t)64 * NGB * 1024;
constexpr size_t OFF_ST = OFF_V + (size_t)128 * NKS3 * 1024;
constexpr size_t OFF_CSP = OFF_ST + (size_t)32 * 8 * 64 * 8;
constexpr size_t OFF_CS = OFF_CSP + (size_t)5 * F * 4;
constexpr size_t OFF_KSC = OFF_CS + (size_t)F * 4;

DEVI float bflo(unsigned v) { return __uint_as_float(v << 16); }
DEVI float bfhi(unsigned v) { return __uint_as_float(v & 0xffff0000u); }
DEVI unsigned pk_bf(float a, float b) { f2 p = {a, b}; return __builtin_bit_cast(unsigned, __builtin_convertvector(p, bf2)); }
DEVI unsigned pk_h(float a, float b) { f2 p = {a, b}; return __builtin_bit_cast(unsigned, __builtin_convertvector(p, h2)); }
DEVI float rbf(float a) { return bflo(pk_bf(a, 0.f)); }
DEVI float hlo(unsigned v) { return (float)__builtin_bit_cast(h2, v).x; }
DEVI float hhi(unsigned v) { return (float)__builtin_bit_cast(h2, v).y; }
DEVI f4 mfma_bf(u4 a, u4 b, f4 c) { return __builtin_amdgcn_mfma_f32_16x16x32_bf16(__builtin_bit_cast(bf8, a), __builtin_bit_cast(bf8, b), c, 0, 0, 0); }
DEVI f4 mfma_h(u4 a, u4 b, f4 c) { return __builtin_amdgcn_mfma_f32_16x16x32_f16(__builtin_bit_cast(h8, a), __builtin_bit_cast(h8, b), c, 0, 0, 0); }
template <int CTRL> DEVI float dpp(float v) { return __int_as_float(__builtin_amdgcn_update_dpp(0, __float_as_int(v), CTRL, 0xf, 0xf, true)); }
constexpr int DPP_ROR1 = 0x121, DPP_ROR2 = 0x122, DPP_ROR4 = 0x124, DPP_ROR8 = 0x128, DPP_MIRROR = 0x140, DPP_HMIRROR = 0x141, DPP_Q3210 = 0x1B, DPP_Q1032 = 0xB1;

DEVI f2 ln_stats(float s, float q, float inv_n) {
  float m = s * inv_n;
  float v = fmaxf(q * inv_n - m * m, 0.f);
  float sd = rbf(__builtin_sqrtf(rbf(rbf(v) + 1e-5f)));
  f2 r = {rbf(m), 1.0f / sd};
  return r;
}
DEVI float gelu_t(float x) {
  float t = __builtin_fmaf(x * x, -0.1029432397f, -2.3022081985f);
  float e = __builtin_amdgcn_exp2f(x * t);
  return x * __builtin_amdgcn_rcpf(1.0f + e);
}

template <int IMM> DEVI void gld16(u4& d, unsigned voff, const void* sb) { asm volatile("global_load_dwordx4 %0, %1, %2 offset:%3" : "=v"(d) : "v"(voff), "s"(sb), "i"(IMM)); }
template <int IMM> DEVI void gld8(u2& d, unsigned voff, const void* sb) { asm volatile("global_load_dwordx2 %0, %1, %2 offset:%3" : "=v"(d) : "v"(voff), "s"(sb), "i"(IMM)); }
template <int IMM> DEVI void gld4(unsigned& d, unsigned voff, const void* sb) { asm volatile("global_load_dword %0, %1, %2 offset:%3" : "=v"(d) : "v"(voff), "s"(sb), "i"(IMM)); }
DEVI void gld2(unsigned& d, unsigned voff, const void* sb) { asm volatile("global_load_ushort %0, %1, %2" : "=v"(d) : "v"(voff), "s"(sb)); }
DEVI void dma16(unsigned ldsdst, unsigned voff, const void* sb) {
  asm volatile("s_mov_b32 m0, %0\n\ts_nop 0\n\tglobal_load_lds_dwordx4 %1, %2" ::"s"(ldsdst), "v"(voff), "s"(sb) : "memory");
}
template <int N> DEVI void waitv() { asm volatile("s_waitcnt vmcnt(%0)" ::"i"(N) : "memory"); }
template <int N> DEVI void waitv(u4& a) { asm volatile("s_waitcnt vmcnt(%1)" : "+v"(a) : "i"(N) : "memory"); }
DEVI void pin(u4& a) { asm volatile("" : "+v"(a)); }
DEVI void pin(u2& a) { asm volatile("" : "+v"(a)); }
DEVI void pin(unsigned& a) { asm volatile("" : "+v"(a)); }
DEVI void bar() { asm volatile("s_waitcnt lgkmcnt(0)\n\ts_barrier" ::: "memory"); }
DEVI unsigned lds_addr(const void* p) { return (unsigned)(size_t)(LDSP const char*)p; }

DEVI void tr8x8(const u4 (&r)[8], u4 (&o)[8]) {
#pragma unroll
  for (int c2 = 0; c2 < 4; ++c2)
#pragma unroll
    for (int m = 0; m < 4; ++m) {
      o[2 * c2][m] = __builtin_amdgcn_perm(r[2 * m + 1][c2], r[2 * m][c2], 0x05040100u);
      o[2 * c2 + 1][m] = __builtin_amdgcn_perm(r[2 * m + 1][c2], r[2 * m][c2], 0x07060302u);
    }
}

constexpr int RO = 2;
constexpr int NB_LN = L / (8 * RO);
constexpr int NB_KG = D;
constexpr int NB_WI = 14 * 5;
constexpr int NB_WO = 34;
constexpr int KG_PITCH = 528;
constexpr int K1_LDS = 64 * KG_PITCH + 1312;

struct cf { float x, y; };
DEVI cf cmul(cf a, cf b) { cf r; r.x = a.x * b.x - a.y * b.y; r.y = a.x * b.y + a.y * b.x; return r; }
DEVI cf cexpp(float a2, float br, float p) {
  float e = __builtin_amdgcn_exp2f(a2 * p);
  float ph = __builtin_amdgcn_fractf(br * p);
  cf r; r.x = e * __builtin_amdgcn_cosf(ph); r.y = e * __builtin_amdgcn_sinf(ph); return r;
}

__global__ __launch_bounds__(256, 4) void k1(const u16* __restrict__ x, const u16* __restrict__ logA, const u16* __restrict__ Aim,
                                          const u16* __restrict__ Cre, const u16* __restrict__ Cim, const u16* __restrict__ logdt,
                                          const u16* __restrict__ w_in, const u16* __restrict__ w_out, char* __restrict__ ws) {
  __shared__ __attribute__((aligned(16))) char sm[K1_LDS];
  const int tid = threadIdx.x, lane = tid & 63;
  const int w = __builtin_amdgcn_readfirstlane(tid >> 6);
  int b = blockIdx.x;
  ST_DECL
  if (b < NB_LN) {
    const int rg = (b & 7) * (NB_LN / 8) + (b >> 3);
    const int ro = tid % RO, co = tid / RO;
    const bool valid = co < 80;
    const int t0 = rg * 8 * RO + ro * 8;
    u4 r[8];
#pragma unroll
    for (int e = 0; e < 8; ++e) r[e] = *(const u4*)(x + (size_t)(t0 + e) * D + (valid ? co : 79) * 8);
    pin(r[7]); ST(1);
    if (valid) {
      u4 o[8];
      tr8x8(r, o);
      u16* xt = (u16*)(ws + OFF_XT) + (size_t)co * 8 * L + t0;
#pragma unroll
      for (int c = 0; c < 8; ++c) *(u4*)(xt + (size_t)c * L) = o[c];
    }
    float* part = (float*)sm;
    f2* stat = (f2*)(sm + 4096);
    float sv[16];
#pragma unroll
    for (int e = 0; e < 8; ++e) {
      float s = 0.f, q = 0.f;
#pragma unroll
      for (int c = 0; c < 4; ++c) { float a = bflo(r[e][c]), bb = bfhi(r[e][c]); s += a + bb; q = __builtin_fmaf(a, a, __builtin_fmaf(bb, bb, q)); }
      sv[2 * e] = valid ? s : 0.f; sv[2 * e + 1] = valid ? q : 0.f;
    }
#pragma unroll
    for (int e = 0; e < 16; ++e) {
      float v = sv[e];
      v += dpp<DPP_ROR8>(v); v += dpp<DPP_ROR4>(v);
      if (RO <= 2) v += dpp<DPP_ROR2>(v);
      if (RO <= 1) v += dpp<DPP_ROR1>(v);
      sv[e] = v;
    }
    if ((tid & 15) < RO) {
      f4* p = (f4*)(part + ((tid >> 4) * RO + ro) * 16);
#pragma unroll
      for (int e = 0; e < 4; ++e) p[e] = (f4){sv[4 * e], sv[4 * e + 1], sv[4 * e + 2], sv[4 * e + 3]};
    }
    bar();
    if (tid < 8 * RO) {
      float s = 0.f, q = 0.f;
#pragma unroll
      for (int gi = 0; gi < 5 * RO; ++gi) { f2 v = *(const f2*)(part + (gi * RO + (tid >> 3)) * 16 + (tid & 7) * 2); s += v.x; q += v.y; }
      stat[tid] = ln_stats(s, q, 1.0f / D);
    }
    bar();
    ST(2);
    if (valid) {
      u4 o[8];
#pragma unroll
      for (int e = 0; e < 8; ++e) {
        f2 st = stat[ro * 8 + e];
#pragma unroll
        for (int c = 0; c < 4; ++c) r[e][c] = pk_h((bflo(r[e][c]) - st.x) * st.y, (bfhi(r[e][c]) - st.x) * st.y);
      }
      tr8x8(r, o);
      u16* ut = (u16*)(ws + OFF_UT) + (size_t)co * 8 * L + t0;
#pragma unroll
      for (int c = 0; c < 8; ++c) *(u4*)(ut + (size_t)c * L) = o[c];
    }
    ST_END(0);
    return;
  }
  b -= NB_LN;
  if (b < NB_KG) {
    const int d = b;
    const unsigned la = ((const unsigned*)logA)[d * 64 + lane], ai = ((const unsigned*)Aim)[d * 64 + lane];
    const float dt = rbf(__expf(bflo(logdt[d])));
    float a2[2], br[2];
    a2[0] = -rbf(__expf(bflo(la))) * dt * 1.4426950408889634f; a2[1] = -rbf(__expf(bfhi(la))) * dt * 1.4426950408889634f;
    br[0] = bflo(ai) * dt * 0.15915494309189535f; br[1] = bfhi(ai) * dt * 0.15915494309189535f;
    ST(1);
    char* base = sm + lane * 8;
    auto put = [&](int row, cf v0, cf v1, float sg) { *(u2*)(base + row * KG_PITCH) = (u2){pk_h(v0.x, sg * v0.y), pk_h(v1.x, sg * v1.y)}; };
    if (w == 0) {
      const unsigned cr = ((const unsigned*)Cre)[d * 64 + lane], ci = ((const unsigned*)Cim)[d * 64 + lane];
      float mx = fmaxf(fmaxf(fabsf(bflo(cr)), fabsf(bfhi(cr))), fmaxf(fabsf(bflo(ci)), fabsf(bfhi(ci))));
#pragma unroll
      for (int o = 32; o; o >>= 1) mx = fmaxf(mx, __shfl_xor(mx, o));
      if (!(mx > 1e-30f)) mx = 1.0f;
      const float im = 1.0f / mx;
      if (lane == 0) ((float*)(ws + OFF_KSC))[d] = 2.0f * dt * mx;
      cf v0 = {bflo(cr) * im, bflo(ci) * im}, v1 = {bfhi(cr) * im, bfhi(ci) * im};
      const cf s0 = cexpp(a2[0], br[0], 8.f), s1 = cexpp(a2[1], br[1], 8.f);
#pragma unroll
      for (int j = 0; j < 16; ++j) { put(48 + j, v0, v1, 1.f); v0 = cmul(v0, s0); v1 = cmul(v1, s1); }
    } else {
      const cf h0 = cexpp(a2[0], br[0], 128.f), h1 = cexpp(a2[1], br[1], 128.f);
      if (w == 1) {
        const cf s0 = cexpp(a2[0], br[0], 1.f), s1 = cexpp(a2[1], br[1], 1.f);
        cf v0 = {1.f, 0.f}, v1 = {1.f, 0.f};
#pragma unroll
        for (int c = 0; c < 8; ++c) { put(c, v0, v1, -1.f); put(8 + c, cmul(v0, h0), cmul(v1, h1), -1.f); v0 = cmul(v0, s0); v1 = cmul(v1, s1); }
      } else if (w == 2) {
        const cf s0 = cexpp(a2[0], br[0], 2.f), s1 = cexpp(a2[1], br[1], 2.f);
        const cf q0 = cmul(s0, s0), q1 = cmul(s1, s1);
        cf v0 = cmul(h0, h0), v1 = cmul(h1, h1);
        cf y0 = cmul(v0, v0), y1 = cmul(v1, v1);
#pragma unroll
        for (int c = 0; c < 4; ++c) { put(16 + c, v0, v1, -1.f); put(20 + c, cmul(v0, h0), cmul(v1, h1), -1.f); v0 = cmul(v0, s0); v1 = cmul(v1, s1); }
#pragma unroll
        for (int i = 0; i < 4; ++i) { put(24 + 2 * i, y0, y1, -1.f); put(25 + 2 * i, cmul(y0, q0), cmul(y1, q1), -1.f); y0 = cmul(y0, h0); y1 = cmul(y1, h1); }
      } else {
        cf v0 = cexpp(a2[0], br[0], 1024.f), v1 = cexpp(a2[1], br[1], 1024.f);
#pragma unroll
        for (int i = 0; i < 9; ++i) { put(32 + i, v0, v1, -1.f); v0 = cmul(v0, h0); v1 = cmul(v1, h1); }
      }
    }
    ST(2);
    __syncthreads();
    ST(3);
    u16* tbl = (u16*)(sm + 64 * KG_PITCH);
    if (w > 0) {
      const int mt = w - 1, c = lane & 15, q = lane >> 4;
      const char* pa = sm + (16 * mt + c) * KG_PITCH + q * 16;
      const char* pb = sm + (48 + c) * KG_PITCH + q * 16;
      f4 acc = {0.f, 0.f, 0.f, 0.f};
#pragma unroll
      for (int ks = 0; ks < 8; ++ks) acc = mfma_h(*(const u4*)(pa + ks * 64), *(const u4*)(pb + ks * 64), acc);
#pragma unroll
      for (int rr = 0; rr < 4; ++rr) {
        const int row = 4 * q + rr;
        int idx; bool ok = true;
        if (mt == 0) idx = 128 * (row >> 3) + (row & 7) + 8 * c;
        else if (mt == 1) idx = row < 8 ? 256 + 64 * (row >> 2) + (row & 3) + 4 * c : 384 + 32 * ((row - 8) >> 1) + (row & 1) + 2 * c;
        else { idx = 512 + 16 * row + c; ok = idx <= 640; }
        if (ok) tbl[idx] = (u16)(pk_h(acc[rr], 0.f) & 0xffffu);
      }
    }
    __syncthreads();
    ST(4);
    unsigned v[9];
#pragma unroll
    for (int e = 0; e < 9; ++e) {
      const int lag = 2047 - 8 * tid - e;
      unsigned u = __float_as_uint((float)lag);
      u = (u + 0x7fffu + ((u >> 16) & 1u)) >> 16;
      const int idx = lag < 128 ? (lag < 0 ? 0 : lag) : (int)u - 0x4300 + 128;
      v[e] = tbl[idx];
    }
    if (tid == 255) v[8] = 0;
    u4 o0, o1;
#pragma unroll
    for (int m = 0; m < 4; ++m) { o0[m] = v[2 * m] | (v[2 * m + 1] << 16); o1[m] = v[2 * m + 1] | (v[2 * m + 2] << 16); }
    u4* kr = (u4*)(ws + OFF_KR + (size_t)d * 8192);
    kr[tid] = o0; kr[256 + tid] = o1;
    ST_END(0);
    return;
  }
  b -= NB_KG;
  if (b < NB_WI) {
    const int kp = b / 14, c0 = (b % 14) * 128 + (tid & 15) * 8, k0 = kp * 128 + (tid >> 4) * 8;
    const bool valid = c0 < F;
    u4 r[8], o[8];
#pragma unroll
    for (int e = 0; e < 8; ++e) r[e] = *(const u4*)(w_in + (size_t)(k0 + e) * F + (valid ? c0 : 0));
    float cs[8];
#pragma unroll
    for (int c = 0; c < 4; ++c) {
      float s0 = 0.f, s1 = 0.f;
#pragma unroll
      for (int e = 0; e < 8; ++e) { s0 += bflo(r[e][c]); s1 += bfhi(r[e][c]); }
      cs[2 * c] = s0; cs[2 * c + 1] = s1;
    }
    float* part = (float*)sm;
    *(f4*)(part + (tid >> 4) * 128 + (tid & 15) * 8) = (f4){cs[0], cs[1], cs[2], cs[3]};
    *(f4*)(part + (tid >> 4) * 128 + (tid & 15) * 8 + 4) = (f4){cs[4], cs[5], cs[6], cs[7]};
    if (valid) {
      tr8x8(r, o);
      const int half = c0 >= FH ? 0 : 1, cc = c0 >= FH ? c0 - FH : c0;
      u4* dst = (u4*)(ws + OFF_WF + ((size_t)(half * NGB + (cc >> 4)) * NKS1 + (k0 >> 5)) * 1024) + ((k0 >> 3) & 3) * 16 + (cc & 15);
#pragma unroll
      for (int c = 0; c < 8; ++c) dst[c] = o[c];
    }
    __syncthreads();
    if (tid < 128 && (b % 14) * 128 + tid < F) {
      float s = 0.f;
#pragma unroll
      for (int e = 0; e < 16; ++e) s += part[e * 128 + tid];
      ((float*)(ws + OFF_CSP))[kp * F + (b % 14) * 128 + tid] = s;
    }
    ST_END(0);
    return;
  }
  b -= NB_WI;
  {
    const int item = b * 256 + tid;
    if (item < 8192) ((u4*)(ws + OFF_ST))[item] = (u4){0, 0, 0, 0};
    if (item < NKS3 * 4 * 80) {
      const int d0 = (item % 80) * 8, kq = item / 80, kap = kq >> 2, q = kq & 3;
      u4 r[8], o[8];
#pragma unroll
      for (int e = 0; e < 8; ++e) r[e] = *(const u4*)(w_out + (size_t)(32 * kap + 16 * (e >> 2) + 4 * q + (e & 3)) * D + d0);
      tr8x8(r, o);
      u4* dst = (u4*)(ws + OFF_WO + ((size_t)(d0 >> 4) * NKS3 + kap) * 1024) + q * 16 + (d0 & 15);
#pragma unroll
      for (int c = 0; c < 8; ++c) dst[c] = o[c];
    }
    ST_END(0);
  }
}

constexpr int K2_K1 = 4160, K2_U = 8320, K2_RED = 12928, K2_LDS = K2_RED + 4 * 6 * 1024;
__global__ __launch_bounds__(256) void k2(const unsigned* __restrict__ dskip, const float* __restrict__ kscale, char* __restrict__ ws) {
  __shared__ __attribute__((aligned(16))) char sm[K2_LDS];
  const int tid = threadIdx.x, lane = tid & 63;
  const int w = __builtin_amdgcn_readfirstlane(tid >> 6);
  const int d = blockIdx.x;
  if (d >= D) {
    const int c = (d - D) * 256 + tid;
    if (c < F) {
      const float* p = (const float*)(ws + OFF_CSP) + c;
      ((float*)(ws + OFF_CS))[c] = (((p[0] + p[F]) + p[2 * F]) + p[3 * F]) + p[4 * F];
    }
    return;
  }
  ST_DECL
  const float ksc = kscale[d];
  const unsigned dsw = dskip[d >> 1];
  const float dsk = (d & 1) ? bfhi(dsw) : bflo(dsw);
  const unsigned sb = lds_addr(sm);
  const unsigned vo = w * 1024 + lane * 16;
  dma16(sb + w * 1024, vo, ws + OFF_KR + (size_t)d * 8192);
  dma16(sb + K2_K1 + w * 1024, vo, ws + OFF_KR + (size_t)d * 8192 + 4096);
  dma16(sb + K2_U + 480 + w * 1024, vo, ws + OFF_UT + (size_t)d * 4096);
  const int col = lane & 15, q = lane >> 4;
  u2 xv[2];
  const unsigned xo = (512 * w + 16 * col + 4 * q) * 2;
  gld8<0>(xv[0], xo, ws + OFF_XT + (size_t)d * 4096);
  gld8<512>(xv[1], xo, ws + OFF_XT + (size_t)d * 4096);
  if (tid < 30) *(u4*)(sm + K2_U + tid * 16) = (u4){0, 0, 0, 0};
  else if (tid < 34) *(u4*)(sm + ((tid & 2) ? K2_K1 : 0) + 4096 + (tid & 1) * 16) = (u4){0, 0, 0, 0};
  waitv<2>();
  ST(1);
  bar();
  ST(2);
  f4 acc[8];
#pragma unroll
  for (int n = 0; n < 8; ++n) acc[n] = (f4){0.f, 0.f, 0.f, 0.f};
  const char* pa = sm + ((col & 1) ? 0 : K2_K1) + 4 * (1015 - (col >> 1) + 4 * q) - 128 * w;
  const char* pb = sm + K2_U + 448 + 32 * col + 16 * q - 128 * w;
#pragma unroll
  for (int rr = 0; rr < 2; ++rr) {
    u4 A[8], B[8];
#pragma unroll
    for (int p = 0; p < 8; ++p) {
      const unsigned* ap = (const unsigned*)(pa - 64 * rr - 512 * p);
      A[p] = (u4){ap[0], ap[1], ap[2], ap[3]};
      B[p] = *(const u4*)(pb - 64 * rr + 512 * p);
    }
#pragma unroll
    for (int s = 0; s < 8; ++s)
#pragma unroll
      for (int p = 0; p <= s; ++p) acc[s] = mfma_h(A[p], B[s - p], acc[s]);
  }
  ST(3);
  f4 own[2];
  {
    f4* red = (f4*)(sm + K2_RED + w * 6144) + lane;
#pragma unroll
    for (int v = 0; v < 4; ++v) {
      if (v == w) { own[0] = acc[2 * v]; own[1] = acc[2 * v + 1]; }
      else { const int sl = v > w ? v - 1 : v; red[sl * 128] = acc[2 * v]; red[sl * 128 + 64] = acc[2 * v + 1]; }
    }
  }
  bar();
  ST(4);
  f4 tot[2] = {{0.f, 0.f, 0.f, 0.f}, {0.f, 0.f, 0.f, 0.f}};
#pragma unroll
  for (int v = 0; v < 4; ++v) {
    if (v == w) { tot[0] += own[0]; tot[1] += own[1]; }
    else { const f4* rp = (const f4*)(sm + K2_RED + v * 6144) + (w > v ? w - 1 : w) * 128 + lane; tot[0] += rp[0]; tot[1] += rp[64]; }
  }
  asm volatile("s_waitcnt vmcnt(0)" : "+v"(xv[0]), "+v"(xv[1])::"memory");
#pragma unroll
  for (int nn = 0; nn < 2; ++nn) {
    const int t = 512 * w + 256 * nn + 16 * col + 4 * q;
    const u2 uv = *(const u2*)(sm + K2_U + 480 + 2 * t);
    float h[4];
    h[0] = bflo(xv[nn].x) + rbf(rbf(ksc * tot[nn][0]) + rbf(hlo(uv.x) * dsk));
    h[1] = bfhi(xv[nn].x) + rbf(rbf(ksc * tot[nn][1]) + rbf(hhi(uv.x) * dsk));
    h[2] = bflo(xv[nn].y) + rbf(rbf(ksc * tot[nn][2]) + rbf(hlo(uv.y) * dsk));
    h[3] = bfhi(xv[nn].y) + rbf(rbf(ksc * tot[nn][3]) + rbf(hhi(uv.y) * dsk));
    const int pos = (t & ~31) + 16 * ((t >> 2) & 1) + 4 * ((t >> 3) & 3);
    *(u2*)(ws + OFF_HT + (size_t)d * 4096 + pos * 2) = (u2){pk_bf(h[0], h[1]), pk_bf(h[2], h[3])};
  }
  ST_END(1);
}

DEVI void tile_of(int b, int& i, int& j) { const int g = b & 7, r = b >> 3; i = (g >> 1) * 8 + (r >> 2); j = (g & 1) * 4 + (r & 3); }
DEVI int grp_start(int j) { return j < 7 ? 7 * j : 48; }

constexpr int K3_STAT = 81920, K3_PART = K3_STAT + 512, K3_LDS = K3_PART + 8 * 512;
template <int KS> DEVI void k3_ldb(u4 (&b)[20], unsigned vo, const char* base) {
  if constexpr (KS < 20) { gld16<(KS & 3) * 1024>(b[KS], vo, base + (KS >> 2) * 4096); k3_ldb<KS + 1>(b, vo, base); }
}
DEVI u4 k3_afrag(const char* p) {
  const s4 lo = __builtin_amdgcn_ds_read_tr16_b64_v4i16((LDSP s4*)p);
  const s4 hi = __builtin_amdgcn_ds_read_tr16_b64_v4i16((LDSP s4*)(p + 512));
  const u2 a = __builtin_bit_cast(u2, lo), b = __builtin_bit_cast(u2, hi);
  return (u4){a.x, a.y, b.x, b.y};
}
template <int KS, int BASEW, bool SWAP> DEVI void k3_phase(u4 (&b)[20], f4 (&acc)[4], const char* const (&pa)[4]) {
  if constexpr (KS < 20) {
    u4 a[4];
#pragma unroll
    for (int mt = 0; mt < 4; ++mt) a[mt] = k3_afrag(pa[mt] + KS * 4096);
    waitv<BASEW - KS>(b[KS]);
#pragma unroll
    for (int mt = 0; mt < 4; ++mt) acc[mt] = SWAP ? mfma_bf(b[KS], a[mt], acc[mt]) : mfma_bf(a[mt], b[KS], acc[mt]);
    k3_phase<KS + 1, BASEW, SWAP>(b, acc, pa);
  }
}
template <int CTRL, int H> DEVI void bfly(float (&v)[16], bool up) {
#pragma unroll
  for (int k = 0; k < H; ++k) { const float send = up ? v[k] : v[k + H], keep = up ? v[k + H] : v[k]; v[k] = keep + dpp<CTRL>(send); }
}

__global__ __launch_bounds__(512, 2) void k3(char* __restrict__ ws) {
  __shared__ __attribute__((aligned(16))) char sm[K3_LDS];
  const int tid = threadIdx.x, lane = tid & 63;
  const int w = __builtin_amdgcn_readfirstlane(tid >> 6);
  int i, j; tile_of(blockIdx.x, i, j);
  const int c = lane & 15, g4 = lane >> 4;
  const int gbr = grp_start(j) + w;
  const bool live = w < (j < 6 ? 7 : 6);
  const int gb = gbr > NGB - 1 ? NGB - 1 : gbr;
  const unsigned sb = lds_addr(sm);
  ST_DECL
  unsigned cs1; u4 cs2;
  gld4<0>(cs1, (FH + 16 * gb + c) * 4, ws + OFF_CS);
  gld16<0>(cs2, (16 * gb + 4 * g4) * 4, ws + OFF_CS);
  {
    const int r8 = lane >> 3;
    const int sg = 2 * (((r8 >> 1) & 1) | ((w & 1) << 1));
    const unsigned vo = (unsigned)(8 * w + r8) * 4096 + (((lane & 7) ^ sg) * 16);
    const char* src = ws + OFF_HT + (size_t)i * 128;
#pragma unroll
    for (int m = 0; m < 10; ++m) dma16(sb + (w + 8 * m) * 1024, vo, src + (size_t)m * 64 * 4096);
  }
  const char* pa[4];
  {
    const int qq = c >> 2, p = c & 3, sh = (qq >> 1) | ((g4 & 1) << 1);
#pragma unroll
    for (int mt = 0; mt < 4; ++mt) pa[mt] = sm + (8 * g4 + qq) * 128 + (p >> 1) * 16 + 8 * (p & 1) + (mt ^ sh) * 32;
  }
  f2* stat = (f2*)(sm + K3_STAT);
  f2* part = (f2*)(sm + K3_PART);
  u64* st = (u64*)(ws + OFF_ST) + (size_t)i * 512;
  if (w == 7) {
    waitv<0>();
    ST(1);
    bar();
    ST(2);
    float s[4] = {0.f, 0.f, 0.f, 0.f}, q[4] = {0.f, 0.f, 0.f, 0.f};
    const bf2 one = __builtin_bit_cast(bf2, 0x3f803f80u);
#pragma unroll 4
    for (int ks = 0; ks < 20; ++ks)
#pragma unroll
      for (int mt = 0; mt < 4; ++mt) {
        const u4 a = k3_afrag(pa[mt] + ks * 4096);
#pragma unroll
        for (int e = 0; e < 4; ++e) {
          const unsigned ae = a[e];
          const bf2 v = __builtin_bit_cast(bf2, ae);
          s[mt] = __builtin_amdgcn_fdot2_f32_bf16(v, one, s[mt], false);
          q[mt] = __builtin_amdgcn_fdot2_f32_bf16(v, v, q[mt], false);
        }
      }
#pragma unroll
    for (int mt = 0; mt < 4; ++mt) {
      s[mt] += __shfl_xor(s[mt], 16); s[mt] += __shfl_xor(s[mt], 32);
      q[mt] += __shfl_xor(q[mt], 16); q[mt] += __shfl_xor(q[mt], 32);
    }
    const float ss = g4 == 0 ? s[0] : g4 == 1 ? s[1] : g4 == 2 ? s[2] : s[3];
    const float qq = g4 == 0 ? q[0] : g4 == 1 ? q[1] : g4 == 2 ? q[2] : q[3];
    stat[lane] = ln_stats(ss, qq, 1.0f / D);
    ST(3);
    bar();
    ST(4);
    bar();
    ST(5);
    float ps = 0.f, pq = 0.f;
#pragma unroll
    for (int v = 0; v < 7; ++v) { const f2 t = part[v * 64 + lane]; ps += t.x; pq += t.y; }
    __hip_atomic_store(st + j * 64 + lane, ((u64)(__float_as_uint(pq) | 0x80000000u) << 32) | __float_as_uint(ps), __ATOMIC_RELAXED, __HIP_MEMORY_SCOPE_AGENT);
    u64 gv[8];
    for (int spin = 0; spin < (1 << 22); ++spin) {
      bool ok = true;
#pragma unroll
      for (int v = 0; v < 8; ++v) { gv[v] = __hip_atomic_load(st + v * 64 + lane, __ATOMIC_RELAXED, __HIP_MEMORY_SCOPE_AGENT); ok = ok && (gv[v] >> 63); }
      if (__all(ok)) break;
      __builtin_amdgcn_s_sleep(2);
    }
    ST(6);
    float ts = 0.f, tq = 0.f;
#pragma unroll
    for (int v = 0; v < 8; ++v) { ts += __uint_as_float((unsigned)gv[v]); tq += __uint_as_float((unsigned)(gv[v] >> 32) & 0x7fffffffu); }
    part[lane] = ln_stats(ts, tq, 1.0f / FH);
    bar();
    ST_END(2);
    return;
  }
  u4 b1[20], b2[20];
  {
    const unsigned vo = (unsigned)gb * (NKS1 * 1024) + lane * 16;
    k3_ldb<0>(b1, vo, ws + OFF_WF);
    k3_ldb<0>(b2, vo, ws + OFF_WF + (size_t)NGB * NKS1 * 1024);
  }
  waitv<40>();
  ST(1);
  bar();
  ST(2);
  f4 acc[4];
#pragma unroll
  for (int mt = 0; mt < 4; ++mt) acc[mt] = (f4){0.f, 0.f, 0.f, 0.f};
  k3_phase<0, 39, false>(b1, acc, pa);
  pin(cs1); pin(cs2);
  ST(3);
  bar();
  ST(4);
  float z[16];
  {
    float sv[16], qv[16];
    const float csf = __uint_as_float(cs1);
#pragma unroll
    for (int mt = 0; mt < 4; ++mt)
#pragma unroll
      for (int r2 = 0; r2 < 2; ++r2) {
        const f4 s2 = *(const f4*)(stat + 16 * mt + 4 * g4 + 2 * r2);
        const unsigned pz = pk_bf(gelu_t((acc[mt][2 * r2] - s2.x * csf) * s2.y), gelu_t((acc[mt][2 * r2 + 1] - s2.z * csf) * s2.w));
        z[4 * mt + 2 * r2] = bflo(pz); z[4 * mt + 2 * r2 + 1] = bfhi(pz);
      }
#pragma unroll
    for (int k = 0; k < 16; ++k) { sv[k] = live ? z[k] : 0.f; qv[k] = sv[k] * sv[k]; }
    bfly<DPP_MIRROR, 8>(sv, c & 8); bfly<DPP_HMIRROR, 4>(sv, c & 4); bfly<DPP_Q3210, 2>(sv, c & 2); bfly<DPP_Q1032, 1>(sv, c & 1);
    bfly<DPP_MIRROR, 8>(qv, c & 8); bfly<DPP_HMIRROR, 4>(qv, c & 4); bfly<DPP_Q3210, 2>(qv, c & 2); bfly<DPP_Q1032, 1>(qv, c & 1);
    part[w * 64 + 16 * (c >> 2) + 4 * g4 + (c & 3)] = (f2){sv[0], qv[0]};
  }
  bar();
  ST(5);
#pragma unroll
  for (int mt = 0; mt < 4; ++mt) acc[mt] = (f4){0.f, 0.f, 0.f, 0.f};
  k3_phase<0, 19, true>(b2, acc, pa);
  ST(6);
  if (live) {
#pragma unroll
    for (int mt = 0; mt < 4; ++mt) {
      const f2 s2 = stat[16 * mt + c];
      float g[4];
#pragma unroll
      for (int r = 0; r < 4; ++r) g[r] = gelu_t((acc[mt][r] - s2.x * __uint_as_float(cs2[r])) * s2.y);
      *(u2*)(ws + OFF_Z1 + ((size_t)(4 * i + mt) * NGB + gb) * 512 + lane * 8) = (u2){pk_bf(g[0], g[1]), pk_bf(g[2], g[3])};
    }
  }
  ST(7);
  bar();
  ST(8);
  if (live) {
#pragma unroll
    for (int a = 0; a < 2; ++a) {
      u4 o;
#pragma unroll
      for (int hh = 0; hh < 4; ++hh) {
        const int mt = 2 * a + (hh >> 1), r2 = hh & 1;
        const f4 s2 = *(const f4*)(part + 16 * mt + 4 * g4 + 2 * r2);
        o[hh] = pk_bf((z[4 * mt + 2 * r2] - s2.x) * s2.y, (z[4 * mt + 2 * r2 + 1] - s2.z) * s2.w);
      }
      *(u4*)(ws + OFF_Z2 + ((size_t)(2 * i + a) * NGB + gb) * 1024 + lane * 16) = o;
    }
  }
  ST_END(2);
}

constexpr int K4_RING = 3 * 8192, K4_LDS = 4 * K4_RING;
struct K4Ctx { unsigned vw[2], vz[7], ring; const char* wsrc; const char* zsrc; const char* rd[2]; };
template <int CH> DEVI void k4_issue(const K4Ctx& x, u4 (&zf)[3][14]) {
  if constexpr (CH < 8) {
#pragma unroll
    for (int n = 0; n < 8; ++n)
      dma16(x.ring + (CH % 3) * 8192 + n * 1024, x.vw[n & 1], x.wsrc + (size_t)(32 * (n >> 2) + 4 * ((n >> 1) & 1) + 16 * (n & 1)) * 4096 + CH * 128);
#pragma unroll
    for (int kh = 0; kh < 2; ++kh)
#pragma unroll
      for (int fb = 0; fb < 7; ++fb) gld16<0>(zf[CH % 3][kh * 7 + fb], x.vz[fb], x.zsrc + (size_t)(2 * CH + kh) * NGB * 1024);
  }
}
template <int CH> DEVI void k4_loop(const K4Ctx& x, u4 (&zf)[3][14], f4 (&acc)[7][4], u64* st_) {
  if constexpr (CH < 8) {
    if constexpr (CH < 7) waitv<22>(); else waitv<0>();
    if constexpr (CH == 0) ST(2); if constexpr (CH == 1) ST(3); if constexpr (CH == 4) ST(4); if constexpr (CH == 7) ST(5);
#pragma unroll
    for (int k = 0; k < 14; ++k) pin(zf[CH % 3][k]);
    u4 wf[2][4];
#pragma unroll
    for (int kh = 0; kh < 2; ++kh)
#pragma unroll
      for (int mt = 0; mt < 4; ++mt) wf[kh][mt] = *(const u4*)(x.rd[kh] + (CH % 3) * 8192 + mt * 2048);
    k4_issue<CH + 2>(x, zf);
    __builtin_amdgcn_sched_barrier(0);
#pragma unroll
    for (int kh = 0; kh < 2; ++kh)
#pragma unroll
      for (int fb = 0; fb < 7; ++fb)
#pragma unroll
        for (int mt = 0; mt < 4; ++mt) acc[fb][mt] = mfma_bf(zf[CH % 3][kh * 7 + fb], wf[kh][mt], acc[fb][mt]);
    __builtin_amdgcn_sched_barrier(0);
    k4_loop<CH + 1>(x, zf, acc, st_);
  }
}
__global__ __launch_bounds__(256, 1) void k4(const u16* __restrict__ wsp, const u16* __restrict__ bsp, char* __restrict__ ws) {
  __shared__ __attribute__((aligned(16))) char sm[K4_LDS];
  const int tid = threadIdx.x, lane = tid & 63;
  const int w = __builtin_amdgcn_readfirstlane(tid >> 6);
  int i, j; tile_of(blockIdx.x, i, j);
  const int c = lane & 15, g4 = lane >> 4;
  const int gs = grp_start(j), cnt = j < 6 ? 7 : 6;
  ST_DECL
  u2 z1[7]; unsigned bias;
  const int trow = 64 * i + 32 * (w >> 1) + 8 * (c >> 2) + 4 * (w & 1) + (c & 3);
  K4Ctx x;
#pragma unroll
  for (int fb = 0; fb < 7; ++fb) {
    const int gb = gs + fb > NGB - 1 ? NGB - 1 : gs + fb;
    gld8<0>(z1[fb], (unsigned)(w * NGB + gb) * 512 + lane * 8, ws + OFF_Z1 + (size_t)4 * i * NGB * 512);
    x.vz[fb] = (unsigned)(16 * w * NGB + gb) * 1024 + lane * 16;
  }
  gld2(bias, trow * 2, bsp);
#pragma unroll
  for (int o = 0; o < 2; ++o)
    x.vw[o] = (unsigned)(8 * (lane >> 5) + ((lane >> 3) & 3)) * 4096 + w * 1024 + (((lane & 7) ^ ((4 * o + (lane >> 4)) & 7)) * 16);
  x.ring = lds_addr(sm) + w * K4_RING;
  x.wsrc = (const char*)wsp + (size_t)i * 64 * 4096;
  x.zsrc = ws + OFF_Z2;
#pragma unroll
  for (int kh = 0; kh < 2; ++kh) x.rd[kh] = sm + w * K4_RING + c * 128 + (((4 * kh + g4) ^ ((c >> 1) & 7)) * 16);
  u4 zf[3][14];
  f4 acc[7][4];
#pragma unroll
  for (int fb = 0; fb < 7; ++fb)
#pragma unroll
    for (int mt = 0; mt < 4; ++mt) acc[fb][mt] = (f4){0.f, 0.f, 0.f, 0.f};
  k4_issue<0>(x, zf);
  k4_issue<1>(x, zf);
  ST(1);
  k4_loop<0>(x, zf, acc, st_);
  ST(6);
  f4 tot[7];
  {
    f4* red = (f4*)(sm + w * K4_RING) + lane;
#pragma unroll
    for (int v = 0; v < 4; ++v) {
      if (v == w) {
#pragma unroll
        for (int fb = 0; fb < 7; ++fb) tot[fb] = acc[fb][v];
      } else {
        const int sl = v > w ? v - 1 : v;
#pragma unroll
        for (int fb = 0; fb < 7; ++fb) red[(sl * 7 + fb) * 64] = acc[fb][v];
      }
    }
  }
  ST(7);
  bar();
  ST(8);
  {
    f4 sum[7];
#pragma unroll
    for (int fb = 0; fb < 7; ++fb) sum[fb] = (f4){0.f, 0.f, 0.f, 0.f};
#pragma unroll
    for (int v = 0; v < 4; ++v) {
      if (v == w) {
#pragma unroll
        for (int fb = 0; fb < 7; ++fb) sum[fb] += tot[fb];
      } else {
        const f4* rp = (const f4*)(sm + v * K4_RING) + (w > v ? w - 1 : w) * 7 * 64 + lane;
#pragma unroll
        for (int fb = 0; fb < 7; ++fb) sum[fb] += rp[fb * 64];
      }
    }
    pin(bias);
    const float bb = bflo(bias);
#pragma unroll
    for (int fb = 0; fb < 7; ++fb) {
      pin(z1[fb]);
      if (fb < cnt) {
        const int gb = gs + fb;
        const float g0 = rbf(rbf(sum[fb][0]) + bb), g1 = rbf(rbf(sum[fb][1]) + bb), g2 = rbf(rbf(sum[fb][2]) + bb), g3 = rbf(rbf(sum[fb][3]) + bb);
        *(u2*)(ws + OFF_V + ((size_t)(4 * i + w) * NKS3 + (gb >> 1)) * 1024 + lane * 16 + (gb & 1) * 8) =
            (u2){pk_bf(g0 * bflo(z1[fb].x), g1 * bfhi(z1[fb].x)), pk_bf(g2 * bflo(z1[fb].y), g3 * bfhi(z1[fb].y))};
      }
    }
  }
  ST_END(3);
}

constexpr int K5_H = 4 * 15 * 1024, K5_LDS = K5_H + 80 * 128;
constexpr int K5_WIN = 5;
template <int S> DEVI void k5_issue(u4 (&fr)[K5_WIN][9], unsigned vv, unsigned vo, const char* vsrc, const char* osrc, bool on) {
  if constexpr (S < 7) {
    if (S < 6 || on) {
#pragma unroll
      for (int mt = 0; mt < 4; ++mt) gld16<0>(fr[S % K5_WIN][mt], vv, vsrc + (size_t)(mt * NKS3 + S) * 1024);
#pragma unroll
      for (int db = 0; db < 5; ++db) gld16<0>(fr[S % K5_WIN][4 + db], vo, osrc + (size_t)(db * NKS3 + S) * 1024);
    }
  }
}
template <int S> DEVI void k5_loop(u4 (&fr)[K5_WIN][9], f4 (&acc)[5][4], unsigned vv, unsigned vo, const char* vsrc, const char* osrc, bool on, u64* st_) {
  if constexpr (S < 7) {
    if (S < 6 || on) {
      constexpr int younger = (S + K5_WIN - 1 < 5 ? K5_WIN - 1 : (5 - S > 0 ? 5 - S : 0));
      waitv<9 * younger>();
      if constexpr (S == 0) ST(2); if constexpr (S == 2) ST(3); if constexpr (S == 5) ST(4);
#pragma unroll
      for (int k = 0; k < 9; ++k) pin(fr[S % K5_WIN][k]);
#pragma unroll
      for (int db = 0; db < 5; ++db)
#pragma unroll
        for (int mt = 0; mt < 4; ++mt) acc[db][mt] = mfma_bf(fr[S % K5_WIN][4 + db], fr[S % K5_WIN][mt], acc[db][mt]);
      __builtin_amdgcn_sched_barrier(0);
      k5_issue<S + K5_WIN>(fr, vv, vo, vsrc, osrc, on);
    }
    k5_loop<S + 1>(fr, acc, vv, vo, vsrc, osrc, on, st_);
  }
}
template <int S> DEVI void k5_pro(u4 (&fr)[K5_WIN][9], unsigned vv, unsigned vo, const char* vsrc, const char* osrc) {
  if constexpr (S < K5_WIN) { k5_issue<S>(fr, vv, vo, vsrc, osrc, true); k5_pro<S + 1>(fr, vv, vo, vsrc, osrc); }
}
__global__ __launch_bounds__(256, 1) void k5(char* __restrict__ ws, u16* __restrict__ out) {
  __shared__ __attribute__((aligned(16))) char sm[K5_LDS];
  const int tid = threadIdx.x, lane = tid & 63;
  const int w = __builtin_amdgcn_readfirstlane(tid >> 6);
  int i, j; tile_of(blockIdx.x, i, j);
  const int c = lane & 15, g4 = lane >> 4;
  ST_DECL
  {
    const unsigned vo = (unsigned)(8 * w + (lane >> 3)) * 4096 + (lane & 7) * 16;
    const char* src = ws + OFF_HT + (size_t)j * 80 * 4096 + (size_t)i * 128;
    const unsigned dst = lds_addr(sm) + K5_H + w * 1024;
    dma16(dst, vo, src);
    dma16(dst + 4096, vo, src + 32 * 4096);
    if (w < 2) dma16(dst + 8192, vo, src + 64 * 4096);
  }
  const unsigned vv = (unsigned)(7 * w) * 1024 + lane * 16;
  const char* vsrc = ws + OFF_V + (size_t)4 * i * NKS3 * 1024;
  const char* osrc = ws + OFF_WO + (size_t)5 * j * NKS3 * 1024;
  u4 fr[K5_WIN][9];
  f4 acc[5][4];
#pragma unroll
  for (int db = 0; db < 5; ++db)
#pragma unroll
    for (int mt = 0; mt < 4; ++mt) acc[db][mt] = (f4){0.f, 0.f, 0.f, 0.f};
  k5_pro<0>(fr, vv, vv, vsrc, osrc);
  ST(1);
  k5_loop<0>(fr, acc, vv, vv, vsrc, osrc, w < 3, st_);
  ST(5);
  f4 tot[5];
  {
    f4* red = (f4*)(sm + w * 15360) + lane;
#pragma unroll
    for (int v = 0; v < 4; ++v) {
      if (v == w) {
#pragma unroll
        for (int db = 0; db < 5; ++db) tot[db] = acc[db][v];
      } else {
        const int sl = v > w ? v - 1 : v;
#pragma unroll
        for (int db = 0; db < 5; ++db) red[(sl * 5 + db) * 64] = acc[db][v];
      }
    }
  }
  ST(6);
  bar();
  ST(7);
  f4 sum[5];
#pragma unroll
  for (int db = 0; db < 5; ++db) sum[db] = (f4){0.f, 0.f, 0.f, 0.f};
#pragma unroll
  for (int v = 0; v < 4; ++v) {
    if (v == w) {
#pragma unroll
      for (int db = 0; db < 5; ++db) sum[db] += tot[db];
    } else {
      const f4* rp = (const f4*)(sm + v * 15360) + (w > v ? w - 1 : w) * 5 * 64 + lane;
#pragma unroll
      for (int db = 0; db < 5; ++db) sum[db] += rp[db * 64];
    }
  }
  const int trow = 64 * i + 32 * (w >> 1) + 8 * (c >> 2) + 4 * (w & 1) + (c & 3);
  const char* hp = sm + K5_H + (4 * g4 + (c >> 2)) * 128 + w * 32 + (c & 3) * 8;
#pragma unroll
  for (int db = 0; db < 5; ++db) {
    const u2 hv = __builtin_bit_cast(u2, __builtin_amdgcn_ds_read_tr16_b64_v4i16((LDSP s4*)(hp + db * 2048)));
    *(u2*)(out + (size_t)trow * D + 80 * j + 16 * db + 4 * g4) =
        (u2){pk_bf(bflo(hv.x) + rbf(sum[db][0]), bfhi(hv.x) + rbf(sum[db][1])), pk_bf(bflo(hv.y) + rbf(sum[db][2]), bfhi(hv.y) + rbf(sum[db][3]))};
  }
  ST_END(4);
}

extern "C" void kernel_launch(void* const* d_in, const int* in_sizes, int n_in, void* d_out, int out_size, void* d_ws, size_t ws_size,
                              hipStream_t stream) {
  char* ws = (char*)d_ws;
  k1<<<NB_LN + NB_KG + NB_WI + NB_WO, 256, 0, stream>>>((const u16*)d_in[0], (const u16*)d_in[1], (const u16*)d_in[2], (const u16*)d_in[3],
                                                         (const u16*)d_in[4], (const u16*)d_in[5], (const u16*)d_in[8], (const u16*)d_in[11], ws);
  k2<<<D + 7, 256, 0, stream>>>((const unsigned*)d_in[6], (const float*)(ws + OFF_KSC), ws);
  k3<<<256, 512, 0, stream>>>(ws);
  k4<<<256, 256, 0, stream>>>((const u16*)d_in[9], (const u16*)d_in[10], ws);
  k5<<<256, 256, 0, stream>>>(ws, (u16*)d_out);
}
```

```cpp
#include <hip/hip_runtime.h>

typedef unsigned short u16;
typedef unsigned long long u64;
typedef __attribute__((ext_vector_type(8))) __bf16 bf8;
typedef __attribute__((ext_vector_type(2))) __bf16 bf2;
typedef __attribute__((ext_vector_type(8))) _Float16 h8;
typedef __attribute__((ext_vector_type(2))) _Float16 h2;
typedef __attribute__((ext_vector_type(4))) float f4;
typedef __attribute__((ext_vector_type(2))) float f2;
typedef __attribute__((ext_vector_type(4))) short s4;
typedef __attribute__((ext_vector_type(4))) unsigned u4;
typedef __attribute__((ext_vector_type(2))) unsigned u2;
#define LDSP __attribute__((address_space(3)))
#define DEVI __device__ __forceinline__
#define ST_DECL u64* st_ = nullptr;
#define ST(i)
#define ST_END(kid)

constexpr int L = 2048, D = 640, NM = 128, F = 1728, FH = 864;
constexpr int NGB = 54;
constexpr int NKS1 = 20;
constexpr int NKS3 = 27;

constexpr size_t OFF_UT = 0;
constexpr size_t OFF_XT = OFF_UT + (size_t)D * L * 2;
constexpr size_t OFF_KR = OFF_XT + (size_t)D * L * 2;
constexpr size_t OFF_HT = OFF_KR + (size_t)D * L * 4;
constexpr size_t OFF_WF = OFF_HT + (size_t)D * L * 2;
constexpr size_t OFF_WO = OFF_WF + (size_t)2 * NGB * NKS1 * 1024;
constexpr size_t OFF_Z1 = OFF_WO + (size_t)40 * NKS3 * 1024;
constexpr size_t OFF_Z2 = OFF_Z1 + (size_t)128 * NGB * 512;
constexpr size_t OFF_V = OFF_Z2 + (size_t)64 * NGB * 1024;
constexpr size_t OFF_ST = OFF_V + (size_t)128 * NKS3 * 1024;
constexpr size_t OFF_CSP = OFF_ST + (size_t)32 * 8 * 64 * 8;
constexpr size_t OFF_CS = OFF_CSP + (size_t)5 * F * 4;
constexpr size_t OFF_KSC = OFF_CS + (size_t)F * 4;
constexpr size_t OFF_G1 = 32u << 20;
constexpr size_t OFF_F1 = OFF_G1 + 32768;
constexpr size_t OFF_EP = OFF_G1 + 65536;
constexpr size_t OFF_F2 = OFF_G1 + 131072;
constexpr size_t OFF_F3 = OFF_F2 + 32768;
constexpr size_t OFF_ZE = OFF_F3 + 32768;

DEVI float bflo(unsigned v) { return __uint_as_float(v << 16); }
DEVI float bfhi(unsigned v) { return __uint_as_float(v & 0xffff0000u); }
DEVI unsigned pk_bf(float a, float b) { f2 p = {a, b}; return __builtin_bit_cast(unsigned, __builtin_convertvector(p, bf2)); }
DEVI unsigned pk_h(float a, float b) { f2 p = {a, b}; return __builtin_bit_cast(unsigned, __builtin_convertvector(p, h2)); }
DEVI float rbf(float a) { return bflo(pk_bf(a, 0.f)); }
DEVI float hlo(unsigned v) { return (float)__builtin_bit_cast(h2, v).x; }
DEVI float hhi(unsigned v) { return (float)__builtin_bit_cast(h2, v).y; }
DEVI f4 mfma_bf(u4 a, u4 b, f4 c) { return __builtin_amdgcn_mfma_f32_16x16x32_bf16(__builtin_bit_cast(bf8, a), __builtin_bit_cast(bf8, b), c, 0, 0, 0); }
DEVI f4 mfma_h(u4 a, u4 b, f4 c) { return __builtin_amdgcn_mfma_f32_16x16x32_f16(__builtin_bit_cast(h8, a), __builtin_bit_cast(h8, b), c, 0, 0, 0); }
template <int CTRL> DEVI float dpp(float v) { return __int_as_float(__builtin_amdgcn_update_dpp(0, __float_as_int(v), CTRL, 0xf, 0xf, true)); }
constexpr int DPP_ROR1 = 0x121, DPP_ROR2 = 0x122, DPP_ROR4 = 0x124, DPP_ROR8 = 0x128, DPP_MIRROR = 0x140, DPP_HMIRROR = 0x141, DPP_Q3210 = 0x1B, DPP_Q1032 = 0xB1;

DEVI f2 ln_stats(float s, float q, float inv_n) {
  float m = s * inv_n;
  float v = fmaxf(q * inv_n - m * m, 0.f);
  float sd = rbf(__builtin_amdgcn_sqrtf(rbf(rbf(v) + 1e-5f)));
  f2 r = {rbf(m), __builtin_amdgcn_rcpf(sd)};
  return r;
}
DEVI float gelu_t(float x) {
  float t = __builtin_fmaf(x * x, -0.1029432397f, -2.3022081985f);
  float e = __builtin_amdgcn_exp2f(x * t);
  return x * __builtin_amdgcn_rcpf(1.0f + e);
}

template <int IMM> DEVI void gld16(u4& d, unsigned voff, const void* sb) { asm volatile("global_load_dwordx4 %0, %1, %2 offset:%3" : "=v"(d) : "v"(voff), "s"(sb), "i"(IMM)); }
template <int IMM> DEVI void gld8(u2& d, unsigned voff, const void* sb) { asm volatile("global_load_dwordx2 %0, %1, %2 offset:%3" : "=v"(d) : "v"(voff), "s"(sb), "i"(IMM)); }
template <int IMM> DEVI void gld4(unsigned& d, unsigned voff, const void* sb) { asm volatile("global_load_dword %0, %1, %2 offset:%3" : "=v"(d) : "v"(voff), "s"(sb), "i"(IMM)); }
DEVI void gld2(unsigned& d, unsigned voff, const void* sb) { asm volatile("global_load_ushort %0, %1, %2" : "=v"(d) : "v"(voff), "s"(sb)); }
DEVI void dma16(unsigned ldsdst, unsigned voff, const void* sb) {
  asm volatile("s_mov_b32 m0, %0\n\ts_nop 0\n\tglobal_load_lds_dwordx4 %1, %2" ::"s"(ldsdst), "v"(voff), "s"(sb) : "memory");
}
template <int N> DEVI void waitv() { asm volatile("s_waitcnt vmcnt(%0)" ::"i"(N) : "memory"); }
template <int N> DEVI void waitv(u4& a) { asm volatile("s_waitcnt vmcnt(%1)" : "+v"(a) : "i"(N) : "memory"); }
DEVI void pin(u4& a) { asm volatile("" : "+v"(a)); }
DEVI void pin(u2& a) { asm volatile("" : "+v"(a)); }
DEVI void pin(unsigned& a) { asm volatile("" : "+v"(a)); }
DEVI void bar() { asm volatile("s_waitcnt lgkmcnt(0)\n\ts_barrier" ::: "memory"); }
DEVI unsigned lds_addr(const void* p) { return (unsigned)(size_t)(LDSP const char*)p; }

DEVI void tr8x8(const u4 (&r)[8], u4 (&o)[8]) {
#pragma unroll
  for (int c2 = 0; c2 < 4; ++c2)
#pragma unroll
    for (int m = 0; m < 4; ++m) {
      o[2 * c2][m] = __builtin_amdgcn_perm(r[2 * m + 1][c2], r[2 * m][c2], 0x05040100u);
      o[2 * c2 + 1][m] = __builtin_amdgcn_perm(r[2 * m + 1][c2], r[2 * m][c2], 0x07060302u);
    }
}

constexpr int KG_PITCH = 528;
constexpr int KC_PITCH = 4160;
constexpr int RC_SZ = 30208;
constexpr int KA_K = 3 * RC_SZ, KA_U = KA_K + 12 * KC_PITCH, U_PITCH = 4608, KA_T = KA_U + 3 * U_PITCH, T_PITCH = 1536;
constexpr int KA_S = KA_T + 3 * T_PITCH, KA_LDS = KA_S + 96;

struct c2 { f2 x, y; };
DEVI c2 cmul(c2 a, c2 b) { c2 r; r.x = a.x * b.x - a.y * b.y; r.y = a.x * b.y + a.y * b.x; return r; }
DEVI c2 cexpp(f2 a2, f2 br, float p) {
  const f2 ea = a2 * p, ph = br * p;
  const f2 e = {__builtin_amdgcn_exp2f(ea.x), __builtin_amdgcn_exp2f(ea.y)};
  const float p0 = __builtin_amdgcn_fractf(ph.x), p1 = __builtin_amdgcn_fractf(ph.y);
  c2 r;
  r.x = e * (f2){__builtin_amdgcn_cosf(p0), __builtin_amdgcn_cosf(p1)};
  r.y = e * (f2){__builtin_amdgcn_sinf(p0), __builtin_amdgcn_sinf(p1)};
  return r;
}
DEVI unsigned pk_h2(f2 p) { return __builtin_bit_cast(unsigned, __builtin_convertvector(p, h2)); }
template <int CTRL, int RM> DEVI float dppm(float v) { return __int_as_float(__builtin_amdgcn_update_dpp(0, __float_as_int(v), CTRL, RM, 0xf, false)); }
DEVI float wave_sum63(float v) {
  v += dpp<DPP_ROR8>(v); v += dpp<DPP_ROR4>(v); v += dpp<DPP_ROR2>(v); v += dpp<DPP_ROR1>(v);
  v += dppm<0x142, 0xa>(v); v += dppm<0x143, 0xc>(v);
  return v;
}
template <int IMM> DEVI void lds8(u2& d, unsigned a) { asm volatile("ds_read_b64 %0, %1 offset:%2" : "=v"(d) : "v"(a), "i"(IMM)); }
template <int IMM> DEVI void lds16(u4& d, unsigned a) { asm volatile("ds_read_b128 %0, %1 offset:%2" : "=v"(d) : "v"(a), "i"(IMM)); }
template <int N> DEVI void waitl(u2& a, u2& b, u4& c) { asm volatile("s_waitcnt lgkmcnt(%3)" : "+v"(a), "+v"(b), "+v"(c) : "i"(N) : "memory"); }
template <int IMM> DEVI void gld16s(u4& d, unsigned voff, const void* sb) { asm volatile("global_load_dwordx4 %0, %1, %2 offset:%3 sc1" : "=v"(d) : "v"(voff), "s"(sb), "i"(IMM)); }
DEVI void gld4s(unsigned& d, unsigned voff, const void* sb) { asm volatile("global_load_dword %0, %1, %2 sc1" : "=v"(d) : "v"(voff), "s"(sb)); }
DEVI unsigned perm(unsigned hi, unsigned lo, unsigned sel) { return __builtin_amdgcn_perm(hi, lo, sel); }

template <int RR, int P> DEVI void conv_ld(u2 (&al)[8], u2 (&ah)[8], u4 (&bb)[8], unsigned pa, unsigned pb) {
  if constexpr (P < 8) {
    lds8<64 * (1 - RR) + 512 * (7 - P)>(al[P], pa);
    lds8<64 * (1 - RR) + 512 * (7 - P) + 8>(ah[P], pa);
    lds16<64 * (1 - RR) + 512 * P>(bb[P], pb);
    conv_ld<RR, P + 1>(al, ah, bb, pa, pb);
  }
}
template <int S> DEVI void conv_mm(u2 (&al)[8], u2 (&ah)[8], u4 (&bb)[8], f4 (&acc)[8]) {
  if constexpr (S < 8) {
    waitl<(3 * (7 - S) > 15 ? 15 : 3 * (7 - S))>(al[S], ah[S], bb[S]);
#pragma unroll
    for (int p = 0; p <= S; ++p) acc[S] = mfma_h((u4){al[p].x, al[p].y, ah[p].x, ah[p].y}, bb[S - p], acc[S]);
    __builtin_amdgcn_sched_barrier(0);
    conv_mm<S + 1>(al, ah, bb, acc);
  }
}
template <int IMM> DEVI void gld8s(u2& d, unsigned voff, const void* sb) { asm volatile("global_load_dwordx2 %0, %1, %2 offset:%3 sc1" : "=v"(d) : "v"(voff), "s"(sb), "i"(IMM)); }
DEVI void gst16s(unsigned voff, u4 v, const void* sb) { asm volatile("global_store_dwordx4 %0, %1, %2 sc1\n\ts_nop 1" ::"v"(voff), "v"(v), "s"(sb) : "memory"); }
DEVI u4 tr2(const char* p, int step) {
  const u2 a = __builtin_bit_cast(u2, __builtin_amdgcn_ds_read_tr16_b64_v4i16((LDSP s4*)p));
  const u2 b = __builtin_bit_cast(u2, __builtin_amdgcn_ds_read_tr16_b64_v4i16((LDSP s4*)(p + step)));
  return (u4){a.x, a.y, b.x, b.y};
}
template <bool TWO, int K> DEVI void tbl_ld(u4 (&a)[8], u4 (&c)[8], u4 (&e)[8], unsigned pa, unsigned pb) {
  if constexpr (K < 8) {
    lds16<64 * K>(a[K], pa); lds16<64 * K>(c[K], pb);
    if constexpr (TWO) lds16<64 * K + 16 * KG_PITCH>(e[K], pb);
    tbl_ld<TWO, K + 1>(a, c, e, pa, pb);
  }
}
template <bool TWO, int K> DEVI void tbl_mm(u4 (&a)[8], u4 (&c)[8], u4 (&e)[8], f4& acc, f4& ac0) {
  if constexpr (K < 8) {
    constexpr int N = (TWO ? 3 : 2) * (7 - K);
    if constexpr (TWO) asm volatile("s_waitcnt lgkmcnt(%3)" : "+v"(a[K]), "+v"(c[K]), "+v"(e[K]) : "i"(N > 15 ? 15 : N) : "memory");
    else asm volatile("s_waitcnt lgkmcnt(%2)" : "+v"(a[K]), "+v"(c[K]) : "i"(N) : "memory");
    acc = mfma_h(a[K], c[K], acc);
    if constexpr (TWO) ac0 = mfma_h(e[K], c[K], ac0);
    __builtin_amdgcn_sched_barrier(0);
    tbl_mm<TWO, K + 1>(a, c, e, acc, ac0);
  }
}
template <bool TWO> DEVI void tbl_tiles(unsigned pa, unsigned pb, f4& acc, f4& ac0) {
  u4 a[8], c[8], e[8];
  tbl_ld<TWO, 0>(a, c, e, pa, pb);
  tbl_mm<TWO, 0>(a, c, e, acc, ac0);
}
DEVI void chsync(unsigned fl, int wq, unsigned phase) {
  asm volatile("s_waitcnt lgkmcnt(0)\n\tds_write_b32 %0, %1" ::"v"(fl + 4 * wq), "v"(phase) : "memory");
  for (;;) {
    u4 v;
    asm volatile("ds_read_b128 %0, %1\n\ts_waitcnt lgkmcnt(0)" : "=v"(v) : "v"(fl) : "memory");
    if (__builtin_amdgcn_readfirstlane(min(min(v.x, v.y), min(v.z, v.w))) >= phase) break;
    __builtin_amdgcn_s_sleep(1);
  }
}
DEVI void rawbar() { asm volatile("s_barrier" ::: "memory"); }
template <int I> DEVI void poll_ld(u4 (&g)[4], unsigned go, const char* base) {
  if constexpr (I < 4) { gld16s<16 * I>(g[I], go, base); poll_ld<I + 1>(g, go, base); }
}
DEVI void wait4(u4 (&g)[4]) { asm volatile("s_waitcnt vmcnt(0)" : "+v"(g[0]), "+v"(g[1]), "+v"(g[2]), "+v"(g[3])::"memory"); }

__global__ __launch_bounds__(1024, 4) void ka(const u16* __restrict__ x, const u16* __restrict__ logA, const u16* __restrict__ Aim,
                                          const u16* __restrict__ Cre, const u16* __restrict__ Cim, const unsigned* __restrict__ logdt,
                                          const unsigned* __restrict__ dskip, const u16* __restrict__ w_in, const u16* __restrict__ wsp,
                                          const u16* __restrict__ w_out, char* __restrict__ ws) {
  __shared__ __attribute__((aligned(16))) char sm[KA_LDS];
  const int tid = threadIdx.x, lane = tid & 63;
  const int w = __builtin_amdgcn_readfirstlane(tid >> 6);
  const int b = blockIdx.x;
  ST_DECL
  const int g = b & 7, m = b >> 3;
  const bool isconv = m < 27;
  const int d0 = 80 * g + (3 * m < 77 ? 3 * m : 77);
  if (tid < 16) ((int*)(sm + KA_S + 32))[tid] = 0;
  unsigned tag = 0;
  if (w < 4) {
    u4 r0, r1; u2 r2;
    unsigned ep;
    const int row = 256 * g + 8 * m + 2 * w + (lane >> 5), tm = 4 * g + (m >> 3), tg = m & 7;
    const unsigned ro = (unsigned)row * 1280 + (lane & 31) * 16;
    gld4s(ep, 0, ws + OFF_EP);
    gld16<0>(r0, ro, x); gld16<512>(r1, ro, x); gld8<1024>(r2, ro - (lane & 31) * 8, x);
    bar();
    {
      const char* tsrc = (const char*)x + (size_t)tm * 64 * 1280 + tg * 160;
      const unsigned dst = lds_addr(sm) + KA_U + w * 1024;
#pragma unroll
      for (int i = 0; i < 3; ++i) { const unsigned pc = tid + 256 * i < 640 ? tid + 256 * i : 639; dma16(dst + i * 4096, (pc / 10) * 1280 + (pc % 10) * 16, tsrc); }
    }
    __builtin_amdgcn_s_setprio(2);
    asm volatile("s_waitcnt vmcnt(3)" : "+v"(ep), "+v"(r0), "+v"(r1), "+v"(r2)::"memory");
    ST(1);
    tag = ep + 0x9E3779B9u;
    f2 s0 = {bflo(r2.x), bfhi(r2.x)}, s1 = {bflo(r2.y), bfhi(r2.y)}, q0 = s0 * s0, q1 = s1 * s1;
#pragma unroll
    for (int c = 0; c < 4; ++c) {
      const unsigned v = r0[c], v2 = r1[c];
      const f2 a = {bflo(v), bfhi(v)}, a2 = {bflo(v2), bfhi(v2)};
      s0 += a; q0 += a * a; s1 += a2; q1 += a2 * a2;
    }
    s0 += s1; q0 += q1;
    float ss = s0.x + s0.y, qq = q0.x + q0.y;
    ss += dpp<DPP_ROR8>(ss); qq += dpp<DPP_ROR8>(qq); ss += dpp<DPP_ROR4>(ss); qq += dpp<DPP_ROR4>(qq);
    ss += dpp<DPP_ROR2>(ss); qq += dpp<DPP_ROR2>(qq); ss += dpp<DPP_ROR1>(ss); qq += dpp<DPP_ROR1>(qq);
    ss += dppm<0x142, 0xa>(ss); qq += dppm<0x142, 0xa>(qq);
    const float mean = ss * (1.0f / D);
    const float var = fmaxf(qq * (1.0f / D) - mean * mean, 0.f);
    unsigned pay = pk_bf(mean, __builtin_amdgcn_sqrtf(rbf(rbf(var) + 1e-5f)));
    pin(pay);
    u4 gv = {(unsigned)__builtin_amdgcn_readlane(pay, 31), tag, pay, tag};
    pin(gv);
    waitv<0>();
    if (lane == 63) gst16s((row - 1) * 8, gv, ws + OFF_G1);
    chsync(lds_addr(sm) + KA_S + 80, w, 1);
    ST(2);
#pragma unroll 1
    for (int bl = w; bl < 5; bl += 4) {
      const int l = lane & 15, gm = lane >> 4;
      const char* tp = sm + KA_U + (8 * gm + (l >> 2)) * 160 + bl * 32 + (l & 3) * 8;
      const u4 A = tr2(tp, 640), B = tr2(tp + 32 * 160, 640);
      u4 s1v, s2v;
#pragma unroll
      for (int e = 0; e < 4; ++e) {
        const unsigned ae = A[e], be = B[e];
        s1v[e] = __builtin_amdgcn_update_dpp(ae, be, DPP_ROR8, 0xf, 0xc, false);
        s2v[e] = __builtin_amdgcn_update_dpp(be, ae, DPP_ROR8, 0xf, 0x3, false);
      }
      const unsigned vo = (unsigned)(80 * tg + 16 * bl + (l & 7)) * 4096 + tm * 128 + (gm + 4 * (l >> 3)) * 16;
      gst16s(vo, s1v, ws + OFF_XT);
      gst16s(vo + 8 * 4096, s2v, ws + OFF_XT);
    }
    waitv<0>();
    chsync(lds_addr(sm) + KA_S + 80, w, 2);
    if (tid == 0) __hip_atomic_store((unsigned*)(ws + OFF_F1) + (32 * tg + tm) * 32, tag, __ATOMIC_RELAXED, __HIP_MEMORY_SCOPE_AGENT);
    ST(3);
    if (isconv) {
      u4 gr[4], xr[3];
      poll_ld<0>(gr, tid * 64, ws + OFF_G1);
      if (w == 0) {
#pragma unroll 1
        for (int spin = 0; spin < (1 << 16); ++spin) {
          unsigned v;
          asm volatile("global_load_dword %0, %1, %2 sc1\n\ts_waitcnt vmcnt(0)" : "=v"(v) : "v"((lane & 31) * 128), "s"(ws + OFF_F1 + g * 4096) : "memory");
          if (__all(v == tag)) break;
          __builtin_amdgcn_s_sleep(2);
        }
      }
      wait4(gr);
      chsync(lds_addr(sm) + KA_S + 80, w, 3);
      ST(4);
      const char* xsrc = ws + OFF_XT + (size_t)d0 * 4096;
      gld16s<0>(xr[0], tid * 16, xsrc); gld16s<0>(xr[1], tid * 16, xsrc + 4096); gld16s<0>(xr[2], tid * 16, xsrc + 8192);
#pragma unroll 1
      for (int spin = 0; spin < (1 << 16); ++spin) {
        bool ok = true;
#pragma unroll
        for (int i = 0; i < 4; ++i) ok = ok && gr[i].y == tag && gr[i].w == tag;
        if (__all(ok)) break;
        poll_ld<0>(gr, tid * 64, ws + OFF_G1);
        wait4(gr);
      }
      f2 mm[4], rs[4];
#pragma unroll
      for (int i = 0; i < 4; ++i) { mm[i] = (f2){bflo(gr[i].x), bflo(gr[i].z)}; rs[i] = (f2){__builtin_amdgcn_rcpf(bfhi(gr[i].x)), __builtin_amdgcn_rcpf(bfhi(gr[i].z))}; }
      if (tid < 90) *(u4*)(sm + KA_U + (tid / 30) * U_PITCH + (tid % 30) * 16) = (u4){0, 0, 0, 0};
      ST(7);
      asm volatile("s_waitcnt vmcnt(0)" : "+v"(xr[0]), "+v"(xr[1]), "+v"(xr[2])::"memory");
      ST(8);
#pragma unroll
      for (int c = 0; c < 3; ++c) {
        u4 o;
#pragma unroll
        for (int i = 0; i < 4; ++i) { const unsigned v = xr[c][i]; o[i] = pk_h2(((f2){bflo(v), bfhi(v)} - mm[i]) * rs[i]); }
        *(u4*)(sm + KA_U + c * U_PITCH + 480 + tid * 16) = o;
      }
      __builtin_amdgcn_s_setprio(0);
      bar();
      ST(6);
      if (b == 0 && tid == 0) __hip_atomic_store((unsigned*)(ws + OFF_EP), tag, __ATOMIC_RELAXED, __HIP_MEMORY_SCOPE_AGENT);
      __builtin_amdgcn_s_sleep(4);
      if (w != 0) {
        unsigned pw[4];
#pragma unroll
        for (int i = 0; i < 4; ++i) {
          const unsigned ln = (i * 81 + m * 3 + w - 1) * 64 + lane;
          gld4<0>(pw[i], (ln < 16384 ? ln : 16383) * 128, (const char*)wsp + (size_t)(g >> 1) * (16384 * 128));
        }
        asm volatile("s_waitcnt vmcnt(0)" : "+v"(pw[0]), "+v"(pw[1]), "+v"(pw[2]), "+v"(pw[3])::"memory");
      } else {
      const int item = b * 40 + lane;
      if (lane < 40) {
        const int oc = item % 80, kq = item / 80, kap = kq >> 2, qq = kq & 3;
        u4 r[8], o[8];
#pragma unroll
        for (int e = 0; e < 8; ++e) r[e] = *(const u4*)(w_out + (size_t)(32 * kap + 16 * (e >> 2) + 4 * qq + (e & 3)) * D + oc * 8);
        tr8x8(r, o);
        u4* dst = (u4*)(ws + OFF_WO + ((size_t)(oc >> 1) * NKS3 + kap) * 1024) + qq * 16 + (oc & 1) * 8;
#pragma unroll
        for (int c = 0; c < 8; ++c) dst[c] = o[c];
      }
      }
      ST_END(0);
      return;
    }
    __builtin_amdgcn_s_setprio(0);
  }
  if (!isconv) {
    if (w >= 4) rawbar();
    rawbar();
    const int si = b - 216;
    if (si < 27) {
      const int tt = tid - 384, co = tt & 7, ko = tt >> 3, c0 = 64 * si + 8 * co, k0 = 8 * ko;
      float* part = (float*)(sm + 98304);
      if (w >= 6) {
        u4 r[8], o[8];
#pragma unroll
        for (int e = 0; e < 8; ++e) r[e] = *(const u4*)(w_in + (size_t)(k0 + e) * F + c0);
        float cs[8];
#pragma unroll
        for (int c = 0; c < 4; ++c) {
          f2 s = {0.f, 0.f};
#pragma unroll
          for (int e = 0; e < 8; ++e) { const unsigned v = r[e][c]; s += (f2){bflo(v), bfhi(v)}; }
          cs[2 * c] = s.x + dpp<DPP_ROR8>(s.x); cs[2 * c + 1] = s.y + dpp<DPP_ROR8>(s.y);
        }
        if (!(lane & 8)) {
          f4* p = (f4*)(part + ((w - 6) * 4 + (lane >> 4)) * 64 + co * 8);
          p[0] = (f4){cs[0], cs[1], cs[2], cs[3]}; p[1] = (f4){cs[4], cs[5], cs[6], cs[7]};
        }
        tr8x8(r, o);
#pragma unroll
        for (int c = 0; c < 8; ++c) *(u4*)(sm + tt * 144 + c * 16) = o[c];
      }
      __syncthreads();
      if (w < 8) {
        float s = 0.f;
#pragma unroll
        for (int e = 0; e < 5; ++e) s += part[(w * 5 + e) * 64 + lane];
        part[2560 + w * 64 + lane] = s;
      }
#pragma unroll
      for (int i = 0; i < 5; ++i) {
        const int f = w * 5 + i, blk = f / 20, ks = f % 20, gc = 4 * si + blk;
        const u4 v = *(const u4*)(sm + ((4 * ks + (lane >> 4)) * 8 + 2 * blk + ((lane >> 3) & 1)) * 144 + (lane & 7) * 16);
        *(u4*)(ws + OFF_WF + ((size_t)(gc >= NGB ? gc - NGB : gc + NGB) * NKS1 + ks) * 1024 + lane * 16) = v;
      }
      __syncthreads();
      if (w == 0) {
        float s = 0.f;
#pragma unroll
        for (int e = 0; e < 8; ++e) s += part[2560 + e * 64 + lane];
        ((float*)(ws + OFF_CS))[64 * si + lane] = s;
      }
    } else if (si < 35) {
      ((u4*)(ws + OFF_ST))[(si - 27) * 1024 + tid] = (u4){0, 0, 0, 0};
    } else if (si == 35) {
      if (tid < 576) *(u4*)(ws + OFF_F2 + (tid < 512 ? tid * 128 : 65536 + (tid - 512) * 16)) = (u4){0, 0, 0, 0};
    }
    ST_END(0);
    return;
  }
  const int ch = w < 4 ? 0 : (w >> 2) - 1, role = (w + ch) & 3;
  const int d = d0 + ch;
  const int col = lane & 15, q = lane >> 4;
  char* rc = sm + ch * RC_SZ;
  float* slot = (float*)(sm + KA_S) + 2 * ch;
  const unsigned fl = lds_addr(sm) + KA_S + 32 + 16 * ch;
  if (w >= 4) {
    unsigned la, ai, cr, ci, ldw, dw;
    gld4<0>(ldw, 0, logdt + (d >> 1));
    gld4<0>(dw, 0, dskip + (d >> 1));
    gld4<0>(la, lane * 4, logA + d * NM);
    gld4<0>(ai, lane * 4, Aim + d * NM);
    gld4<0>(cr, lane * 4, Cre + d * NM);
    gld4<0>(ci, lane * 4, Cim + d * NM);
    rawbar();
    asm volatile("s_waitcnt vmcnt(0)" : "+v"(la), "+v"(ai), "+v"(cr), "+v"(ci), "+v"(ldw), "+v"(dw)::"memory");
    ST(1);
    const float dt = rbf(__expf((d & 1) ? bfhi(ldw) : bflo(ldw)));
    const unsigned ea = pk_bf(__expf(bflo(la)), __expf(bfhi(la)));
    const f2 a2 = (f2){bflo(ea), bfhi(ea)} * (-1.4426950408889634f * dt);
    f2 br = (f2){bflo(ai), bfhi(ai)} * (dt * 0.15915494309189535f);
    char* base = rc + lane * 8;
    auto put = [&](int row, c2 v) { *(u2*)(base + row * KG_PITCH) = (u2){pk_h2(v.x), pk_h2(v.y)}; };
    if (role == 0) {
      c2 v = {{bflo(cr), bfhi(cr)}, {bflo(ci), bfhi(ci)}};
      float mx = fmaxf(fmaxf(fabsf(v.x.x), fabsf(v.x.y)), fmaxf(fabsf(v.y.x), fabsf(v.y.y)));
      mx = fmaxf(mx, dpp<DPP_ROR8>(mx)); mx = fmaxf(mx, dpp<DPP_ROR4>(mx)); mx = fmaxf(mx, dpp<DPP_ROR2>(mx)); mx = fmaxf(mx, dpp<DPP_ROR1>(mx));
      mx = fmaxf(mx, dppm<0x142, 0xa>(mx)); mx = fmaxf(mx, dppm<0x143, 0xc>(mx));
      mx = __int_as_float(__builtin_amdgcn_readlane(__float_as_int(mx), 63));
      const float dsk = (d & 1) ? bfhi(dw) : bflo(dw);
      const float sc = fmaxf(fmaxf(2.0f * dt * mx, fabsf(dsk) * (1.0f / 16384.0f)), 1e-30f);
      const float isc = 1.0f / sc;
      if (lane == 0) { slot[0] = sc; slot[1] = dsk * isc; }
      v.x *= 2.0f * dt * isc; v.y *= 2.0f * dt * isc;
      const c2 s = cexpp(a2, br, 8.f);
#pragma unroll
      for (int j = 0; j < 16; ++j) { put(j, v); if (j < 15) v = cmul(v, s); }
    } else {
      br = -br;
      const c2 h = cexpp(a2, br, 128.f);
      if (role == 1) {
        const c2 s = cexpp(a2, br, 1.f);
        put(16, (c2){{1.f, 1.f}, {0.f, 0.f}}); put(24, h);
        c2 v = s;
#pragma unroll
        for (int c = 1; c < 8; ++c) { put(16 + c, v); put(24 + c, cmul(v, h)); if (c < 7) v = cmul(v, s); }
      } else if (role == 2) {
        const c2 s = cexpp(a2, br, 2.f);
        c2 v = cmul(h, h);
        const c2 q = cmul(s, s), y = cmul(v, v), z = cmul(y, h);
        put(40, y); put(41, cmul(y, q)); put(42, z); put(43, cmul(z, q));
#pragma unroll
        for (int c = 0; c < 4; ++c) { put(32 + c, v); put(36 + c, cmul(v, h)); if (c < 3) v = cmul(v, s); }
      } else {
        const c2 q = cexpp(a2, br, 4.f);
        c2 v = cexpp(a2, br, 768.f);
        put(44, v); put(45, cmul(v, q)); v = cmul(v, h);
        put(46, v); put(47, cmul(v, q));
#pragma unroll
        for (int i = 0; i < 9; ++i) { v = cmul(v, h); put(48 + i, v); }
      }
    }
  ST(2);
  chsync(fl, w & 3, 1);
  char* tb = sm + KA_T + ch * T_PITCH;
  {
    const int wq = w & 3;
    const unsigned pb = lds_addr(rc) + col * KG_PITCH + q * 16;
    f4 acc = {0.f, 0.f, 0.f, 0.f}, ac0 = {0.f, 0.f, 0.f, 0.f};
    const float dadd = slot[1];
    if (wq == 3) tbl_tiles<true>(pb + 32 * KG_PITCH, pb, acc, ac0); else tbl_tiles<false>(pb + (wq < 2 ? 48 : 40) * KG_PITCH, pb, acc, ac0);
    const unsigned p0 = pk_h(acc[0], acc[1]), p1 = pk_h(acc[2], acc[3]);
    if (wq < 2) {
      u16* t = (u16*)tb + 512 + 64 * q + col;
      t[0] = (u16)p0; t[16] = (u16)(p0 >> 16); t[32] = (u16)p1; t[48] = (u16)(p1 >> 16);
    } else if (wq == 2) {
      if (q < 2) { *(unsigned*)(tb + 2 * (384 + 64 * q + 2 * col)) = p0; *(unsigned*)(tb + 2 * (416 + 64 * q + 2 * col)) = p1; }
      else if (lane == 32) ((u16*)tb)[512] = (u16)p0;
    } else {
      if (q < 2) *(u2*)(tb + 2 * (256 + 64 * q + 4 * col)) = (u2){p0, p1};
      else if (lane == 32) ((u16*)tb)[384] = (u16)p0;
      if (lane == 0) ac0[0] += dadd;
      *(u2*)(tb + 2 * (128 * (q >> 1) + 4 * (q & 1) + 8 * col)) = (u2){pk_h(ac0[0], ac0[1]), pk_h(ac0[2], ac0[3])};
    }
  }
  asm volatile("s_waitcnt lgkmcnt(0)" ::: "memory");
  ST(3);
  {
    const int t8 = tid & 255, a = 255 - t8, wq = w & 3;
    u4 o0, o1;
    if (wq < 2) {
      const u16* t = (const u16*)tb + 384 + a;
      const unsigned lo = t[0], hi = t[1];
      const unsigned hh = perm(hi, hi, 0x01000100u), ll = perm(lo, lo, 0x01000100u), tie = (a & 1) ? hi : lo;
      o0 = (u4){hh, perm(tie, hi, 0x05040100u), ll, ll}; o1 = (u4){hh, perm(lo, tie, 0x05040100u), ll, ll};
    } else if (wq == 2) {
      const unsigned* t = (const unsigned*)(tb + 2 * (256 + 2 * a));
      const unsigned v01 = t[0], v2 = t[1];
      const unsigned t11 = perm(v01, v01, 0x03020302u), t00 = perm(v01, v01, 0x01000100u);
      o0 = (u4){perm(v2, v2, 0x01000100u), t11, perm(v01, v01, 0x01000302u), t00}; o1 = (u4){perm(v01, v2, 0x07060100u), t11, t00, t00};
    } else if (a >= 32) {
      const unsigned* t = (const unsigned*)(tb + 2 * (128 + 4 * a));
      const unsigned vm = t[-1], v01 = t[0], v23 = t[1], v4 = t[2];
      const unsigned t22 = perm(v23, v23, 0x01000100u);
      o0 = (u4){perm(v23, v4, 0x07060100u), t22, perm(v01, v23, 0x07060100u), perm(v01, v01, 0x01000100u)};
      o1 = (u4){perm(v23, v23, 0x01000302u), t22, perm(v01, v01, 0x01000302u), perm(vm, v01, a == 32 ? 0x07060100u : 0x01000100u)};
    } else {
      const u4 v = *(const u4*)(tb + 16 * a);
      const unsigned vm = a ? *(const unsigned*)(tb + 16 * a - 4) : 0u;
      o0 = (u4){perm(v.w, v.w, 0x01000302u), perm(v.z, v.z, 0x01000302u), perm(v.y, v.y, 0x01000302u), perm(v.x, v.x, 0x01000302u)};
      o1 = (u4){perm(v.z, v.w, 0x07060100u), perm(v.y, v.z, 0x07060100u), perm(v.x, v.y, 0x07060100u), perm(vm, v.x, 0x07060100u)};
    }
    char* kcb = sm + KA_K + ch * 4 * KC_PITCH;
    char* kc = kcb + 16 * t8;
    *(u4*)kc = o0; *(u4*)(kc + KC_PITCH) = o1;
    unsigned* k2 = (unsigned*)(kc + 2 * KC_PITCH - 4);
    unsigned* k3 = (unsigned*)(kc + 3 * KC_PITCH - 4);
    k2[0] = o0.x; k2[1] = o0.y; k2[2] = o0.z; k2[3] = o0.w;
    k3[0] = o1.x; k3[1] = o1.y; k3[2] = o1.z; k3[3] = o1.w;
    if (wq == 0 && lane < 36) { const int s = lane / 9; *(unsigned*)(kcb + s * KC_PITCH + 4096 - (s >> 1) * 4 + (lane - 9 * s) * 4) = 0; }
  }
  ST(4);
  }
  ST(5);
  bar();
  ST(6);
  const int wq = w & 3;
  u2 xv[2];
  gld8s<0>(xv[0], (512 * wq + 16 * col + 4 * q) * 2, ws + OFF_XT + (size_t)d * 4096);
  gld8s<512>(xv[1], (512 * wq + 16 * col + 4 * q) * 2, ws + OFF_XT + (size_t)d * 4096);
  f4 acc[8];
#pragma unroll
  for (int n = 0; n < 8; ++n) acc[n] = (f4){0.f, 0.f, 0.f, 0.f};
  {
    const int s = (3 - col) & 3;
    const unsigned pa = lds_addr(sm) + KA_K + (4 * ch + s) * KC_PITCH + 2 * (2031 - s - col + 8 * q) - 64 * (2 * wq + 1) - 512 * 7;
    const unsigned pb = lds_addr(sm) + KA_U + ch * U_PITCH + 448 + 32 * col + 16 * q - 64 * (2 * wq + 1);
    u2 al[8], ah[8]; u4 bb[8];
    conv_ld<0, 0>(al, ah, bb, pa, pb);
    conv_mm<0>(al, ah, bb, acc);
    __builtin_amdgcn_sched_barrier(0);
    conv_ld<1, 0>(al, ah, bb, pa, pb);
    conv_mm<0>(al, ah, bb, acc);
  }
  ST(7);
  f4 tot[2];
  {
    f4* red = (f4*)(rc + wq * 6144) + lane;
#pragma unroll
    for (int v = 0; v < 4; ++v) {
      if (v == wq) { tot[0] = acc[2 * v]; tot[1] = acc[2 * v + 1]; }
      else { const int sl = v > wq ? v - 1 : v; red[sl * 128] = acc[2 * v]; red[sl * 128 + 64] = acc[2 * v + 1]; }
    }
  }
  chsync(fl, w & 3, 2);
  ST(8);
#pragma unroll
  for (int v = 0; v < 3; ++v) {
    const int src = v < wq ? v : v + 1;
    const f4* rp = (const f4*)(rc + src * 6144) + (wq > src ? wq - 1 : wq) * 128 + lane;
    tot[0] += rp[0]; tot[1] += rp[64];
  }
  const float sc = slot[0];
  asm volatile("s_waitcnt vmcnt(0)" : "+v"(xv[0]), "+v"(xv[1])::"memory");
#pragma unroll
  for (int nn = 0; nn < 2; ++nn) {
    const int t = 512 * wq + 256 * nn + 16 * col + 4 * q;
    const int pos = (t & ~31) + 16 * ((t >> 2) & 1) + 4 * ((t >> 3) & 3);
    const float h[4] = {__builtin_fmaf(tot[nn][0], sc, bflo(xv[nn].x)), __builtin_fmaf(tot[nn][1], sc, bfhi(xv[nn].x)),
                        __builtin_fmaf(tot[nn][2], sc, bflo(xv[nn].y)), __builtin_fmaf(tot[nn][3], sc, bfhi(xv[nn].y))};
    *(u2*)(ws + OFF_HT + (size_t)(pos >> 6) * 81920 + d * 128 + (pos & 63) * 2) = (u2){pk_bf(h[0], h[1]), pk_bf(h[2], h[3])};
  }
  ST_END(0);
}

DEVI void tile_of(int b, int& i, int& j) { const int g = b & 7, r = b >> 3; i = (g >> 1) * 8 + (r >> 2); j = (g & 1) * 4 + (r & 3); }
constexpr int KB_RED = 81920, KB_SLOT = 7168, KB_Z1 = KB_RED + 65536, KB_S2 = KB_Z1 + 14336, KB_S3 = KB_S2 + 512,
              KB_CNT = KB_S3 + 512, KB_LDS = KB_CNT + 16;
#define KSP 12
constexpr int KSPLIT = KSP;

#define STSET 0
#define STK(set, i) do { if constexpr (STSET == set) ST(i); } while (0)
template <bool Z> DEVI void mmv(f4& d, const u4& a, const u4& b) {
  if constexpr (Z) asm volatile("v_mfma_f32_16x16x32_bf16 %0, %1, %2, 0" : "=v"(d) : "v"(a), "v"(b));
  else asm volatile("v_mfma_f32_16x16x32_bf16 %0, %1, %2, %0" : "+v"(d) : "v"(a), "v"(b));
}
template <bool Z> DEVI void mmva(f4& d, const u4& a, const u4& b) {
  if constexpr (Z) asm volatile("v_mfma_f32_16x16x32_bf16 %0, %1, %2, 0" : "=v"(d) : "v"(a), "a"(b));
  else asm volatile("v_mfma_f32_16x16x32_bf16 %0, %1, %2, %0" : "+v"(d) : "v"(a), "a"(b));
}
template <bool Z> DEVI void mmav(f4& d, const u4& a, const u4& b) {
  if constexpr (Z) asm volatile("v_mfma_f32_16x16x32_bf16 %0, %1, %2, 0" : "=v"(d) : "a"(a), "v"(b));
  else asm volatile("v_mfma_f32_16x16x32_bf16 %0, %1, %2, %0" : "+v"(d) : "a"(a), "v"(b));
}
template <int IMM> DEVI void gld16a(u4& d, unsigned voff, const void* sb) { asm volatile("global_load_dwordx4 %0, %1, %2 offset:%3" : "=a"(d) : "v"(voff), "s"(sb), "i"(IMM)); }
template <int IMM> DEVI void rld16(u4& d, unsigned voff, const void* sb) { asm volatile("global_load_dwordx4 %0, %1, %2 offset:%3" : "+v"(d) : "v"(voff), "s"(sb), "i"(IMM)); }
template <int IMM> DEVI void rld16s(u4& d, unsigned voff, const void* sb) { asm volatile("global_load_dwordx4 %0, %1, %2 offset:%3 sc1" : "+v"(d) : "v"(voff), "s"(sb), "i"(IMM)); }
template <int IMM> DEVI void trd(u2& d, unsigned a) { asm volatile("ds_read_b64_tr_b16 %0, %1 offset:%2" : "=v"(d) : "v"(a), "i"(IMM)); }
template <int IMM> DEVI void ldsw16(unsigned a, const f4& v) { asm volatile("ds_write_b128 %0, %1 offset:%2" ::"v"(a), "v"(v), "i"(IMM) : "memory"); }
DEVI void dot2(float& acc, unsigned a, unsigned b) { asm volatile("v_dot2_f32_bf16 %0, %1, %2, %0" : "+v"(acc) : "v"(a), "v"(b)); }
DEVI void dot2s(float& acc, unsigned a, unsigned b) { asm volatile("v_dot2_f32_bf16 %0, %1, %2, %0" : "+v"(acc) : "v"(a), "s"(b)); }
DEVI void settle(f4& a, f4& b, f4& c, f4& d) { asm volatile("s_nop 7" : "+v"(a), "+v"(b), "+v"(c), "+v"(d)); }
DEVI void hold(f4& a, f4& b, f4& c, f4& d) { asm volatile("" : "+v"(a), "+v"(b), "+v"(c), "+v"(d)); }
DEVI u4 cat(const u2& a, const u2& b) { return (u4){a.x, a.y, b.x, b.y}; }
DEVI f2 gelu2(f2 x) {
  const f2 u = x * (x * x * -0.1029432397f + -2.3022081985f);
  const f2 d = (f2){__builtin_amdgcn_exp2f(u.x), __builtin_amdgcn_exp2f(u.y)} + 1.0f;
  return x * (f2){__builtin_amdgcn_rcpf(d.x), __builtin_amdgcn_rcpf(d.y)};
}
template <int CTRL, int H> DEVI void bfly(float (&v)[16], bool up) {
#pragma unroll
  for (int k = 0; k < H; ++k) { const float send = up ? v[k] : v[k + H], keep = up ? v[k + H] : v[k]; v[k] = keep + dpp<CTRL>(send); }
}
DEVI float rows_sum(float v) {
  auto a = __builtin_amdgcn_permlane32_swap(__float_as_uint(v), __float_as_uint(v), false, false);
  v = __uint_as_float(a[0]) + __uint_as_float(a[1]);
  auto b = __builtin_amdgcn_permlane16_swap(__float_as_uint(v), __float_as_uint(v), false, false);
  return __uint_as_float(b[0]) + __uint_as_float(b[1]);
}
DEVI int fresh_lane() {
  int l;
  asm volatile("v_mbcnt_lo_u32_b32 %0, -1, 0\n\tv_mbcnt_hi_u32_b32 %0, -1, %0" : "=v"(l));
  return l;
}
DEVI bool last_of_four(unsigned cnt_addr) {
  unsigned o = 0;
  if (fresh_lane() == 0) asm volatile("ds_add_rtn_u32 %0, %1, %2\n\ts_waitcnt lgkmcnt(0)" : "=v"(o) : "v"(cnt_addr), "v"(1u) : "memory");
  return __builtin_amdgcn_readfirstlane(o) == 3;
}
DEVI void poll8(unsigned voff, const void* sb) {
  unsigned t, n;
  asm volatile(
      "s_mov_b32 %1, 0x40000\n"
      "1:\n\tglobal_load_dword %0, %2, %3 sc1\n\ts_waitcnt vmcnt(0)\n\tv_cmp_ne_u32 vcc, 0, %0\n\ts_sub_u32 %1, %1, 1\n\ts_cbranch_scc1 2f\n\t"
      "s_cmp_eq_u64 vcc, -1\n\ts_cbranch_scc1 2f\n\ts_sleep 1\n\ts_branch 1b\n2:"
      : "=&v"(t), "=&s"(n) : "v"(voff), "s"(sb) : "vcc", "scc", "memory");
}

struct G1 {
  u2 al[2][4], ah[2][4];
  u4 ring[8][2], ones;
  unsigned touch[3], va[2], sb;
  const char* hsrc;
  f4 acc[2][4], sx, sq;
  unsigned pa[4], vb[2];
  const char *b2, *b1;
  u64* st_;
};
struct WS { unsigned vw[2], rd[2], ring; const char* src[4]; };
template <int C, int M> DEVI void w_dma(const WS& x) {
  asm volatile("s_mov_b32 m0, %0\n\ts_nop 0\n\tglobal_load_lds_dwordx4 %1, %3\n\tglobal_load_lds_dwordx4 %2, %3 offset:1024"
               ::"s"(x.ring + (C & 1) * 8192 + M * 2048), "v"(x.vw[0]), "v"(x.vw[1]), "s"(x.src[M] + C * 128) : "memory");
}
DEVI void w_dma_rt(const WS& x, int C, int M) {
  asm volatile("s_mov_b32 m0, %0\n\ts_nop 0\n\tglobal_load_lds_dwordx4 %1, %3\n\tglobal_load_lds_dwordx4 %2, %3 offset:1024"
               ::"s"(x.ring + (C & 1) * 8192 + M * 2048), "v"(x.vw[0]), "v"(x.vw[1]), "s"(x.src[M] + C * 128) : "memory");
}
template <int C, int M, int KH> DEVI void w_read(const WS& x, u4 (&W)[16][4]) {
  asm volatile("ds_read_b128 %0, %1 offset:%2" : "=a"(W[2 * C + KH][M]) : "v"(x.rd[KH]), "i"((C & 1) * 8192 + M * 2048));
}
template <int C> DEVI void w_dma_all(const WS& x) { w_dma<C, 0>(x); w_dma<C, 1>(x); w_dma<C, 2>(x); w_dma<C, 3>(x); }
template <int C> DEVI void w_read_all(const WS& x, u4 (&W)[16][4]) {
  w_read<C, 0, 0>(x, W); w_read<C, 0, 1>(x, W); w_read<C, 1, 0>(x, W); w_read<C, 1, 1>(x, W);
  w_read<C, 2, 0>(x, W); w_read<C, 2, 1>(x, W); w_read<C, 3, 0>(x, W); w_read<C, 3, 1>(x, W);
}
DEVI void w_setup(WS& x, int lane, int w, int i, unsigned sb, const u16* wsp) {
  const int c = lane & 15, g4 = lane >> 4;
  x.ring = sb + KB_RED + w * 16384;
#pragma unroll
  for (int o = 0; o < 2; ++o) {
    x.vw[o] = (unsigned)(16 * o + 8 * (lane >> 5) + ((lane >> 3) & 3)) * 4096 + (((lane & 7) ^ ((4 * o + g4) & 7)) * 16) - 1024 * o;
    x.rd[o] = x.ring + c * 128 + (((4 * o + g4) ^ ((c >> 1) & 7)) * 16);
  }
#pragma unroll
  for (int m = 0; m < 4; ++m) x.src[m] = (const char*)wsp + (size_t)(64 * i + 32 * ((m ^ w) >> 1) + 4 * ((m ^ w) & 1)) * 4096 + w * 1024;
}
template <int KS> DEVI const char* wf_base(const char* b) { return b + (KS >> 3) * 8192 + 4096; }
template <int KS, int X> DEVI void ld_new(G1& g, u4& d, const char* b) { gld16<(KS & 7) * 1024 - 4096>(d, g.vb[X], wf_base<KS>(b)); }
template <int KS, int X> DEVI void ld_ring(G1& g, u4& d, const char* b) { rld16<(KS & 7) * 1024 - 4096>(d, g.vb[X], wf_base<KS>(b)); }
template <int S, int M> DEVI void rd_lo(G1& g) { trd<(S & 15) * 4096>(g.al[S & 1][M], g.pa[M]); }
template <int S, int M> DEVI void rd_hi(G1& g) { trd<(S & 15) * 4096 + 512>(g.ah[S & 1][M], g.pa[M]); }
template <int N, int LG> DEVI void g1_wait(G1& g, int c, int m) {
  asm volatile("s_waitcnt vmcnt(%4) lgkmcnt(%5)" : "+v"(g.al[c][m]), "+v"(g.ah[c][m]), "+v"(g.al[c][m + 1]), "+v"(g.ah[c][m + 1]) : "i"(N), "i"(LG) : "memory");
}
template <int S> DEVI void pa_step(G1& g) {
  if constexpr (S == 16) { for (int m = 0; m < 4; ++m) g.pa[m] += 65536; }
  if constexpr (S == 20) { for (int m = 0; m < 4; ++m) g.pa[m] -= 65536; }
}
template <int KS, int H> DEVI void a_dma(const G1& g) {
  asm volatile("s_mov_b32 m0, %0\n\ts_nop 0\n\tglobal_load_lds_dwordx4 %1, %3\n\tglobal_load_lds_dwordx4 %2, %3 offset:1024"
               ::"s"(g.sb + KS * 4096 + H * 2048), "v"(g.va[0]), "v"(g.va[1]), "s"(g.hsrc + KS * 4096 + H * 2048) : "memory");
}
template <int G, int X> DEVI void b_next(G1& g) {
  if constexpr (G + 8 < 20) ld_ring<G + 8, X>(g, g.ring[G & 7][X], g.b2);
  else if constexpr (G + 8 < 40) ld_ring<G + 8 - 20, X>(g, g.ring[G & 7][X], g.b1);
}
constexpr bool w_rd_at(int s) { return (s >= 0 && s < 8) || (s >= 10 && s < 18); }
constexpr bool w_dma_at(int s) { return w_rd_at(s - 1); }
constexpr int w_tile(int s) { return s < 8 ? s : s - 2; }
constexpr int ops_at(int nb, int k) { return (k < 32 ? nb : 0) + (nb == 1 && k < 10 ? 4 : 0) + (nb == 1 && k == 20 ? 3 : 0) + (w_dma_at(k - 20) ? 2 : 0); }
constexpr int ops_in(int nb, int a, int b) { int n = 0; for (int k = a; k < b; ++k) n += ops_at(nb, k); return n; }
constexpr int imin(int a, int b) { return a < b ? a : b; }
constexpr int g1_vm(int nb, int G) {
  int n = G < 4 ? nb * (7 - G) + 6 + ops_in(nb, 0, G) : G < 8 ? nb * (7 - G) + ops_in(nb, 0, G) : ops_in(nb, G - 7, G);
  if (G == 3) n = imin(n, 4 * nb + ops_in(nb, 0, 3));
  if (nb == 1 && G == 9) n = imin(n, 1 + ops_in(1, 5, 9));
  if (nb == 1 && G == 14) n = imin(n, 1 + ops_in(1, 10, 14));
  if (G >= 20 && G < 28) n = 2 * (27 - G) + ops_in(nb, 20, G);
  if (G >= 30 && G < 38) n = imin(n, (G - 9 < 32 ? nb - 1 : 0) + ops_in(nb, G - 8, G));
  return imin(n, 63);
}
template <int NB, int S> DEVI void g1_first(G1& g) {
  if constexpr (S < 20) {
    constexpr int c = S & 1, nx = (S + 1) % 20;
    constexpr bool Z = S == 0;
    g1_wait<g1_vm(NB, S), 4>(g, c, 0);
    if constexpr (S == 3 || S == 9 || S == 14) asm volatile("s_barrier" ::: "memory");
    { u64* st_ = g.st_; if constexpr (S == 1) STK(1, 4); if constexpr (S == 5) STK(1, 5); if constexpr (S == 10) STK(1, 6); if constexpr (S == 15) STK(1, 7); if constexpr (S == 19) STK(1, 8); }
    pa_step<S + 1>(g);
    const u4& b0 = g.ring[S & 7][0]; const u4& b1 = g.ring[S & 7][1];
    const u4 a0 = cat(g.al[c][0], g.ah[c][0]), a1 = cat(g.al[c][1], g.ah[c][1]);
    mmv<Z>(g.acc[0][0], a0, b0); rd_lo<nx, 0>(g);
    if constexpr (NB == 2) mmv<Z>(g.acc[1][0], a0, b1);
    rd_hi<nx, 0>(g);
    mmv<Z>(g.sx, a0, g.ones);
    if constexpr (NB == 1 && S < 10) a_dma<S + 10, 0>(g);
    mmv<Z>(g.acc[0][1], a1, b0); rd_lo<nx, 1>(g);
    if constexpr (NB == 2) mmv<Z>(g.acc[1][1], a1, b1);
    rd_hi<nx, 1>(g);
    mmv<Z>(g.sq, a0, a0);
    if constexpr (NB == 1 && S < 10) a_dma<S + 10, 1>(g);
    g1_wait<63, 4>(g, c, 2);
    const u4 a2 = cat(g.al[c][2], g.ah[c][2]), a3 = cat(g.al[c][3], g.ah[c][3]);
    mmv<Z>(g.acc[0][2], a2, b0); rd_lo<nx, 2>(g);
    if constexpr (NB == 2) mmv<Z>(g.acc[1][2], a2, b1);
    rd_hi<nx, 2>(g);
    mmv<Z>(g.acc[0][3], a3, b0); rd_lo<nx, 3>(g);
    b_next<S, 0>(g);
    if constexpr (NB == 2) mmv<Z>(g.acc[1][3], a3, b1);
    rd_hi<nx, 3>(g);
    if constexpr (NB == 2) b_next<S, 1>(g);
    g1_first<NB, S + 1>(g);
  }
}
template <int NB, int S, int X, int M> DEVI void mm1(G1& g) {
  if constexpr (X < NB) mmv<S == 0>(g.acc[X][M], g.ring[(S + 20) & 7][X], cat(g.al[S & 1][M], g.ah[S & 1][M]));
}
template <int NB, int S, int E> DEVI void g1_second(G1& g, const WS& x, u4 (&W)[16][4], unsigned tv, const char* tb) {
  if constexpr (S < E) {
    constexpr bool R = S < 19, WR = w_rd_at(S), WP = w_rd_at(S - 1);
    constexpr int T = w_tile(S), TD = w_tile(S - 1) + 8;
    g1_wait<g1_vm(NB, S + 20), (WP ? 5 : 4)>(g, S & 1, 0);
    { u64* st_ = g.st_; if constexpr (S == 0) STK(2, 1); if constexpr (S == 5) STK(2, 2); if constexpr (S == 10) STK(2, 5); if constexpr (S == 15) STK(2, 6); if constexpr (S == 19) STK(2, 7); }
    if constexpr (R) pa_step<S + 1>(g);
    if constexpr (NB == 1 && S == 0) { gld4<0>(g.touch[0], tv, tb); gld4<0>(g.touch[1], tv, tb + 45 * 128); gld4<0>(g.touch[2], tv, tb + 90 * 128); }
    mm1<NB, S, 0, 0>(g); if constexpr (R) rd_lo<S + 1, 0>(g);
    mm1<NB, S, 1, 0>(g); if constexpr (R) rd_hi<S + 1, 0>(g);
    if constexpr (WR) w_read<(T >> 2), (T & 3), 0>(x, W);
    mm1<NB, S, 0, 1>(g); if constexpr (R) rd_lo<S + 1, 1>(g);
    mm1<NB, S, 1, 1>(g); if constexpr (R) rd_hi<S + 1, 1>(g);
    g1_wait<63, (R ? (WR ? 5 : 4) : 0)>(g, S & 1, 2);
    mm1<NB, S, 0, 2>(g); if constexpr (R) rd_lo<S + 1, 2>(g);
    mm1<NB, S, 1, 2>(g); if constexpr (R) rd_hi<S + 1, 2>(g);
    if constexpr (WR) w_read<(T >> 2), (T & 3), 1>(x, W);
    mm1<NB, S, 0, 3>(g); if constexpr (R) rd_lo<S + 1, 3>(g);
    b_next<S + 20, 0>(g);
    if constexpr (w_dma_at(S)) w_dma<(TD >> 2), (TD & 3)>(x);
    mm1<NB, S, 1, 3>(g); if constexpr (R) rd_hi<S + 1, 3>(g);
    if constexpr (NB == 2) b_next<S + 20, 1>(g);
    g1_second<NB, S + 1, E>(g, x, W, tv, tb);
  }
}
template <int NB, int KS, int E> DEVI void g1_pro(G1& g) {
  if constexpr (KS < E) { ld_new<KS, 0>(g, g.ring[KS][0], g.b2); if constexpr (NB == 2) ld_new<KS, 1>(g, g.ring[KS][1], g.b2); g1_pro<NB, KS + 1, E>(g); }
}
#define FOLD2(CT, MA, MB, H) \
  "v_add_f32_dpp %0, %0, %0 " CT " row_mask:0xf bank_mask:" MA "\n\tv_add_f32_dpp %1, %1, %1 " CT " row_mask:0xf bank_mask:" MA "\n\t" \
  "v_add_f32_dpp %2, %2, %2 " CT " row_mask:0xf bank_mask:" MA "\n\tv_add_f32_dpp %3, %3, %3 " CT " row_mask:0xf bank_mask:" MA "\n\t" \
  "v_add_f32_dpp %0, %4, %4 " CT " row_mask:0xf bank_mask:" MB "\n\tv_add_f32_dpp %1, %5, %5 " CT " row_mask:0xf bank_mask:" MB "\n\t" \
  "v_add_f32_dpp %2, %6, %6 " CT " row_mask:0xf bank_mask:" MB "\n\tv_add_f32_dpp %3, %7, %7 " CT " row_mask:0xf bank_mask:" MB
DEVI void fold16(float (&v)[16]) {
  asm volatile("s_nop 1\n\t" FOLD2("row_mirror", "0x3", "0xc", 8) : "+v"(v[0]), "+v"(v[1]), "+v"(v[2]), "+v"(v[3]) : "v"(v[8]), "v"(v[9]), "v"(v[10]), "v"(v[11]));
  asm volatile("s_nop 1\n\t" FOLD2("row_mirror", "0x3", "0xc", 8) : "+v"(v[4]), "+v"(v[5]), "+v"(v[6]), "+v"(v[7]) : "v"(v[12]), "v"(v[13]), "v"(v[14]), "v"(v[15]));
  asm volatile(FOLD2("row_half_mirror", "0x5", "0xa", 4) "\n\t"
               "v_add_f32_dpp %0, %0, %0 quad_perm:[2,3,0,1] row_mask:0xf bank_mask:0xf\n\tv_add_f32_dpp %1, %1, %1 quad_perm:[2,3,0,1] row_mask:0xf bank_mask:0xf\n\t"
               "v_add_f32_dpp %2, %2, %2 quad_perm:[2,3,0,1] row_mask:0xf bank_mask:0xf\n\tv_add_f32_dpp %3, %3, %3 quad_perm:[2,3,0,1] row_mask:0xf bank_mask:0xf\n\t"
               "v_add_f32_dpp %0, %0, %0 quad_perm:[1,0,3,2] row_mask:0xf bank_mask:0xf\n\tv_add_f32_dpp %1, %1, %1 quad_perm:[1,0,3,2] row_mask:0xf bank_mask:0xf\n\t"
               "v_add_f32_dpp %2, %2, %2 quad_perm:[1,0,3,2] row_mask:0xf bank_mask:0xf\n\tv_add_f32_dpp %3, %3, %3 quad_perm:[1,0,3,2] row_mask:0xf bank_mask:0xf"
               : "+v"(v[0]), "+v"(v[1]), "+v"(v[2]), "+v"(v[3]) : "v"(v[4]), "v"(v[5]), "v"(v[6]), "v"(v[7]));
}

struct G2 {
  u4 z[4][7];
  f4 acc[7][4];
  unsigned vz, vz6, slot[3];
  const char *zsrc, *wo, *wo6;
};
template <int S, int FB> DEVI void ld_z(G2& g) {
  const char* sb = g.zsrc + (size_t)S * (NGB * 1024);
  if constexpr (S < 4) gld16s<(FB - 3) * 1024>(g.z[S][FB], FB == 6 ? g.vz6 : g.vz, sb);
  else rld16s<(FB - 3) * 1024>(g.z[S & 3][FB], FB == 6 ? g.vz6 : g.vz, sb);
}
template <int S, int FB> DEVI void ld_z_all(G2& g) { if constexpr (S < 4) { ld_z<S, FB>(g); if constexpr (FB < 6) ld_z_all<S, FB + 1>(g); else ld_z_all<S + 1, 0>(g); } }
template <int N> DEVI void ld_wo(G2& g, u4 (&WO)[7][5]) {
  constexpr int KS = N / 5, DB = N % 5;
  if constexpr (KS < 6) gld16a<(KS - 3) * 1024>(WO[KS][DB], g.vz, g.wo + DB * (NKS3 * 1024));
  else gld16a<0>(WO[KS][DB], g.vz, g.wo6 == nullptr ? g.wo + DB * (NKS3 * 1024) + 3072 : g.wo6);
}
template <int S, int FB> DEVI void g2_main(G2& g, u4 (&W)[16][4], u4 (&WO)[7][5], u64* st_) {
  if constexpr (S < 14) {
    if constexpr (S == 12 && FB == 0) asm volatile("s_waitcnt lgkmcnt(0)" ::: "memory");
    waitv<27>();
    if constexpr (FB == 0) { if constexpr (S == 0) { STK(0, 5); STK(4, 1); } if constexpr (S == 2) STK(4, 2); if constexpr (S == 4) STK(4, 4); if constexpr (S == 6) STK(4, 5); if constexpr (S == 8) STK(4, 6); if constexpr (S == 10) STK(4, 7); if constexpr (S == 13) STK(4, 8); }
    mmva<S == 0>(g.acc[FB][0], g.z[S & 3][FB], W[S][0]); mmva<S == 0>(g.acc[FB][1], g.z[S & 3][FB], W[S][1]);
    mmva<S == 0>(g.acc[FB][2], g.z[S & 3][FB], W[S][2]); mmva<S == 0>(g.acc[FB][3], g.z[S & 3][FB], W[S][3]);
    if constexpr (S < 12) ld_z<S + 4, FB>(g); else ld_wo<7 * (S - 12) + FB>(g, WO);
    if constexpr (FB < 6) g2_main<S, FB + 1>(g, W, WO, st_); else g2_main<S + 1, 0>(g, W, WO, st_);
  }
}
template <int GI, int K> DEVI void g2_tail(G2& g, u4 (&W)[16][4], u4 (&WO)[7][5]) {
  if constexpr (GI < 4) {
    constexpr int m = (GI + 1) & 3, s = 14 + K / 7, fb = K % 7;
    if constexpr (GI == 0 && K == 0) waitv<21>();
    if constexpr (GI == 0 && K == 7) waitv<14>();
    mmva<false>(g.acc[fb][m], g.z[s & 3][fb], W[s][m]);
    if constexpr (GI > 0 && K >= 1 && K < 8) ldsw16<(K - 1) * 1024>(g.slot[GI - 1], g.acc[K - 1][GI]);
    if constexpr (K >= 8 && 14 + 6 * GI + K - 8 < 35) ld_wo<14 + 6 * GI + K - 8>(g, WO);
    if constexpr (K < 13) g2_tail<GI, K + 1>(g, W, WO); else g2_tail<GI + 1, 0>(g, W, WO);
  }
}
struct G3 { u4 v[4][7]; f4 acc[5][4]; };
constexpr int V_PRE = 14;
template <int N, int E> DEVI void ld_v(G3& g, unsigned vo, unsigned vo6, const char* const (&vb)[4]) {
  if constexpr (N < E && N < 28) { gld16s<(N % 7 - 3) * 1024>(g.v[N / 7][N % 7], N % 7 == 6 ? vo6 : vo, vb[(N / 7 + 1) & 3]); ld_v<N + 1, E>(g, vo, vo6, vb); }
}
template <int N> DEVI void g3_loop(G3& g, u4 (&WO)[7][5], const unsigned (&slot)[3], unsigned vo, unsigned vo6, const char* const (&vb)[4], u64* st_) {
  if constexpr (N < 28) {
    constexpr int GI = N / 7, s = N % 7, m = (GI + 1) & 3;
    waitv<imin(V_PRE + 2 * N, 28) - 1 - N>();
    if constexpr (N == 0) STK(0, 8);
    mmav<s == 0>(g.acc[0][m], WO[s][0], g.v[GI][s]); mmav<s == 0>(g.acc[1][m], WO[s][1], g.v[GI][s]);
    ld_v<V_PRE + 2 * N, V_PRE + 2 * N + 1>(g, vo, vo6, vb);
    mmav<s == 0>(g.acc[2][m], WO[s][2], g.v[GI][s]); mmav<s == 0>(g.acc[3][m], WO[s][3], g.v[GI][s]);
    ld_v<V_PRE + 2 * N + 1, V_PRE + 2 * N + 2>(g, vo, vo6, vb);
    mmav<s == 0>(g.acc[4][m], WO[s][4], g.v[GI][s]);
    if constexpr (GI > 0 && s >= 1 && s < 6) ldsw16<(s - 1) * 1024>(slot[GI - 1], g.acc[s - 1][GI]);
    g3_loop<N + 1>(g, WO, slot, vo, vo6, vb, st_);
  }
}

struct Late { f4 acc[4]; float r[4], n[4]; unsigned o[4], rd[2]; u4 cs; };
DEVI void z1_block(const Late& t, char* sm) {
  const f2 c0 = {__uint_as_float(t.cs.x), __uint_as_float(t.cs.y)}, c1 = {__uint_as_float(t.cs.z), __uint_as_float(t.cs.w)};
  u2 y[4];
#pragma unroll
  for (int m = 0; m < 4; ++m) {
    const f2 y0 = gelu2((f2){t.acc[m][0], t.acc[m][1]} * t.r[m] + c0 * t.n[m]), y1 = gelu2((f2){t.acc[m][2], t.acc[m][3]} * t.r[m] + c1 * t.n[m]);
    y[m] = (u2){pk_bf(y0.x, y0.y), pk_bf(y1.x, y1.y)};
  }
#pragma unroll
  for (int m = 0; m < 4; ++m) *(u2*)(sm + t.o[m]) = y[m];
}
template <int NB> DEVI void phase1(char* sm, char* ws, const int i, const int j, const int w, const int gs, const int cnt, u4 (&W)[16][4], Late& late, const u16* wsp, u64* st_) {
  const int lane = fresh_lane();
  const int c = lane & 15, g4 = lane >> 4;
  const bool liveA = 2 * w < cnt;
  const int ga = gs + (liveA ? 2 * w : cnt - 1), gb = ga + 1;
  const unsigned sb = lds_addr(sm);
  G1 g;
  g.st_ = st_;
  g.vb[0] = (unsigned)ga * (NKS1 * 1024) + lane * 16; g.vb[1] = g.vb[0] + NKS1 * 1024;
  g.b2 = ws + OFF_WF; g.b1 = ws + OFF_WF + (size_t)NGB * NKS1 * 1024;
  unsigned cs2[2]; u4 cs1[2];
  {
    const int r8 = lane >> 3, sg = (r8 >> 1) & 1;
    g.va[0] = r8 * 128 + (((lane & 7) ^ (2 * sg)) * 16); g.va[1] = r8 * 128 + (((lane & 7) ^ (2 * sg + 4)) * 16);
    g.sb = sb; g.hsrc = ws + OFF_HT + (size_t)i * 81920;
    const unsigned vo = w & 1 ? g.va[1] : g.va[0];
#pragma unroll
    for (int n = 0; n < 4; ++n) dma16(sb + (w + 4 * n) * 1024, vo, g.hsrc + (w + 4 * n) * 1024);
#pragma unroll
    for (int x = 0; x < NB; ++x) { gld4<0>(cs2[x], (FH + 16 * (ga + x) + c) * 4, ws + OFF_CS); gld16<0>(cs1[x], (16 * (ga + x) + 4 * g4) * 4, ws + OFF_CS); }
    g1_pro<NB, 0, 4>(g);
    STK(5, 1);
#pragma unroll
    for (int n = 4; n < 10; ++n) dma16(sb + (w + 4 * n) * 1024, vo, g.hsrc + (w + 4 * n) * 1024);
    g1_pro<NB, 4, 8>(g);
  }
  STK(5, 4);
  WS x;
  w_setup(x, lane, w, i, sb, wsp);
  {
    const int qq = c >> 2, p = c & 3, sh = (qq >> 1) | ((g4 & 1) << 1);
#pragma unroll
    for (int m = 0; m < 4; ++m) {
      g.pa[m] = sb + (8 * g4 + qq) * 128 + (p >> 1) * 16 + 8 * (p & 1) + (m ^ w ^ sh) * 32;
    }
  }
  g.ones = (u4){0x3f803f80u, 0x3f803f80u, 0x3f803f80u, 0x3f803f80u};
  unsigned aq[4], al[4], zo[2], z1o[4], po, so;
#pragma unroll
  for (int m = 0; m < 4; ++m) { aq[m] = KB_S2 + (m ^ w) * 64 + g4 * 16; al[m] = KB_S2 + (m ^ w) * 64 + c * 4; z1o[m] = KB_Z1 + ((m ^ w) * 7 + 2 * w) * 512 + lane * 8; }
  zo[0] = (unsigned)(w >> 1) * (NGB * 1024) + lane * 16; zo[1] = (unsigned)(1 ^ (w >> 1)) * (NGB * 1024) + lane * 16;
  po = KB_Z1 + (w * 64 + 16 * ((c >> 2) ^ w) + 4 * g4) * 4; so = KB_S2 + (16 * w + c) * 4;
  const char* zb = ws + OFF_Z2 + ((size_t)2 * i * NGB + ga) * 1024;
  u64* st = (u64*)(ws + OFF_ST) + (size_t)i * 512;
  asm volatile("" : "+v"(aq[0]), "+v"(aq[1]), "+v"(aq[2]), "+v"(aq[3]), "+v"(al[0]), "+v"(al[1]), "+v"(al[2]), "+v"(al[3]), "+v"(zo[0]), "+v"(zo[1]),
               "+v"(z1o[0]), "+v"(z1o[1]), "+v"(z1o[2]), "+v"(z1o[3]), "+v"(po), "+v"(so), "+v"(x.vw[0]), "+v"(x.vw[1]), "+v"(x.rd[0]), "+v"(x.rd[1]));
  asm volatile("" : "+s"(x.src[0]), "+s"(x.src[1]), "+s"(x.src[2]), "+s"(x.src[3]), "+s"(x.ring), "+s"(zb), "+s"(st));
  if (w == 0 && lane < 4) *(unsigned*)(sm + KB_CNT + 4 * lane) = 0;
  if constexpr (NB == 2) asm volatile("s_waitcnt vmcnt(22)" : "+v"(cs2[0]), "+v"(cs2[1]), "+v"(cs1[0]), "+v"(cs1[1]), "+v"(g.ones)::"memory");
  else asm volatile("s_waitcnt vmcnt(14)" : "+v"(cs2[0]), "+v"(cs1[0]), "+v"(g.ones)::"memory");
  STK(5, 5);
  bar();
  STK(0, 1); STK(1, 1); STK(5, 6);
  rd_lo<0, 0>(g); rd_hi<0, 0>(g); rd_lo<0, 1>(g); rd_hi<0, 1>(g); rd_lo<0, 2>(g); rd_hi<0, 2>(g); rd_lo<0, 3>(g); rd_hi<0, 3>(g);
  g1_first<NB, 0>(g);
  asm volatile("s_nop 1" : "+v"(g.sx), "+v"(g.sq));
  {
    const float sx = c & 2 ? (c & 1 ? g.sx[3] : g.sx[2]) : (c & 1 ? g.sx[1] : g.sx[0]);
    const float sq = c & 2 ? (c & 1 ? g.sq[3] : g.sq[2]) : (c & 1 ? g.sq[1] : g.sq[0]);
    const f2 st = ln_stats(sx, sq, 1.0f / D);
    if ((c >> 2) == g4) { *(float*)(sm + so) = st.y; *(float*)(sm + so + 256) = -st.x * st.y; }
  }
  bar();
  hold(g.acc[0][0], g.acc[0][1], g.acc[0][2], g.acc[0][3]);
  if constexpr (NB == 2) hold(g.acc[1][0], g.acc[1][1], g.acc[1][2], g.acc[1][3]);
  STK(0, 2); STK(1, 2);
  f2 z[2][8];
  float r1[4], n1[4];
  float* ps = (float*)(sm + KB_Z1); float* pq = ps + 256;
  {
    float sv[16], qv[16];
    f4 r4[4], n4[4];
#pragma unroll
    for (int m = 0; m < 4; ++m) { r4[m] = *(const f4*)(sm + aq[m]); n4[m] = *(const f4*)(sm + aq[m] + 256); }
#pragma unroll
    for (int m = 0; m < 4; ++m) { r1[m] = *(const float*)(sm + al[m]); n1[m] = *(const float*)(sm + al[m] + 256); }
#pragma unroll
    for (int m = 0; m < 4; ++m) {
#pragma unroll
      for (int e = 0; e < 2; ++e) {
        const f2 r = {r4[m][2 * e], r4[m][2 * e + 1]}, n = {n4[m][2 * e], n4[m][2 * e + 1]};
#pragma unroll
        for (int y = 0; y < NB; ++y) z[y][2 * m + e] = gelu2((f2){g.acc[y][m][2 * e], g.acc[y][m][2 * e + 1]} * r + n * __uint_as_float(cs2[y]));
        f2 s2 = z[0][2 * m + e], q2 = s2 * s2;
        if constexpr (NB == 2) { s2 += z[1][2 * m + e]; q2 += z[1][2 * m + e] * z[1][2 * m + e]; }
        sv[4 * m + 2 * e] = s2.x; sv[4 * m + 2 * e + 1] = s2.y; qv[4 * m + 2 * e] = q2.x; qv[4 * m + 2 * e + 1] = q2.y;
        __builtin_amdgcn_sched_barrier(0);
        w_dma_rt(x, (2 * m + e) >> 2, (2 * m + e) & 3);
        __builtin_amdgcn_sched_barrier(0);
      }
    }
    fold16(sv); fold16(qv);
    if ((c & 3) == 0) {
      *(f4*)(sm + po) = liveA ? (f4){sv[0], sv[1], sv[2], sv[3]} : (f4){0.f, 0.f, 0.f, 0.f};
      *(f4*)(sm + po + 1024) = liveA ? (f4){qv[0], qv[1], qv[2], qv[3]} : (f4){0.f, 0.f, 0.f, 0.f};
    }
    if (last_of_four(sb + KB_CNT)) {
      const float ts = (ps[lane] + ps[64 + lane]) + (ps[128 + lane] + ps[192 + lane]), tq = (pq[lane] + pq[64 + lane]) + (pq[128 + lane] + pq[192 + lane]);
      __hip_atomic_store(st + j * 64 + lane, ((u64)(__float_as_uint(tq) | 0x80000000u) << 32) | __float_as_uint(ts), __ATOMIC_RELAXED, __HIP_MEMORY_SCOPE_AGENT);
    }
  }
  asm volatile("" : "+v"(r1[0]), "+v"(r1[1]), "+v"(r1[2]), "+v"(r1[3]), "+v"(n1[0]), "+v"(n1[1]), "+v"(n1[2]), "+v"(n1[3]));
  ST(3);
  const unsigned tv = (lane < 45 ? lane : 44) * 128;
  const char* tb = ws + OFF_WO + (size_t)5 * j * NKS3 * 1024 + (i & 7) * 135 * 128;
  g1_second<NB, 0, KSPLIT>(g, x, W, tv, tb);
  if constexpr (NB == 1) asm volatile("" : "+v"(g.touch[0]), "+v"(g.touch[1]), "+v"(g.touch[2]));
  float* rs3 = (float*)(sm + KB_S3); float* nm3 = rs3 + 64;
  STK(2, 8);
  if constexpr (NB == 1) {
    u2 gv[8]; unsigned n;
    asm volatile(
        "s_mov_b32 %8, 0x40000\n"
        "1:\n\tglobal_load_dwordx2 %0, %9, %10 sc1\n\tglobal_load_dwordx2 %1, %9, %10 offset:512 sc1\n\tglobal_load_dwordx2 %2, %9, %10 offset:1024 sc1\n\t"
        "global_load_dwordx2 %3, %9, %10 offset:1536 sc1\n\tglobal_load_dwordx2 %4, %9, %10 offset:2048 sc1\n\tglobal_load_dwordx2 %5, %9, %10 offset:2560 sc1\n\t"
        "global_load_dwordx2 %6, %9, %10 offset:3072 sc1\n\tglobal_load_dwordx2 %7, %9, %10 offset:3584 sc1\n\ts_waitcnt vmcnt(0)\n\t"
        "v_cmp_gt_i64 vcc, 0, %0\n\tv_cmp_gt_i64 s[44:45], 0, %1\n\tv_cmp_gt_i64 s[46:47], 0, %2\n\tv_cmp_gt_i64 s[48:49], 0, %3\n\t"
        "s_and_b64 vcc, vcc, s[44:45]\n\ts_and_b64 s[46:47], s[46:47], s[48:49]\n\ts_and_b64 s[50:51], vcc, s[46:47]\n\t"
        "v_cmp_gt_i64 vcc, 0, %4\n\tv_cmp_gt_i64 s[44:45], 0, %5\n\tv_cmp_gt_i64 s[46:47], 0, %6\n\tv_cmp_gt_i64 s[48:49], 0, %7\n\t"
        "s_and_b64 vcc, vcc, s[44:45]\n\ts_and_b64 s[46:47], s[46:47], s[48:49]\n\ts_and_b64 vcc, vcc, s[46:47]\n\ts_and_b64 vcc, vcc, s[50:51]\n\t"
        "s_sub_u32 %8, %8, 1\n\ts_cbranch_scc1 2f\n\ts_cmp_eq_u64 vcc, -1\n\ts_cbranch_scc1 2f\n\ts_sleep 1\n\ts_branch 1b\n2:"
        : "=&v"(gv[0]), "=&v"(gv[1]), "=&v"(gv[2]), "=&v"(gv[3]), "=&v"(gv[4]), "=&v"(gv[5]), "=&v"(gv[6]), "=&v"(gv[7]), "=&s"(n)
        : "v"(lane * 8), "s"(st)
        : "vcc", "scc", "s44", "s45", "s46", "s47", "s48", "s49", "s50", "s51", "memory");
    float ts = 0.f, tq = 0.f;
#pragma unroll
    for (int v = 0; v < 8; ++v) { ts += __uint_as_float(gv[v].x); tq += __uint_as_float(gv[v].y & 0x7fffffffu); }
    const f2 s3 = ln_stats(ts, tq, 1.0f / FH);
    rs3[lane] = s3.y; nm3[lane] = -s3.x * s3.y;
  }
  bar();
  STK(0, 4); STK(2, 4); STK(3, 4);
  if (liveA) {
    unsigned o[2][8];
#pragma unroll
    for (int m = 0; m < 4; ++m) {
      const f4 r4 = *(const f4*)(sm + aq[m] + 512), n4 = *(const f4*)(sm + aq[m] + 768);
#pragma unroll
      for (int e = 0; e < 2; ++e) {
        const f2 r = {r4[2 * e], r4[2 * e + 1]}, n = {n4[2 * e], n4[2 * e + 1]};
#pragma unroll
        for (int x = 0; x < NB; ++x) { const f2 a = z[x][2 * m + e] * r + n; o[x][2 * m + e] = pk_bf(a.x, a.y); }
      }
    }
    const unsigned r0 = zo[0], r1 = zo[1];
    if (w & 1) {
#pragma unroll
      for (int x = 0; x < NB; ++x) { gst16s(r0, (u4){o[x][2], o[x][3], o[x][0], o[x][1]}, zb + x * 1024); gst16s(r1, (u4){o[x][6], o[x][7], o[x][4], o[x][5]}, zb + x * 1024); }
    } else {
#pragma unroll
      for (int x = 0; x < NB; ++x) { gst16s(r0, (u4){o[x][0], o[x][1], o[x][2], o[x][3]}, zb + x * 1024); gst16s(r1, (u4){o[x][4], o[x][5], o[x][6], o[x][7]}, zb + x * 1024); }
    }
  }
  __builtin_amdgcn_sched_barrier(0);
  STK(3, 1);
  g1_second<NB, KSPLIT, 20>(g, x, W, tv, tb);
  STK(3, 2);
  waitv<ops_in(1, KSPLIT + 20, 40)>();
  if (last_of_four(sb + KB_CNT + 4)) {
    if (lane == 0) __hip_atomic_store((unsigned*)(ws + OFF_F2) + (32 * j + i) * 32, 1u, __ATOMIC_RELAXED, __HIP_MEMORY_SCOPE_AGENT);
  }
  STK(3, 5);
  settle(g.acc[0][0], g.acc[0][1], g.acc[0][2], g.acc[0][3]);
#pragma unroll
  for (int m = 0; m < 4; ++m) { late.acc[m] = g.acc[0][m]; late.r[m] = r1[m]; late.n[m] = n1[m]; late.o[m] = z1o[m]; }
  late.cs = cs1[0];
  if constexpr (NB == 2) {
    z1_block(late, sm);
    hold(g.acc[1][0], g.acc[1][1], g.acc[1][2], g.acc[1][3]);
#pragma unroll
    for (int m = 0; m < 4; ++m) { late.acc[m] = g.acc[1][m]; late.o[m] += 512; }
    late.cs = cs1[1];
  }
  __builtin_amdgcn_sched_barrier(0);
  STK(3, 6);
  waitv<0>();
  w_read_all<4>(x, W); w_read_all<5>(x, W);
  asm volatile("s_waitcnt lgkmcnt(8)" ::: "memory");
  w_dma_all<6>(x);
  asm volatile("s_waitcnt lgkmcnt(0)" ::: "memory");
  w_dma_all<7>(x);
  late.rd[0] = x.rd[0]; late.rd[1] = x.rd[1];
  STK(3, 7);
  poll8((lane & 7) * 128, ws + OFF_F2 + (32 * j + 8 * w) * 128);
  STK(3, 8);
}

__global__ __launch_bounds__(256, 1) void kb(const u16* __restrict__ wsp, const u16* __restrict__ bsp, char* __restrict__ ws, u16* __restrict__ out) {
  __shared__ __attribute__((aligned(16))) char sm[KB_LDS];
  const int w = __builtin_amdgcn_readfirstlane(threadIdx.x >> 6);
  int i, j; tile_of(blockIdx.x, i, j);
  const int gs = j < 7 ? 7 * j : 48, cnt = j < 6 ? 7 : 6;
  const unsigned sb = lds_addr(sm);
  const int hz = j < 4 ? 40960 : 0;
  ST_DECL
  u4 W[16][4];
  Late late;
  if (w < 3) phase1<2>(sm, ws, i, j, w, gs, cnt, W, late, wsp, st_); else phase1<1>(sm, ws, i, j, w, gs, cnt, W, late, wsp, st_);
  G2 h;
  unsigned bias;
  {
    const int ln = fresh_lane(), c = ln & 15;
    h.vz = ln * 16 + 3072; h.vz6 = cnt == 7 ? h.vz : h.vz - 1024;
    gld2(bias, (64 * i + 32 * (w >> 1) + 8 * (c >> 2) + 4 * (w & 1) + (c & 3)) * 2, bsp);
  }
  h.zsrc = ws + OFF_Z2 + ((size_t)16 * w * NGB + gs) * 1024;
  h.wo = ws + OFF_WO + ((size_t)5 * j * NKS3 + 7 * w) * 1024;
  h.wo6 = w == 3 ? ws + OFF_ZE - 3072 : nullptr;
  ld_z_all<0, 0>(h);
  {
    WS x;
    x.rd[0] = late.rd[0]; x.rd[1] = late.rd[1];
    w_read_all<6>(x, W); w_read_all<7>(x, W);
  }
  __builtin_amdgcn_sched_barrier(0);
  z1_block(late, sm);
  __builtin_amdgcn_sched_barrier(0);
  u4 WO[7][5];
  g2_main<0, 0>(h, W, WO, st_);
  const int l2 = fresh_lane();
  const char* rd[3];
#pragma unroll
  for (int m = 1; m < 4; ++m) {
    h.slot[m - 1] = sb + (m < 3 ? KB_RED + w * 16384 + (m - 1) * KB_SLOT : hz + w * KB_SLOT) + l2 * 16;
    rd[m - 1] = sm + (m < 3 ? KB_RED + (w ^ m) * 16384 + (m - 1) * KB_SLOT : hz + (w ^ m) * KB_SLOT) + l2 * 16;
  }
  bar();
  g2_tail<0, 0>(h, W, WO);
  asm volatile("s_nop 7" : "+v"(h.acc[0][0]), "+v"(h.acc[1][0]), "+v"(h.acc[2][0]), "+v"(h.acc[3][0]), "+v"(h.acc[4][0]), "+v"(h.acc[5][0]), "+v"(h.acc[6][0]));
  bar();
  STK(0, 6);
  {
    pin(bias);
    const float bb = bflo(bias);
    const unsigned vl = l2 * 16;
    u2 vv[7];
#pragma unroll
    for (int fb = 0; fb < 7; ++fb) {
      const f4 sum = ((h.acc[fb][0] + *(const f4*)(rd[0] + fb * 1024)) + *(const f4*)(rd[1] + fb * 1024)) + *(const f4*)(rd[2] + fb * 1024);
      const u2 z1 = *(const u2*)(sm + KB_Z1 + (w * 7 + fb) * 512 + l2 * 8);
      const unsigned s0 = pk_bf(sum[0], sum[1]), s1 = pk_bf(sum[2], sum[3]);
      const unsigned g0 = pk_bf(bflo(s0) + bb, bfhi(s0) + bb), g1 = pk_bf(bflo(s1) + bb, bfhi(s1) + bb);
      vv[fb] = (u2){pk_bf(bflo(g0) * bflo(z1.x), bfhi(g0) * bfhi(z1.x)), pk_bf(bflo(g1) * bflo(z1.y), bfhi(g1) * bfhi(z1.y))};
    }
    const char* vb = ws + OFF_V + ((size_t)(4 * i + w) * NKS3 + (gs >> 1)) * 1024;
    if (gs & 1) {
      asm volatile("global_store_dwordx2 %0, %1, %2 offset:8 sc1" ::"v"(vl), "v"(vv[0]), "s"(vb) : "memory");
#pragma unroll
      for (int p = 0; p < 3; ++p) gst16s(vl + 1024 * (p + 1), cat(vv[2 * p + 1], vv[2 * p + 2]), vb);
    } else {
#pragma unroll
      for (int p = 0; p < 3; ++p) gst16s(vl + 1024 * p, cat(vv[2 * p], vv[2 * p + 1]), vb);
      if (cnt == 7) asm volatile("global_store_dwordx2 %0, %1, %2 offset:3072 sc1" ::"v"(vl), "v"(vv[6]), "s"(vb) : "memory");
    }
  }
  waitv<0>();
  if (last_of_four(sb + KB_CNT + 8)) {
    if (l2 == 0) __hip_atomic_store((unsigned*)(ws + OFF_F3) + (8 * i + j) * 32, 1u, __ATOMIC_RELAXED, __HIP_MEMORY_SCOPE_AGENT);
  }
  STK(0, 7);
  G3 o;
  const char* vb[4];
#pragma unroll
  for (int m = 0; m < 4; ++m) vb[m] = ws + OFF_V + ((size_t)(4 * i + (m ^ w)) * NKS3 + 7 * w) * 1024;
  poll8((l2 & 7) * 128, ws + OFF_F3 + i * 1024);
  const unsigned vz6 = w == 3 ? h.vz - 1024 : h.vz;
  ld_v<0, V_PRE>(o, h.vz, vz6, vb);
  g3_loop<0>(o, WO, h.slot, h.vz, vz6, vb, st_);
  asm volatile("s_nop 7" : "+v"(o.acc[0][0]), "+v"(o.acc[1][0]), "+v"(o.acc[2][0]), "+v"(o.acc[3][0]), "+v"(o.acc[4][0]));
  bar();
  const int c3 = l2 & 15, g3 = l2 >> 4;
  const int trow3 = 64 * i + 32 * (w >> 1) + 8 * (c3 >> 2) + 4 * (w & 1) + (c3 & 3);
  const char* hp = sm + (80 * j + 4 * g3 + (c3 >> 2)) * 128 + (((2 * w + ((c3 >> 1) & 1)) ^ (2 * (((c3 >> 3) & 1) | ((g3 >> 1) << 1)))) * 16) + 8 * (c3 & 1);
#pragma unroll
  for (int db = 0; db < 5; ++db) {
    const f4 sum = ((o.acc[db][0] + *(const f4*)(rd[0] + db * 1024)) + *(const f4*)(rd[1] + db * 1024)) + *(const f4*)(rd[2] + db * 1024);
    const u2 hv = __builtin_bit_cast(u2, __builtin_amdgcn_ds_read_tr16_b64_v4i16((LDSP s4*)(hp + db * 2048)));
    const unsigned s0 = pk_bf(sum[0], sum[1]), s1 = pk_bf(sum[2], sum[3]);
    *(u2*)(out + (size_t)trow3 * D + 80 * j + 16 * db + 4 * g3) =
        (u2){pk_bf(bflo(hv.x) + bflo(s0), bfhi(hv.x) + bfhi(s0)), pk_bf(bflo(hv.y) + bflo(s1), bfhi(hv.y) + bfhi(s1))};
  }
  ST_END(1);
}

extern "C" void kernel_launch(void* const* d_in, const int* in_sizes, int n_in, void* d_out, int out_size, void* d_ws, size_t ws_size,
                              hipStream_t stream) {
  char* ws = (char*)d_ws;
  ka<<<256, 1024, 0, stream>>>((const u16*)d_in[0], (const u16*)d_in[1], (const u16*)d_in[2], (const u16*)d_in[3], (const u16*)d_in[4],
                              (const unsigned*)d_in[5], (const unsigned*)d_in[6], (const u16*)d_in[8], (const u16*)d_in[9], (const u16*)d_in[11], ws);
  kb<<<256, 256, 0, stream>>>((const u16*)d_in[9], (const u16*)d_in[10], ws, (u16*)d_out);
}
```

```cpp
#include <hip/hip_runtime.h>

typedef unsigned short u16;
typedef unsigned long long u64;
typedef __attribute__((ext_vector_type(8))) __bf16 bf8;
typedef __attribute__((ext_vector_type(2))) __bf16 bf2;
typedef __attribute__((ext_vector_type(8))) _Float16 h8;
typedef __attribute__((ext_vector_type(2))) _Float16 h2;
typedef __attribute__((ext_vector_type(4))) float f4;
typedef __attribute__((ext_vector_type(2))) float f2;
typedef __attribute__((ext_vector_type(4))) short s4;
typedef __attribute__((ext_vector_type(4))) unsigned u4;
typedef __attribute__((ext_vector_type(2))) unsigned u2;
#define LDSP __attribute__((address_space(3)))
#define DEVI __device__ __forceinline__
#define ST_DECL u64* st_ = nullptr;
#define ST(i)
#define ST_END(kid)

constexpr int L = 2048, D = 640, NM = 128, F = 1728, FH = 864;
constexpr int NGB = 54;
constexpr int NKS1 = 20;
constexpr int NKS3 = 27;

constexpr size_t OFF_UT = 0;
constexpr size_t OFF_XT = OFF_UT + (size_t)D * L * 2;
constexpr size_t OFF_KR = OFF_XT + (size_t)D * L * 2;
constexpr size_t OFF_HT = OFF_KR + (size_t)D * L * 4;
constexpr size_t OFF_WF = OFF_HT + (size_t)D * L * 2;
constexpr size_t OFF_WO = OFF_WF + (size_t)2 * NGB * NKS1 * 1024;
constexpr size_t OFF_Z1 = OFF_WO + (size_t)40 * NKS3 * 1024;
constexpr size_t OFF_Z2 = OFF_Z1 + (size_t)128 * NGB * 512;
constexpr size_t OFF_V = OFF_Z2 + (size_t)64 * NGB * 1024;
constexpr size_t OFF_ST = OFF_V + (size_t)128 * NKS3 * 1024;
constexpr size_t OFF_CSP = OFF_ST + (size_t)32 * 8 * 64 * 8;
constexpr size_t OFF_CS = OFF_CSP + (size_t)5 * F * 4;
constexpr size_t OFF_KSC = OFF_CS + (size_t)F * 4;
constexpr size_t OFF_G1 = 32u << 20;
constexpr size_t OFF_F1 = OFF_G1 + 32768;
constexpr size_t OFF_EP = OFF_G1 + 65536;
constexpr size_t OFF_F2 = OFF_G1 + 131072;
constexpr size_t OFF_F3 = OFF_F2 + 32768;
constexpr size_t OFF_ZE = OFF_F3 + 32768;

DEVI float bflo(unsigned v) { return __uint_as_float(v << 16); }
DEVI float bfhi(unsigned v) { return __uint_as_float(v & 0xffff0000u); }
DEVI unsigned pk_bf(float a, float b) { f2 p = {a, b}; return __builtin_bit_cast(unsigned, __builtin_convertvector(p, bf2)); }
DEVI unsigned pk_h(float a, float b) { f2 p = {a, b}; return __builtin_bit_cast(unsigned, __builtin_convertvector(p, h2)); }
DEVI float rbf(float a) { return bflo(pk_bf(a, 0.f)); }
DEVI float hlo(unsigned v) { return (float)__builtin_bit_cast(h2, v).x; }
DEVI float hhi(unsigned v) { return (float)__builtin_bit_cast(h2, v).y; }
DEVI f4 mfma_bf(u4 a, u4 b, f4 c) { return __builtin_amdgcn_mfma_f32_16x16x32_bf16(__builtin_bit_cast(bf8, a), __builtin_bit_cast(bf8, b), c, 0, 0, 0); }
DEVI f4 mfma_h(u4 a, u4 b, f4 c) { return __builtin_amdgcn_mfma_f32_16x16x32_f16(__builtin_bit_cast(h8, a), __builtin_bit_cast(h8, b), c, 0, 0, 0); }
template <int CTRL> DEVI float dpp(float v) { return __int_as_float(__builtin_amdgcn_update_dpp(0, __float_as_int(v), CTRL, 0xf, 0xf, true)); }
constexpr int DPP_ROR1 = 0x121, DPP_ROR2 = 0x122, DPP_ROR4 = 0x124, DPP_ROR8 = 0x128, DPP_MIRROR = 0x140, DPP_HMIRROR = 0x141, DPP_Q3210 = 0x1B, DPP_Q1032 = 0xB1;

DEVI f2 ln_stats(float s, float q, float inv_n) {
  float m = s * inv_n;
  float v = fmaxf(q * inv_n - m * m, 0.f);
  float sd = rbf(__builtin_amdgcn_sqrtf(rbf(rbf(v) + 1e-5f)));
  f2 r = {rbf(m), __builtin_amdgcn_rcpf(sd)};
  return r;
}
DEVI float gelu_t(float x) {
  float t = __builtin_fmaf(x * x, -0.1029432397f, -2.3022081985f);
  float e = __builtin_amdgcn_exp2f(x * t);
  return x * __builtin_amdgcn_rcpf(1.0f + e);
}

template <int IMM> DEVI void gld16(u4& d, unsigned voff, const void* sb) { asm volatile("global_load_dwordx4 %0, %1, %2 offset:%3" : "=v"(d) : "v"(voff), "s"(sb), "i"(IMM)); }
template <int IMM> DEVI void gld8(u2& d, unsigned voff, const void* sb) { asm volatile("global_load_dwordx2 %0, %1, %2 offset:%3" : "=v"(d) : "v"(voff), "s"(sb), "i"(IMM)); }
template <int IMM> DEVI void gld4(unsigned& d, unsigned voff, const void* sb) { asm volatile("global_load_dword %0, %1, %2 offset:%3" : "=v"(d) : "v"(voff), "s"(sb), "i"(IMM)); }
DEVI void gld2(unsigned& d, unsigned voff, const void* sb) { asm volatile("global_load_ushort %0, %1, %2" : "=v"(d) : "v"(voff), "s"(sb)); }
DEVI void dma16(unsigned ldsdst, unsigned voff, const void* sb) {
  asm volatile("s_mov_b32 m0, %0\n\ts_nop 0\n\tglobal_load_lds_dwordx4 %1, %2" ::"s"(ldsdst), "v"(voff), "s"(sb) : "memory");
}
template <int N> DEVI void waitv() { asm volatile("s_waitcnt vmcnt(%0)" ::"i"(N) : "memory"); }
template <int N> DEVI void waitv(u4& a) { asm volatile("s_waitcnt vmcnt(%1)" : "+v"(a) : "i"(N) : "memory"); }
DEVI void pin(u4& a) { asm volatile("" : "+v"(a)); }
DEVI void pin(u2& a) { asm volatile("" : "+v"(a)); }
DEVI void pin(unsigned& a) { asm volatile("" : "+v"(a)); }
DEVI void bar() { asm volatile("s_waitcnt lgkmcnt(0)\n\ts_barrier" ::: "memory"); }
DEVI unsigned lds_addr(const void* p) { return (unsigned)(size_t)(LDSP const char*)p; }

DEVI void tr8x8(const u4 (&r)[8], u4 (&o)[8]) {
#pragma unroll
  for (int c2 = 0; c2 < 4; ++c2)
#pragma unroll
    for (int m = 0; m < 4; ++m) {
      o[2 * c2][m] = __builtin_amdgcn_perm(r[2 * m + 1][c2], r[2 * m][c2], 0x05040100u);
      o[2 * c2 + 1][m] = __builtin_amdgcn_perm(r[2 * m + 1][c2], r[2 * m][c2], 0x07060302u);
    }
}

constexpr int KG_PITCH = 528;
constexpr int KC_PITCH = 4160;
constexpr int RC_SZ = 30208;
constexpr int KA_K = 3 * RC_SZ, KA_U = KA_K + 12 * KC_PITCH, U_PITCH = 4608, KA_T = KA_U + 3 * U_PITCH, T_PITCH = 1536;
constexpr int KA_S = KA_T + 3 * T_PITCH, KA_LDS = KA_S + 96;

struct c2 { f2 x, y; };
DEVI c2 cmul(c2 a, c2 b) { c2 r; r.x = a.x * b.x - a.y * b.y; r.y = a.x * b.y + a.y * b.x; return r; }
DEVI c2 cexpp(f2 a2, f2 br, float p) {
  const f2 ea = a2 * p, ph = br * p;
  const f2 e = {__builtin_amdgcn_exp2f(ea.x), __builtin_amdgcn_exp2f(ea.y)};
  const float p0 = __builtin_amdgcn_fractf(ph.x), p1 = __builtin_amdgcn_fractf(ph.y);
  c2 r;
  r.x = e * (f2){__builtin_amdgcn_cosf(p0), __builtin_amdgcn_cosf(p1)};
  r.y = e * (f2){__builtin_amdgcn_sinf(p0), __builtin_amdgcn_sinf(p1)};
  return r;
}
DEVI unsigned pk_h2(f2 p) { return __builtin_bit_cast(unsigned, __builtin_convertvector(p, h2)); }
template <int CTRL, int RM> DEVI float dppm(float v) { return __int_as_float(__builtin_amdgcn_update_dpp(0, __float_as_int(v), CTRL, RM, 0xf, false)); }
DEVI float wave_sum63(float v) {
  v += dpp<DPP_ROR8>(v); v += dpp<DPP_ROR4>(v); v += dpp<DPP_ROR2>(v); v += dpp<DPP_ROR1>(v);
  v += dppm<0x142, 0xa>(v); v += dppm<0x143, 0xc>(v);
  return v;
}
template <int IMM> DEVI void lds8(u2& d, unsigned a) { asm volatile("ds_read_b64 %0, %1 offset:%2" : "=v"(d) : "v"(a), "i"(IMM)); }
template <int IMM> DEVI void lds16(u4& d, unsigned a) { asm volatile("ds_read_b128 %0, %1 offset:%2" : "=v"(d) : "v"(a), "i"(IMM)); }
template <int N> DEVI void waitl(u2& a, u2& b, u4& c) { asm volatile("s_waitcnt lgkmcnt(%3)" : "+v"(a), "+v"(b), "+v"(c) : "i"(N) : "memory"); }
template <int IMM> DEVI void gld16s(u4& d, unsigned voff, const void* sb) { asm volatile("global_load_dwordx4 %0, %1, %2 offset:%3 sc1" : "=v"(d) : "v"(voff), "s"(sb), "i"(IMM)); }
DEVI void gld4s(unsigned& d, unsigned voff, const void* sb) { asm volatile("global_load_dword %0, %1, %2 sc1" : "=v"(d) : "v"(voff), "s"(sb)); }
DEVI unsigned perm(unsigned hi, unsigned lo, unsigned sel) { return __builtin_amdgcn_perm(hi, lo, sel); }

template <int RR, int P> DEVI void conv_ld(u2 (&al)[8], u2 (&ah)[8], u4 (&bb)[8], unsigned pa, unsigned pb) {
  if constexpr (P < 8) {
    lds8<64 * (1 - RR) + 512 * (7 - P)>(al[P], pa);
    lds8<64 * (1 - RR) + 512 * (7 - P) + 8>(ah[P], pa);
    lds16<64 * (1 - RR) + 512 * P>(bb[P], pb);
    conv_ld<RR, P + 1>(al, ah, bb, pa, pb);
  }
}
template <int S> DEVI void conv_mm(u2 (&al)[8], u2 (&ah)[8], u4 (&bb)[8], f4 (&acc)[8]) {
  if constexpr (S < 8) {
    waitl<(3 * (7 - S) > 15 ? 15 : 3 * (7 - S))>(al[S], ah[S], bb[S]);
#pragma unroll
    for (int p = 0; p <= S; ++p) acc[S] = mfma_h((u4){al[p].x, al[p].y, ah[p].x, ah[p].y}, bb[S - p], acc[S]);
    __builtin_amdgcn_sched_barrier(0);
    conv_mm<S + 1>(al, ah, bb, acc);
  }
}
template <int IMM> DEVI void gld8s(u2& d, unsigned voff, const void* sb) { asm volatile("global_load_dwordx2 %0, %1, %2 offset:%3 sc1" : "=v"(d) : "v"(voff), "s"(sb), "i"(IMM)); }
DEVI void gst16s(unsigned voff, u4 v, const void* sb) { asm volatile("global_store_dwordx4 %0, %1, %2 sc1\n\ts_nop 1" ::"v"(voff), "v"(v), "s"(sb) : "memory"); }
DEVI u4 tr2(const char* p, int step) {
  const u2 a = __builtin_bit_cast(u2, __builtin_amdgcn_ds_read_tr16_b64_v4i16((LDSP s4*)p));
  const u2 b = __builtin_bit_cast(u2, __builtin_amdgcn_ds_read_tr16_b64_v4i16((LDSP s4*)(p + step)));
  return (u4){a.x, a.y, b.x, b.y};
}
template <bool TWO, int K> DEVI void tbl_ld(u4 (&a)[8], u4 (&c)[8], u4 (&e)[8], unsigned pa, unsigned pb) {
  if constexpr (K < 8) {
    lds16<64 * K>(a[K], pa); lds16<64 * K>(c[K], pb);
    if constexpr (TWO) lds16<64 * K + 16 * KG_PITCH>(e[K], pb);
    tbl_ld<TWO, K + 1>(a, c, e, pa, pb);
  }
}
template <bool TWO, int K> DEVI void tbl_mm(u4 (&a)[8], u4 (&c)[8], u4 (&e)[8], f4& acc, f4& ac0) {
  if constexpr (K < 8) {
    constexpr int N = (TWO ? 3 : 2) * (7 - K);
    if constexpr (TWO) asm volatile("s_waitcnt lgkmcnt(%3)" : "+v"(a[K]), "+v"(c[K]), "+v"(e[K]) : "i"(N > 15 ? 15 : N) : "memory");
    else asm volatile("s_waitcnt lgkmcnt(%2)" : "+v"(a[K]), "+v"(c[K]) : "i"(N) : "memory");
    acc = mfma_h(a[K], c[K], acc);
    if constexpr (TWO) ac0 = mfma_h(e[K], c[K], ac0);
    __builtin_amdgcn_sched_barrier(0);
    tbl_mm<TWO, K + 1>(a, c, e, acc, ac0);
  }
}
template <bool TWO> DEVI void tbl_tiles(unsigned pa, unsigned pb, f4& acc, f4& ac0) {
  u4 a[8], c[8], e[8];
  tbl_ld<TWO, 0>(a, c, e, pa, pb);
  tbl_mm<TWO, 0>(a, c, e, acc, ac0);
}
DEVI void chsync(unsigned fl, int wq, unsigned phase) {
  asm volatile("s_waitcnt lgkmcnt(0)\n\tds_write_b32 %0, %1" ::"v"(fl + 4 * wq), "v"(phase) : "memory");
  for (;;) {
    u4 v;
    asm volatile("ds_read_b128 %0, %1\n\ts_waitcnt lgkmcnt(0)" : "=v"(v) : "v"(fl) : "memory");
    if (__builtin_amdgcn_readfirstlane(min(min(v.x, v.y), min(v.z, v.w))) >= phase) break;
    __builtin_amdgcn_s_sleep(1);
  }
}
DEVI void rawbar() { asm volatile("s_barrier" ::: "memory"); }
template <int I> DEVI void poll_ld(u4 (&g)[4], unsigned go, const char* base) {
  if constexpr (I < 4) { gld16s<16 * I>(g[I], go, base); poll_ld<I + 1>(g, go, base); }
}
DEVI void wait4(u4 (&g)[4]) { asm volatile("s_waitcnt vmcnt(0)" : "+v"(g[0]), "+v"(g[1]), "+v"(g[2]), "+v"(g[3])::"memory"); }

constexpr int KA_CODE_LINES = 118, KB_CODE_LINES = 290;
__global__ __launch_bounds__(1024, 4) void ka(const u16* __restrict__ x, const u16* __restrict__ logA, const u16* __restrict__ Aim,
                                          const u16* __restrict__ Cre, const u16* __restrict__ Cim, const unsigned* __restrict__ logdt,
                                          const unsigned* __restrict__ dskip, const u16* __restrict__ w_in, const u16* __restrict__ wsp,
                                          const u16* __restrict__ w_out, char* __restrict__ ws) {
  __shared__ __attribute__((aligned(16))) char sm[KA_LDS];
  const int tid = threadIdx.x, lane = tid & 63;
  const int w = __builtin_amdgcn_readfirstlane(tid >> 6);
  const int b = blockIdx.x;
  ST_DECL
  const int g = b & 7, m = b >> 3;
  const bool isconv = m < 27;
  const int d0 = 80 * g + (3 * m < 77 ? 3 * m : 77);
  if (tid < 16) ((int*)(sm + KA_S + 32))[tid] = 0;
  unsigned tag = 0;
  if (w < 4) {
    u4 r0, r1; u2 r2;
    unsigned ep;
    const int row = 256 * g + 8 * m + 2 * w + (lane >> 5), tm = 4 * g + (m >> 3), tg = m & 7;
    const unsigned ro = (unsigned)row * 1280 + (lane & 31) * 16;
    gld4s(ep, 0, ws + OFF_EP);
    gld16<0>(r0, ro, x); gld16<512>(r1, ro, x); gld8<1024>(r2, ro - (lane & 31) * 8, x);
    bar();
    {
      const char* tsrc = (const char*)x + (size_t)tm * 64 * 1280 + tg * 160;
      const unsigned dst = lds_addr(sm) + KA_U + w * 1024;
#pragma unroll
      for (int i = 0; i < 3; ++i) { const unsigned pc = tid + 256 * i < 640 ? tid + 256 * i : 639; dma16(dst + i * 4096, (pc / 10) * 1280 + (pc % 10) * 16, tsrc); }
    }
    __builtin_amdgcn_s_setprio(2);
    asm volatile("s_waitcnt vmcnt(3)" : "+v"(ep), "+v"(r0), "+v"(r1), "+v"(r2)::"memory");
    ST(1);
    tag = ep + 0x9E3779B9u;
    f2 s0 = {bflo(r2.x), bfhi(r2.x)}, s1 = {bflo(r2.y), bfhi(r2.y)}, q0 = s0 * s0, q1 = s1 * s1;
#pragma unroll
    for (int c = 0; c < 4; ++c) {
      const unsigned v = r0[c], v2 = r1[c];
      const f2 a = {bflo(v), bfhi(v)}, a2 = {bflo(v2), bfhi(v2)};
      s0 += a; q0 += a * a; s1 += a2; q1 += a2 * a2;
    }
    s0 += s1; q0 += q1;
    float ss = s0.x + s0.y, qq = q0.x + q0.y;
    ss += dpp<DPP_ROR8>(ss); qq += dpp<DPP_ROR8>(qq); ss += dpp<DPP_ROR4>(ss); qq += dpp<DPP_ROR4>(qq);
    ss += dpp<DPP_ROR2>(ss); qq += dpp<DPP_ROR2>(qq); ss += dpp<DPP_ROR1>(ss); qq += dpp<DPP_ROR1>(qq);
    ss += dppm<0x142, 0xa>(ss); qq += dppm<0x142, 0xa>(qq);
    const float mean = ss * (1.0f / D);
    const float var = fmaxf(qq * (1.0f / D) - mean * mean, 0.f);
    unsigned pay = pk_bf(mean, __builtin_amdgcn_sqrtf(rbf(rbf(var) + 1e-5f)));
    pin(pay);
    u4 gv = {(unsigned)__builtin_amdgcn_readlane(pay, 31), tag, pay, tag};
    pin(gv);
    waitv<0>();
    if (lane == 63) gst16s((row - 1) * 8, gv, ws + OFF_G1);
    chsync(lds_addr(sm) + KA_S + 80, w, 1);
    ST(2);
#pragma unroll 1
    for (int bl = w; bl < 5; bl += 4) {
      const int l = lane & 15, gm = lane >> 4;
      const char* tp = sm + KA_U + (8 * gm + (l >> 2)) * 160 + bl * 32 + (l & 3) * 8;
      const u4 A = tr2(tp, 640), B = tr2(tp + 32 * 160, 640);
      u4 s1v, s2v;
#pragma unroll
      for (int e = 0; e < 4; ++e) {
        const unsigned ae = A[e], be = B[e];
        s1v[e] = __builtin_amdgcn_update_dpp(ae, be, DPP_ROR8, 0xf, 0xc, false);
        s2v[e] = __builtin_amdgcn_update_dpp(be, ae, DPP_ROR8, 0xf, 0x3, false);
      }
      const unsigned vo = (unsigned)(80 * tg + 16 * bl + (l & 7)) * 4096 + tm * 128 + (gm + 4 * (l >> 3)) * 16;
      gst16s(vo, s1v, ws + OFF_XT);
      gst16s(vo + 8 * 4096, s2v, ws + OFF_XT);
    }
    waitv<0>();
    chsync(lds_addr(sm) + KA_S + 80, w, 2);
    if (tid == 0) __hip_atomic_store((unsigned*)(ws + OFF_F1) + (32 * tg + tm) * 32, tag, __ATOMIC_RELAXED, __HIP_MEMORY_SCOPE_AGENT);
    ST(3);
    if (isconv) {
      u4 gr[4], xr[3];
      poll_ld<0>(gr, tid * 64, ws + OFF_G1);
      if (w == 0) {
#pragma unroll 1
        for (int spin = 0; spin < (1 << 16); ++spin) {
          unsigned v;
          asm volatile("global_load_dword %0, %1, %2 sc1\n\ts_waitcnt vmcnt(0)" : "=v"(v) : "v"((lane & 31) * 128), "s"(ws + OFF_F1 + g * 4096) : "memory");
          if (__all(v == tag)) break;
          __builtin_amdgcn_s_sleep(2);
        }
      }
      wait4(gr);
      chsync(lds_addr(sm) + KA_S + 80, w, 3);
      ST(4);
      const char* xsrc = ws + OFF_XT + (size_t)d0 * 4096;
      gld16s<0>(xr[0], tid * 16, xsrc); gld16s<0>(xr[1], tid * 16, xsrc + 4096); gld16s<0>(xr[2], tid * 16, xsrc + 8192);
#pragma unroll 1
      for (int spin = 0; spin < (1 << 16); ++spin) {
        bool ok = true;
#pragma unroll
        for (int i = 0; i < 4; ++i) ok = ok && gr[i].y == tag && gr[i].w == tag;
        if (__all(ok)) break;
        poll_ld<0>(gr, tid * 64, ws + OFF_G1);
        wait4(gr);
      }
      f2 mm[4], rs[4];
#pragma unroll
      for (int i = 0; i < 4; ++i) { mm[i] = (f2){bflo(gr[i].x), bflo(gr[i].z)}; rs[i] = (f2){__builtin_amdgcn_rcpf(bfhi(gr[i].x)), __builtin_amdgcn_rcpf(bfhi(gr[i].z))}; }
      if (tid < 90) *(u4*)(sm + KA_U + (tid / 30) * U_PITCH + (tid % 30) * 16) = (u4){0, 0, 0, 0};
      ST(7);
      asm volatile("s_waitcnt vmcnt(0)" : "+v"(xr[0]), "+v"(xr[1]), "+v"(xr[2])::"memory");
      ST(8);
#pragma unroll
      for (int c = 0; c < 3; ++c) {
        u4 o;
#pragma unroll
        for (int i = 0; i < 4; ++i) { const unsigned v = xr[c][i]; o[i] = pk_h2(((f2){bflo(v), bfhi(v)} - mm[i]) * rs[i]); }
        *(u4*)(sm + KA_U + c * U_PITCH + 480 + tid * 16) = o;
      }
      __builtin_amdgcn_s_setprio(0);
      bar();
      ST(6);
      if (b == 0 && tid == 0) __hip_atomic_store((unsigned*)(ws + OFF_EP), tag, __ATOMIC_RELAXED, __HIP_MEMORY_SCOPE_AGENT);
      __builtin_amdgcn_s_sleep(4);
      if (w != 0) {
        unsigned pw[4];
#pragma unroll
        for (int i = 0; i < 4; ++i) {
          const unsigned ln = (i * 81 + m * 3 + w - 1) * 64 + lane;
          gld4<0>(pw[i], (ln < 16384 ? ln : 16383) * 128, (const char*)wsp + (size_t)(g >> 1) * (16384 * 128));
        }
        if (m < 2) {
          const unsigned ln = (m * 3 + w - 1) * 64 + lane;
          const char* code;
          unsigned pc;
          asm volatile("s_getpc_b64 s[44:45]\n\ts_add_u32 s44, s44, _Z2kbPKtS0_PcPt@rel32@lo+4\n\ts_addc_u32 s45, s45, _Z2kbPKtS0_PcPt@rel32@hi+12\n\ts_mov_b64 %0, s[44:45]"
                       : "=s"(code)::"s44", "s45", "scc");
          gld4<0>(pc, (ln < KB_CODE_LINES ? ln : KB_CODE_LINES - 1) * 128, code);
          asm volatile("s_waitcnt vmcnt(0)" : "+v"(pc)::"memory");
        }
        asm volatile("s_waitcnt vmcnt(0)" : "+v"(pw[0]), "+v"(pw[1]), "+v"(pw[2]), "+v"(pw[3])::"memory");
      } else {
      const int item = b * 40 + lane;
      if (lane < 40) {
        const int oc = item % 80, kq = item / 80, kap = kq >> 2, qq = kq & 3;
        u4 r[8], o[8];
#pragma unroll
        for (int e = 0; e < 8; ++e) r[e] = *(const u4*)(w_out + (size_t)(32 * kap + 16 * (e >> 2) + 4 * qq + (e & 3)) * D + oc * 8);
        tr8x8(r, o);
        u4* dst = (u4*)(ws + OFF_WO + ((size_t)(oc >> 1) * NKS3 + kap) * 1024) + qq * 16 + (oc & 1) * 8;
#pragma unroll
        for (int c = 0; c < 8; ++c) dst[c] = o[c];
      }
      }
      ST_END(0);
      return;
    }
    __builtin_amdgcn_s_setprio(0);
  }
  if (!isconv) {
    unsigned pc = 0;
    if (w == 4) {
      const unsigned ln = (m - 27) * 64 + lane;
      const char* code;
      asm volatile("s_getpc_b64 s[44:45]\n\ts_add_u32 s44, s44, _Z2kaPKtS0_S0_S0_S0_PKjS2_S0_S0_S0_Pc@rel32@lo+4\n\t"
                   "s_addc_u32 s45, s45, _Z2kaPKtS0_S0_S0_S0_PKjS2_S0_S0_S0_Pc@rel32@hi+12\n\ts_mov_b64 %0, s[44:45]" : "=s"(code)::"s44", "s45", "scc");
      gld4<0>(pc, (ln < KA_CODE_LINES ? ln : KA_CODE_LINES - 1) * 128, code);
    }
    if (w >= 4) rawbar();
    rawbar();
    if (w == 4) asm volatile("s_waitcnt vmcnt(0)" : "+v"(pc)::"memory");
    const int si = b - 216;
    if (si < 27) {
      const int tt = tid - 384, co = tt & 7, ko = tt >> 3, c0 = 64 * si + 8 * co, k0 = 8 * ko;
      float* part = (float*)(sm + 98304);
      if (w >= 6) {
        u4 r[8], o[8];
#pragma unroll
        for (int e = 0; e < 8; ++e) r[e] = *(const u4*)(w_in + (size_t)(k0 + e) * F + c0);
        float cs[8];
#pragma unroll
        for (int c = 0; c < 4; ++c) {
          f2 s = {0.f, 0.f};
#pragma unroll
          for (int e = 0; e < 8; ++e) { const unsigned v = r[e][c]; s += (f2){bflo(v), bfhi(v)}; }
          cs[2 * c] = s.x + dpp<DPP_ROR8>(s.x); cs[2 * c + 1] = s.y + dpp<DPP_ROR8>(s.y);
        }
        if (!(lane & 8)) {
          f4* p = (f4*)(part + ((w - 6) * 4 + (lane >> 4)) * 64 + co * 8);
          p[0] = (f4){cs[0], cs[1], cs[2], cs[3]}; p[1] = (f4){cs[4], cs[5], cs[6], cs[7]};
        }
        tr8x8(r, o);
#pragma unroll
        for (int c = 0; c < 8; ++c) *(u4*)(sm + tt * 144 + c * 16) = o[c];
      }
      __syncthreads();
      if (w < 8) {
        float s = 0.f;
#pragma unroll
        for (int e = 0; e < 5; ++e) s += part[(w * 5 + e) * 64 + lane];
        part[2560 + w * 64 + lane] = s;
      }
#pragma unroll
      for (int i = 0; i < 5; ++i) {
        const int f = w * 5 + i, blk = f / 20, ks = f % 20, gc = 4 * si + blk;
        const u4 v = *(const u4*)(sm + ((4 * ks + (lane >> 4)) * 8 + 2 * blk + ((lane >> 3) & 1)) * 144 + (lane & 7) * 16);
        *(u4*)(ws + OFF_WF + ((size_t)(gc >= NGB ? gc - NGB : gc + NGB) * NKS1 + ks) * 1024 + lane * 16) = v;
      }
      __syncthreads();
      if (w == 0) {
        float s = 0.f;
#pragma unroll
        for (int e = 0; e < 8; ++e) s += part[2560 + e * 64 + lane];
        ((float*)(ws + OFF_CS))[64 * si + lane] = s;
      }
    } else if (si < 35) {
      ((u4*)(ws + OFF_ST))[(si - 27) * 1024 + tid] = (u4){0, 0, 0, 0};
    } else if (si == 35) {
      if (tid < 576) *(u4*)(ws + OFF_F2 + (tid < 512 ? tid * 128 : 65536 + (tid - 512) * 16)) = (u4){0, 0, 0, 0};
    }
    ST_END(0);
    return;
  }
  const int ch = w < 4 ? 0 : (w >> 2) - 1, role = (w + ch) & 3;
  const int d = d0 + ch;
  const int col = lane & 15, q = lane >> 4;
  char* rc = sm + ch * RC_SZ;
  float* slot = (float*)(sm + KA_S) + 2 * ch;
  const unsigned fl = lds_addr(sm) + KA_S + 32 + 16 * ch;
  if (w >= 4) {
    unsigned la, ai, cr, ci, ldw, dw;
    gld4<0>(ldw, 0, logdt + (d >> 1));
    gld4<0>(dw, 0, dskip + (d >> 1));
    gld4<0>(la, lane * 4, logA + d * NM);
    gld4<0>(ai, lane * 4, Aim + d * NM);
    gld4<0>(cr, lane * 4, Cre + d * NM);
    gld4<0>(ci, lane * 4, Cim + d * NM);
    rawbar();
    asm volatile("s_waitcnt vmcnt(0)" : "+v"(la), "+v"(ai), "+v"(cr), "+v"(ci), "+v"(ldw), "+v"(dw)::"memory");
    ST(1);
    const float dt = rbf(__expf((d & 1) ? bfhi(ldw) : bflo(ldw)));
    const unsigned ea = pk_bf(__expf(bflo(la)), __expf(bfhi(la)));
    const f2 a2 = (f2){bflo(ea), bfhi(ea)} * (-1.4426950408889634f * dt);
    f2 br = (f2){bflo(ai), bfhi(ai)} * (dt * 0.15915494309189535f);
    char* base = rc + lane * 8;
    auto put = [&](int row, c2 v) { *(u2*)(base + row * KG_PITCH) = (u2){pk_h2(v.x), pk_h2(v.y)}; };
    if (role == 0) {
      c2 v = {{bflo(cr), bfhi(cr)}, {bflo(ci), bfhi(ci)}};
      float mx = fmaxf(fmaxf(fabsf(v.x.x), fabsf(v.x.y)), fmaxf(fabsf(v.y.x), fabsf(v.y.y)));
      mx = fmaxf(mx, dpp<DPP_ROR8>(mx)); mx = fmaxf(mx, dpp<DPP_ROR4>(mx)); mx = fmaxf(mx, dpp<DPP_ROR2>(mx)); mx = fmaxf(mx, dpp<DPP_ROR1>(mx));
      mx = fmaxf(mx, dppm<0x142, 0xa>(mx)); mx = fmaxf(mx, dppm<0x143, 0xc>(mx));
      mx = __int_as_float(__builtin_amdgcn_readlane(__float_as_int(mx), 63));
      const float dsk = (d & 1) ? bfhi(dw) : bflo(dw);
      const float sc = fmaxf(fmaxf(2.0f * dt * mx, fabsf(dsk) * (1.0f / 16384.0f)), 1e-30f);
      const float isc = 1.0f / sc;
      if (lane == 0) { slot[0] = sc; slot[1] = dsk * isc; }
      v.x *= 2.0f * dt * isc; v.y *= 2.0f * dt * isc;
      const c2 s = cexpp(a2, br, 8.f);
#pragma unroll
      for (int j = 0; j < 16; ++j) { put(j, v); if (j < 15) v = cmul(v, s); }
    } else {
      br = -br;
      const c2 h = cexpp(a2, br, 128.f);
      if (role == 1) {
        const c2 s = cexpp(a2, br, 1.f);
        put(16, (c2){{1.f, 1.f}, {0.f, 0.f}}); put(24, h);
        c2 v = s;
#pragma unroll
        for (int c = 1; c < 8; ++c) { put(16 + c, v); put(24 + c, cmul(v, h)); if (c < 7) v = cmul(v, s); }
      } else if (role == 2) {
        const c2 s = cexpp(a2, br, 2.f);
        c2 v = cmul(h, h);
        const c2 q = cmul(s, s), y = cmul(v, v), z = cmul(y, h);
        put(40, y); put(41, cmul(y, q)); put(42, z); put(43, cmul(z, q));
#pragma unroll
        for (int c = 0; c < 4; ++c) { put(32 + c, v); put(36 + c, cmul(v, h)); if (c < 3) v = cmul(v, s); }
      } else {
        const c2 q = cexpp(a2, br, 4.f);
        c2 v = cexpp(a2, br, 768.f);
        put(44, v); put(45, cmul(v, q)); v = cmul(v, h);
        put(46, v); put(47, cmul(v, q));
#pragma unroll
        for (int i = 0; i < 9; ++i) { v = cmul(v, h); put(48 + i, v); }
      }
    }
  ST(2);
  chsync(fl, w & 3, 1);
  char* tb = sm + KA_T + ch * T_PITCH;
  {
    const int wq = w & 3;
    const unsigned pb = lds_addr(rc) + col * KG_PITCH + q * 16;
    f4 acc = {0.f, 0.f, 0.f, 0.f}, ac0 = {0.f, 0.f, 0.f, 0.f};
    const float dadd = slot[1];
    if (wq == 3) tbl_tiles<true>(pb + 32 * KG_PITCH, pb, acc, ac0); else tbl_tiles<false>(pb + (wq < 2 ? 48 : 40) * KG_PITCH, pb, acc, ac0);
    const unsigned p0 = pk_h(acc[0], acc[1]), p1 = pk_h(acc[2], acc[3]);
    if (wq < 2) {
      u16* t = (u16*)tb + 512 + 64 * q + col;
      t[0] = (u16)p0; t[16] = (u16)(p0 >> 16); t[32] = (u16)p1; t[48] = (u16)(p1 >> 16);
    } else if (wq == 2) {
      if (q < 2) { *(unsigned*)(tb + 2 * (384 + 64 * q + 2 * col)) = p0; *(unsigned*)(tb + 2 * (416 + 64 * q + 2 * col)) = p1; }
      else if (lane == 32) ((u16*)tb)[512] = (u16)p0;
    } else {
      if (q < 2) *(u2*)(tb + 2 * (256 + 64 * q + 4 * col)) = (u2){p0, p1};
      else if (lane == 32) ((u16*)tb)[384] = (u16)p0;
      if (lane == 0) ac0[0] += dadd;
      *(u2*)(tb + 2 * (128 * (q >> 1) + 4 * (q & 1) + 8 * col)) = (u2){pk_h(ac0[0], ac0[1]), pk_h(ac0[2], ac0[3])};
    }
  }
  asm volatile("s_waitcnt lgkmcnt(0)" ::: "memory");
  ST(3);
  {
    const int t8 = tid & 255, a = 255 - t8, wq = w & 3;
    u4 o0, o1;
    if (wq < 2) {
      const u16* t = (const u16*)tb + 384 + a;
      const unsigned lo = t[0], hi = t[1];
      const unsigned hh = perm(hi, hi, 0x01000100u), ll = perm(lo, lo, 0x01000100u), tie = (a & 1) ? hi : lo;
      o0 = (u4){hh, perm(tie, hi, 0x05040100u), ll, ll}; o1 = (u4){hh, perm(lo, tie, 0x05040100u), ll, ll};
    } else if (wq == 2) {
      const unsigned* t = (const unsigned*)(tb + 2 * (256 + 2 * a));
      const unsigned v01 = t[0], v2 = t[1];
      const unsigned t11 = perm(v01, v01, 0x03020302u), t00 = perm(v01, v01, 0x01000100u);
      o0 = (u4){perm(v2, v2, 0x01000100u), t11, perm(v01, v01, 0x01000302u), t00}; o1 = (u4){perm(v01, v2, 0x07060100u), t11, t00, t00};
    } else if (a >= 32) {
      const unsigned* t = (const unsigned*)(tb + 2 * (128 + 4 * a));
      const unsigned vm = t[-1], v01 = t[0], v23 = t[1], v4 = t[2];
      const unsigned t22 = perm(v23, v23, 0x01000100u);
      o0 = (u4){perm(v23, v4, 0x07060100u), t22, perm(v01, v23, 0x07060100u), perm(v01, v01, 0x01000100u)};
      o1 = (u4){perm(v23, v23, 0x01000302u), t22, perm(v01, v01, 0x01000302u), perm(vm, v01, a == 32 ? 0x07060100u : 0x01000100u)};
    } else {
      const u4 v = *(const u4*)(tb + 16 * a);
      const unsigned vm = a ? *(const unsigned*)(tb + 16 * a - 4) : 0u;
      o0 = (u4){perm(v.w, v.w, 0x01000302u), perm(v.z, v.z, 0x01000302u), perm(v.y, v.y, 0x01000302u), perm(v.x, v.x, 0x01000302u)};
      o1 = (u4){perm(v.z, v.w, 0x07060100u), perm(v.y, v.z, 0x07060100u), perm(v.x, v.y, 0x07060100u), perm(vm, v.x, 0x07060100u)};
    }
    char* kcb = sm + KA_K + ch * 4 * KC_PITCH;
    char* kc = kcb + 16 * t8;
    *(u4*)kc = o0; *(u4*)(kc + KC_PITCH) = o1;
    unsigned* k2 = (unsigned*)(kc + 2 * KC_PITCH - 4);
    unsigned* k3 = (unsigned*)(kc + 3 * KC_PITCH - 4);
    k2[0] = o0.x; k2[1] = o0.y; k2[2] = o0.z; k2[3] = o0.w;
    k3[0] = o1.x; k3[1] = o1.y; k3[2] = o1.z; k3[3] = o1.w;
    if (wq == 0 && lane < 36) { const int s = lane / 9; *(unsigned*)(kcb + s * KC_PITCH + 4096 - (s >> 1) * 4 + (lane - 9 * s) * 4) = 0; }
  }
  ST(4);
  }
  ST(5);
  bar();
  ST(6);
  const int wq = w & 3;
  u2 xv[2];
  gld8s<0>(xv[0], (512 * wq + 16 * col + 4 * q) * 2, ws + OFF_XT + (size_t)d * 4096);
  gld8s<512>(xv[1], (512 * wq + 16 * col + 4 * q) * 2, ws + OFF_XT + (size_t)d * 4096);
  f4 acc[8];
#pragma unroll
  for (int n = 0; n < 8; ++n) acc[n] = (f4){0.f, 0.f, 0.f, 0.f};
  {
    const int s = (3 - col) & 3;
    const unsigned pa = lds_addr(sm) + KA_K + (4 * ch + s) * KC_PITCH + 2 * (2031 - s - col + 8 * q) - 64 * (2 * wq + 1) - 512 * 7;
    const unsigned pb = lds_addr(sm) + KA_U + ch * U_PITCH + 448 + 32 * col + 16 * q - 64 * (2 * wq + 1);
    u2 al[8], ah[8]; u4 bb[8];
    conv_ld<0, 0>(al, ah, bb, pa, pb);
    conv_mm<0>(al, ah, bb, acc);
    __builtin_amdgcn_sched_barrier(0);
    conv_ld<1, 0>(al, ah, bb, pa, pb);
    conv_mm<0>(al, ah, bb, acc);
  }
  ST(7);
  f4 tot[2];
  {
    f4* red = (f4*)(rc + wq * 6144) + lane;
#pragma unroll
    for (int v = 0; v < 4; ++v) {
      if (v == wq) { tot[0] = acc[2 * v]; tot[1] = acc[2 * v + 1]; }
      else { const int sl = v > wq ? v - 1 : v; red[sl * 128] = acc[2 * v]; red[sl * 128 + 64] = acc[2 * v + 1]; }
    }
  }
  chsync(fl, w & 3, 2);
  ST(8);
#pragma unroll
  for (int v = 0; v < 3; ++v) {
    const int src = v < wq ? v : v + 1;
    const f4* rp = (const f4*)(rc + src * 6144) + (wq > src ? wq - 1 : wq) * 128 + lane;
    tot[0] += rp[0]; tot[1] += rp[64];
  }
  const float sc = slot[0];
  asm volatile("s_waitcnt vmcnt(0)" : "+v"(xv[0]), "+v"(xv[1])::"memory");
#pragma unroll
  for (int nn = 0; nn < 2; ++nn) {
    const int t = 512 * wq + 256 * nn + 16 * col + 4 * q;
    const int pos = (t & ~31) + 16 * ((t >> 2) & 1) + 4 * ((t >> 3) & 3);
    const float h[4] = {__builtin_fmaf(tot[nn][0], sc, bflo(xv[nn].x)), __builtin_fmaf(tot[nn][1], sc, bfhi(xv[nn].x)),
                        __builtin_fmaf(tot[nn][2], sc, bflo(xv[nn].y)), __builtin_fmaf(tot[nn][3], sc, bfhi(xv[nn].y))};
    *(u2*)(ws + OFF_HT + (size_t)(pos >> 6) * 81920 + d * 128 + (pos & 63) * 2) = (u2){pk_bf(h[0], h[1]), pk_bf(h[2], h[3])};
  }
  ST_END(0);
}

DEVI void tile_of(int b, int& i, int& j) { const int g = b & 7, r = b >> 3; i = (g >> 1) * 8 + (r >> 2); j = (g & 1) * 4 + (r & 3); }
constexpr int KB_RED = 81920, KB_SLOT = 7168, KB_Z1 = KB_RED + 65536, KB_S2 = KB_Z1 + 14336, KB_S3 = KB_S2 + 512,
              KB_CNT = KB_S3 + 512, KB_LDS = KB_CNT + 16;
#define KSP 12
constexpr int KSPLIT = KSP;

#define STSET 0
#define STK(set, i) do { if constexpr (STSET == set) ST(i); } while (0)
template <bool Z> DEVI void mmv(f4& d, const u4& a, const u4& b) {
  if constexpr (Z) asm volatile("v_mfma_f32_16x16x32_bf16 %0, %1, %2, 0" : "=v"(d) : "v"(a), "v"(b));
  else asm volatile("v_mfma_f32_16x16x32_bf16 %0, %1, %2, %0" : "+v"(d) : "v"(a), "v"(b));
}
template <bool Z> DEVI void mmva(f4& d, const u4& a, const u4& b) {
  if constexpr (Z) asm volatile("v_mfma_f32_16x16x32_bf16 %0, %1, %2, 0" : "=v"(d) : "v"(a), "a"(b));
  else asm volatile("v_mfma_f32_16x16x32_bf16 %0, %1, %2, %0" : "+v"(d) : "v"(a), "a"(b));
}
template <bool Z> DEVI void mmav(f4& d, const u4& a, const u4& b) {
  if constexpr (Z) asm volatile("v_mfma_f32_16x16x32_bf16 %0, %1, %2, 0" : "=v"(d) : "a"(a), "v"(b));
  else asm volatile("v_mfma_f32_16x16x32_bf16 %0, %1, %2, %0" : "+v"(d) : "a"(a), "v"(b));
}
template <int IMM> DEVI void gld16a(u4& d, unsigned voff, const void* sb) { asm volatile("global_load_dwordx4 %0, %1, %2 offset:%3" : "=a"(d) : "v"(voff), "s"(sb), "i"(IMM)); }
template <int IMM> DEVI void rld16(u4& d, unsigned voff, const void* sb) { asm volatile("global_load_dwordx4 %0, %1, %2 offset:%3" : "+v"(d) : "v"(voff), "s"(sb), "i"(IMM)); }
template <int IMM> DEVI void rld16s(u4& d, unsigned voff, const void* sb) { asm volatile("global_load_dwordx4 %0, %1, %2 offset:%3 sc1" : "+v"(d) : "v"(voff), "s"(sb), "i"(IMM)); }
template <int IMM> DEVI void trd(u2& d, unsigned a) { asm volatile("ds_read_b64_tr_b16 %0, %1 offset:%2" : "=v"(d) : "v"(a), "i"(IMM)); }
template <int IMM> DEVI void ldsw16(unsigned a, const f4& v) { asm volatile("ds_write_b128 %0, %1 offset:%2" ::"v"(a), "v"(v), "i"(IMM) : "memory"); }
DEVI void dot2(float& acc, unsigned a, unsigned b) { asm volatile("v_dot2_f32_bf16 %0, %1, %2, %0" : "+v"(acc) : "v"(a), "v"(b)); }
DEVI void dot2s(float& acc, unsigned a, unsigned b) { asm volatile("v_dot2_f32_bf16 %0, %1, %2, %0" : "+v"(acc) : "v"(a), "s"(b)); }
DEVI void settle(f4& a, f4& b, f4& c, f4& d) { asm volatile("s_nop 7" : "+v"(a), "+v"(b), "+v"(c), "+v"(d)); }
DEVI void hold(f4& a, f4& b, f4& c, f4& d) { asm volatile("" : "+v"(a), "+v"(b), "+v"(c), "+v"(d)); }
DEVI u4 cat(const u2& a, const u2& b) { return (u4){a.x, a.y, b.x, b.y}; }
DEVI f2 gelu2(f2 x) {
  const f2 u = x * (x * x * -0.1029432397f + -2.3022081985f);
  const f2 d = (f2){__builtin_amdgcn_exp2f(u.x), __builtin_amdgcn_exp2f(u.y)} + 1.0f;
  return x * (f2){__builtin_amdgcn_rcpf(d.x), __builtin_amdgcn_rcpf(d.y)};
}
template <int CTRL, int H> DEVI void bfly(float (&v)[16], bool up) {
#pragma unroll
  for (int k = 0; k < H; ++k) { const float send = up ? v[k] : v[k + H], keep = up ? v[k + H] : v[k]; v[k] = keep + dpp<CTRL>(send); }
}
DEVI float rows_sum(float v) {
  auto a = __builtin_amdgcn_permlane32_swap(__float_as_uint(v), __float_as_uint(v), false, false);
  v = __uint_as_float(a[0]) + __uint_as_float(a[1]);
  auto b = __builtin_amdgcn_permlane16_swap(__float_as_uint(v), __float_as_uint(v), false, false);
  return __uint_as_float(b[0]) + __uint_as_float(b[1]);
}
DEVI int fresh_lane() {
  int l;
  asm volatile("v_mbcnt_lo_u32_b32 %0, -1, 0\n\tv_mbcnt_hi_u32_b32 %0, -1, %0" : "=v"(l));
  return l;
}
DEVI bool last_of_four(unsigned cnt_addr) {
  unsigned o = 0;
  if (fresh_lane() == 0) asm volatile("ds_add_rtn_u32 %0, %1, %2\n\ts_waitcnt lgkmcnt(0)" : "=v"(o) : "v"(cnt_addr), "v"(1u) : "memory");
  return __builtin_amdgcn_readfirstlane(o) == 3;
}
DEVI void poll8(unsigned voff, const void* sb) {
  unsigned t, n;
  asm volatile(
      "s_mov_b32 %1, 0x40000\n"
      "1:\n\tglobal_load_dword %0, %2, %3 sc1\n\ts_waitcnt vmcnt(0)\n\tv_cmp_ne_u32 vcc, 0, %0\n\ts_sub_u32 %1, %1, 1\n\ts_cbranch_scc1 2f\n\t"
      "s_cmp_eq_u64 vcc, -1\n\ts_cbranch_scc1 2f\n\ts_sleep 1\n\ts_branch 1b\n2:"
      : "=&v"(t), "=&s"(n) : "v"(voff), "s"(sb) : "vcc", "scc", "memory");
}

struct G1 {
  u2 al[2][4], ah[2][4];
  u4 ring[8][2], ones;
  unsigned touch[4], va[2], sb;
  const char* hsrc;
  f4 acc[2][4], sx, sq;
  unsigned pa[4], vb[2];
  const char *b2, *b1;
  u64* st_;
};
struct WS { unsigned vw[2], rd[2], ring; const char* src[4]; };
template <int C, int M> DEVI void w_dma(const WS& x) {
  asm volatile("s_mov_b32 m0, %0\n\ts_nop 0\n\tglobal_load_lds_dwordx4 %1, %3\n\tglobal_load_lds_dwordx4 %2, %3 offset:1024"
               ::"s"(x.ring + (C & 1) * 8192 + M * 2048), "v"(x.vw[0]), "v"(x.vw[1]), "s"(x.src[M] + C * 128) : "memory");
}
DEVI void w_dma_rt(const WS& x, int C, int M) {
  asm volatile("s_mov_b32 m0, %0\n\ts_nop 0\n\tglobal_load_lds_dwordx4 %1, %3\n\tglobal_load_lds_dwordx4 %2, %3 offset:1024"
               ::"s"(x.ring + (C & 1) * 8192 + M * 2048), "v"(x.vw[0]), "v"(x.vw[1]), "s"(x.src[M] + C * 128) : "memory");
}
template <int C, int M, int KH> DEVI void w_read(const WS& x, u4 (&W)[16][4]) {
  asm volatile("ds_read_b128 %0, %1 offset:%2" : "=a"(W[2 * C + KH][M]) : "v"(x.rd[KH]), "i"((C & 1) * 8192 + M * 2048));
}
template <int C> DEVI void w_dma_all(const WS& x) { w_dma<C, 0>(x); w_dma<C, 1>(x); w_dma<C, 2>(x); w_dma<C, 3>(x); }
template <int C> DEVI void w_read_all(const WS& x, u4 (&W)[16][4]) {
  w_read<C, 0, 0>(x, W); w_read<C, 0, 1>(x, W); w_read<C, 1, 0>(x, W); w_read<C, 1, 1>(x, W);
  w_read<C, 2, 0>(x, W); w_read<C, 2, 1>(x, W); w_read<C, 3, 0>(x, W); w_read<C, 3, 1>(x, W);
}
DEVI void w_setup(WS& x, int lane, int w, int i, unsigned sb, const u16* wsp) {
  const int c = lane & 15, g4 = lane >> 4;
  x.ring = sb + KB_RED + w * 16384;
#pragma unroll
  for (int o = 0; o < 2; ++o) {
    x.vw[o] = (unsigned)(16 * o + 8 * (lane >> 5) + ((lane >> 3) & 3)) * 4096 + (((lane & 7) ^ ((4 * o + g4) & 7)) * 16) - 1024 * o;
    x.rd[o] = x.ring + c * 128 + (((4 * o + g4) ^ ((c >> 1) & 7)) * 16);
  }
#pragma unroll
  for (int m = 0; m < 4; ++m) x.src[m] = (const char*)wsp + (size_t)(64 * i + 32 * ((m ^ w) >> 1) + 4 * ((m ^ w) & 1)) * 4096 + w * 1024;
}
template <int KS> DEVI const char* wf_base(const char* b) { return b + (KS >> 3) * 8192 + 4096; }
template <int KS, int X> DEVI void ld_new(G1& g, u4& d, const char* b) { gld16<(KS & 7) * 1024 - 4096>(d, g.vb[X], wf_base<KS>(b)); }
template <int KS, int X> DEVI void ld_ring(G1& g, u4& d, const char* b) { rld16<(KS & 7) * 1024 - 4096>(d, g.vb[X], wf_base<KS>(b)); }
template <int S, int M> DEVI void rd_lo(G1& g) { trd<(S & 15) * 4096>(g.al[S & 1][M], g.pa[M]); }
template <int S, int M> DEVI void rd_hi(G1& g) { trd<(S & 15) * 4096 + 512>(g.ah[S & 1][M], g.pa[M]); }
template <int N, int LG> DEVI void g1_wait(G1& g, int c, int m) {
  asm volatile("s_waitcnt vmcnt(%4) lgkmcnt(%5)" : "+v"(g.al[c][m]), "+v"(g.ah[c][m]), "+v"(g.al[c][m + 1]), "+v"(g.ah[c][m + 1]) : "i"(N), "i"(LG) : "memory");
}
template <int S> DEVI void pa_step(G1& g) {
  if constexpr (S == 16) { for (int m = 0; m < 4; ++m) g.pa[m] += 65536; }
  if constexpr (S == 20) { for (int m = 0; m < 4; ++m) g.pa[m] -= 65536; }
}
template <int KS, int H> DEVI void a_dma(const G1& g) {
  asm volatile("s_mov_b32 m0, %0\n\ts_nop 0\n\tglobal_load_lds_dwordx4 %1, %3\n\tglobal_load_lds_dwordx4 %2, %3 offset:1024"
               ::"s"(g.sb + KS * 4096 + H * 2048), "v"(g.va[0]), "v"(g.va[1]), "s"(g.hsrc + KS * 4096 + H * 2048) : "memory");
}
template <int G, int X> DEVI void b_next(G1& g) {
  if constexpr (G + 8 < 20) ld_ring<G + 8, X>(g, g.ring[G & 7][X], g.b2);
  else if constexpr (G + 8 < 40) ld_ring<G + 8 - 20, X>(g, g.ring[G & 7][X], g.b1);
}
constexpr bool w_rd_at(int s) { return (s >= 0 && s < 8) || (s >= 10 && s < 18); }
constexpr bool w_dma_at(int s) { return w_rd_at(s - 1); }
constexpr int w_tile(int s) { return s < 8 ? s : s - 2; }
constexpr int ops_at(int nb, int k) { return (k < 32 ? nb : 0) + (nb == 1 && k < 10 ? 4 : 0) + (nb == 1 && k == 20 ? 3 : 0) + (w_dma_at(k - 20) ? 2 : 0); }
constexpr int ops_in(int nb, int a, int b) { int n = 0; for (int k = a; k < b; ++k) n += ops_at(nb, k); return n; }
constexpr int imin(int a, int b) { return a < b ? a : b; }
constexpr int g1_vm(int nb, int G) {
  int n = G < 4 ? nb * (7 - G) + 6 + ops_in(nb, 0, G) : G < 8 ? nb * (7 - G) + ops_in(nb, 0, G) : ops_in(nb, G - 7, G);
  if (G == 3) n = imin(n, 4 * nb + ops_in(nb, 0, 3));
  if (nb == 1 && G == 9) n = imin(n, 1 + ops_in(1, 5, 9));
  if (nb == 1 && G == 14) n = imin(n, 1 + ops_in(1, 10, 14));
  if (G >= 20 && G < 28) n = 2 * (27 - G) + ops_in(nb, 20, G);
  if (G >= 30 && G < 38) n = imin(n, (G - 9 < 32 ? nb - 1 : 0) + ops_in(nb, G - 8, G));
  return imin(n, 63);
}
template <int NB, int S> DEVI void g1_first(G1& g) {
  if constexpr (S < 20) {
    constexpr int c = S & 1, nx = (S + 1) % 20;
    constexpr bool Z = S == 0;
    g1_wait<g1_vm(NB, S), 4>(g, c, 0);
    if constexpr (S == 3 || S == 9 || S == 14) asm volatile("s_barrier" ::: "memory");
    { u64* st_ = g.st_; if constexpr (S == 1) STK(1, 4); if constexpr (S == 5) STK(1, 5); if constexpr (S == 10) STK(1, 6); if constexpr (S == 15) STK(1, 7); if constexpr (S == 19) STK(1, 8); }
    pa_step<S + 1>(g);
    const u4& b0 = g.ring[S & 7][0]; const u4& b1 = g.ring[S & 7][1];
    const u4 a0 = cat(g.al[c][0], g.ah[c][0]), a1 = cat(g.al[c][1], g.ah[c][1]);
    mmv<Z>(g.acc[0][0], a0, b0); rd_lo<nx, 0>(g);
    if constexpr (NB == 2) mmv<Z>(g.acc[1][0], a0, b1);
    rd_hi<nx, 0>(g);
    mmv<Z>(g.sx, a0, g.ones);
    if constexpr (NB == 1 && S < 10) a_dma<S + 10, 0>(g);
    mmv<Z>(g.acc[0][1], a1, b0); rd_lo<nx, 1>(g);
    if constexpr (NB == 2) mmv<Z>(g.acc[1][1], a1, b1);
    rd_hi<nx, 1>(g);
    mmv<Z>(g.sq, a0, a0);
    if constexpr (NB == 1 && S < 10) a_dma<S + 10, 1>(g);
    g1_wait<63, 4>(g, c, 2);
    const u4 a2 = cat(g.al[c][2], g.ah[c][2]), a3 = cat(g.al[c][3], g.ah[c][3]);
    mmv<Z>(g.acc[0][2], a2, b0); rd_lo<nx, 2>(g);
    if constexpr (NB == 2) mmv<Z>(g.acc[1][2], a2, b1);
    rd_hi<nx, 2>(g);
    mmv<Z>(g.acc[0][3], a3, b0); rd_lo<nx, 3>(g);
    b_next<S, 0>(g);
    if constexpr (NB == 2) mmv<Z>(g.acc[1][3], a3, b1);
    rd_hi<nx, 3>(g);
    if constexpr (NB == 2) b_next<S, 1>(g);
    g1_first<NB, S + 1>(g);
  }
}
template <int NB, int S, int X, int M> DEVI void mm1(G1& g) {
  if constexpr (X < NB) mmv<S == 0>(g.acc[X][M], g.ring[(S + 20) & 7][X], cat(g.al[S & 1][M], g.ah[S & 1][M]));
}
template <int NB, int S, int E> DEVI void g1_second(G1& g, const WS& x, u4 (&W)[16][4], unsigned tv, const char* tb) {
  if constexpr (S < E) {
    constexpr bool R = S < 19, WR = w_rd_at(S), WP = w_rd_at(S - 1);
    constexpr int T = w_tile(S), TD = w_tile(S - 1) + 8;
    g1_wait<g1_vm(NB, S + 20), (WP ? 5 : 4)>(g, S & 1, 0);
    { u64* st_ = g.st_; if constexpr (S == 0) STK(2, 1); if constexpr (S == 5) STK(2, 2); if constexpr (S == 10) STK(2, 5); if constexpr (S == 15) STK(2, 6); if constexpr (S == 19) STK(2, 7); }
    if constexpr (R) pa_step<S + 1>(g);
    if constexpr (NB == 1 && S == 0) { gld4<0>(g.touch[0], tv, tb); gld4<0>(g.touch[1], tv, tb + 45 * 128); gld4<0>(g.touch[2], tv, tb + 90 * 128); }
    mm1<NB, S, 0, 0>(g); if constexpr (R) rd_lo<S + 1, 0>(g);
    mm1<NB, S, 1, 0>(g); if constexpr (R) rd_hi<S + 1, 0>(g);
    if constexpr (WR) w_read<(T >> 2), (T & 3), 0>(x, W);
    mm1<NB, S, 0, 1>(g); if constexpr (R) rd_lo<S + 1, 1>(g);
    mm1<NB, S, 1, 1>(g); if constexpr (R) rd_hi<S + 1, 1>(g);
    g1_wait<63, (R ? (WR ? 5 : 4) : 0)>(g, S & 1, 2);
    mm1<NB, S, 0, 2>(g); if constexpr (R) rd_lo<S + 1, 2>(g);
    mm1<NB, S, 1, 2>(g); if constexpr (R) rd_hi<S + 1, 2>(g);
    if constexpr (WR) w_read<(T >> 2), (T & 3), 1>(x, W);
    mm1<NB, S, 0, 3>(g); if constexpr (R) rd_lo<S + 1, 3>(g);
    b_next<S + 20, 0>(g);
    if constexpr (w_dma_at(S)) w_dma<(TD >> 2), (TD & 3)>(x);
    mm1<NB, S, 1, 3>(g); if constexpr (R) rd_hi<S + 1, 3>(g);
    if constexpr (NB == 2) b_next<S + 20, 1>(g);
    g1_second<NB, S + 1, E>(g, x, W, tv, tb);
  }
}
template <int NB, int KS, int E> DEVI void g1_pro(G1& g) {
  if constexpr (KS < E) { ld_new<KS, 0>(g, g.ring[KS][0], g.b2); if constexpr (NB == 2) ld_new<KS, 1>(g, g.ring[KS][1], g.b2); g1_pro<NB, KS + 1, E>(g); }
}
#define FOLD2(CT, MA, MB, H) \
  "v_add_f32_dpp %0, %0, %0 " CT " row_mask:0xf bank_mask:" MA "\n\tv_add_f32_dpp %1, %1, %1 " CT " row_mask:0xf bank_mask:" MA "\n\t" \
  "v_add_f32_dpp %2, %2, %2 " CT " row_mask:0xf bank_mask:" MA "\n\tv_add_f32_dpp %3, %3, %3 " CT " row_mask:0xf bank_mask:" MA "\n\t" \
  "v_add_f32_dpp %0, %4, %4 " CT " row_mask:0xf bank_mask:" MB "\n\tv_add_f32_dpp %1, %5, %5 " CT " row_mask:0xf bank_mask:" MB "\n\t" \
  "v_add_f32_dpp %2, %6, %6 " CT " row_mask:0xf bank_mask:" MB "\n\tv_add_f32_dpp %3, %7, %7 " CT " row_mask:0xf bank_mask:" MB
DEVI void fold16(float (&v)[16]) {
  asm volatile("s_nop 1\n\t" FOLD2("row_mirror", "0x3", "0xc", 8) : "+v"(v[0]), "+v"(v[1]), "+v"(v[2]), "+v"(v[3]) : "v"(v[8]), "v"(v[9]), "v"(v[10]), "v"(v[11]));
  asm volatile("s_nop 1\n\t" FOLD2("row_mirror", "0x3", "0xc", 8) : "+v"(v[4]), "+v"(v[5]), "+v"(v[6]), "+v"(v[7]) : "v"(v[12]), "v"(v[13]), "v"(v[14]), "v"(v[15]));
  asm volatile(FOLD2("row_half_mirror", "0x5", "0xa", 4) "\n\t"
               "v_add_f32_dpp %0, %0, %0 quad_perm:[2,3,0,1] row_mask:0xf bank_mask:0xf\n\tv_add_f32_dpp %1, %1, %1 quad_perm:[2,3,0,1] row_mask:0xf bank_mask:0xf\n\t"
               "v_add_f32_dpp %2, %2, %2 quad_perm:[2,3,0,1] row_mask:0xf bank_mask:0xf\n\tv_add_f32_dpp %3, %3, %3 quad_perm:[2,3,0,1] row_mask:0xf bank_mask:0xf\n\t"
               "v_add_f32_dpp %0, %0, %0 quad_perm:[1,0,3,2] row_mask:0xf bank_mask:0xf\n\tv_add_f32_dpp %1, %1, %1 quad_perm:[1,0,3,2] row_mask:0xf bank_mask:0xf\n\t"
               "v_add_f32_dpp %2, %2, %2 quad_perm:[1,0,3,2] row_mask:0xf bank_mask:0xf\n\tv_add_f32_dpp %3, %3, %3 quad_perm:[1,0,3,2] row_mask:0xf bank_mask:0xf"
               : "+v"(v[0]), "+v"(v[1]), "+v"(v[2]), "+v"(v[3]) : "v"(v[4]), "v"(v[5]), "v"(v[6]), "v"(v[7]));
}

struct G2 {
  u4 z[4][7];
  f4 acc[7][4];
  unsigned vz, vz6, slot[3];
  const char *zsrc, *wo, *wo6;
};
template <int S, int FB> DEVI void ld_z(G2& g) {
  const char* sb = g.zsrc + (size_t)S * (NGB * 1024);
  if constexpr (S < 4) gld16s<(FB - 3) * 1024>(g.z[S][FB], FB == 6 ? g.vz6 : g.vz, sb);
  else rld16s<(FB - 3) * 1024>(g.z[S & 3][FB], FB == 6 ? g.vz6 : g.vz, sb);
}
template <int S, int FB> DEVI void ld_z_all(G2& g) { if constexpr (S < 4) { ld_z<S, FB>(g); if constexpr (FB < 6) ld_z_all<S, FB + 1>(g); else ld_z_all<S + 1, 0>(g); } }
template <int N> DEVI void ld_wo(G2& g, u4 (&WO)[7][5]) {
  constexpr int KS = N / 5, DB = N % 5;
  if constexpr (KS < 6) gld16a<(KS - 3) * 1024>(WO[KS][DB], g.vz, g.wo + DB * (NKS3 * 1024));
  else gld16a<0>(WO[KS][DB], g.vz, g.wo6 == nullptr ? g.wo + DB * (NKS3 * 1024) + 3072 : g.wo6);
}
template <int S, int FB> DEVI void g2_main(G2& g, u4 (&W)[16][4], u4 (&WO)[7][5], u64* st_) {
  if constexpr (S < 14) {
    if constexpr (S == 12 && FB == 0) asm volatile("s_waitcnt lgkmcnt(0)" ::: "memory");
    waitv<27>();
    if constexpr (FB == 0) { if constexpr (S == 0) { STK(0, 5); STK(4, 1); } if constexpr (S == 2) STK(4, 2); if constexpr (S == 4) STK(4, 4); if constexpr (S == 6) STK(4, 5); if constexpr (S == 8) STK(4, 6); if constexpr (S == 10) STK(4, 7); if constexpr (S == 13) STK(4, 8); }
    mmva<S == 0>(g.acc[FB][0], g.z[S & 3][FB], W[S][0]); mmva<S == 0>(g.acc[FB][1], g.z[S & 3][FB], W[S][1]);
    mmva<S == 0>(g.acc[FB][2], g.z[S & 3][FB], W[S][2]); mmva<S == 0>(g.acc[FB][3], g.z[S & 3][FB], W[S][3]);
    if constexpr (S < 12) ld_z<S + 4, FB>(g); else ld_wo<7 * (S - 12) + FB>(g, WO);
    if constexpr (FB < 6) g2_main<S, FB + 1>(g, W, WO, st_); else g2_main<S + 1, 0>(g, W, WO, st_);
  }
}
template <int GI, int K> DEVI void g2_tail(G2& g, u4 (&W)[16][4], u4 (&WO)[7][5]) {
  if constexpr (GI < 4) {
    constexpr int m = (GI + 1) & 3, s = 14 + K / 7, fb = K % 7;
    if constexpr (GI == 0 && K == 0) waitv<21>();
    if constexpr (GI == 0 && K == 7) waitv<14>();
    mmva<false>(g.acc[fb][m], g.z[s & 3][fb], W[s][m]);
    if constexpr (GI > 0 && K >= 1 && K < 8) ldsw16<(K - 1) * 1024>(g.slot[GI - 1], g.acc[K - 1][GI]);
    if constexpr (K >= 8 && 14 + 6 * GI + K - 8 < 35) ld_wo<14 + 6 * GI + K - 8>(g, WO);
    if constexpr (K < 13) g2_tail<GI, K + 1>(g, W, WO); else g2_tail<GI + 1, 0>(g, W, WO);
  }
}
struct G3 { u4 v[4][7]; f4 acc[5][4]; };
constexpr int V_PRE = 14;
template <int N, int E> DEVI void ld_v(G3& g, unsigned vo, unsigned vo6, const char* const (&vb)[4]) {
  if constexpr (N < E && N < 28) { gld16s<(N % 7 - 3) * 1024>(g.v[N / 7][N % 7], N % 7 == 6 ? vo6 : vo, vb[(N / 7 + 1) & 3]); ld_v<N + 1, E>(g, vo, vo6, vb); }
}
template <int N> DEVI void g3_loop(G3& g, u4 (&WO)[7][5], const unsigned (&slot)[3], unsigned vo, unsigned vo6, const char* const (&vb)[4], u64* st_) {
  if constexpr (N < 28) {
    constexpr int GI = N / 7, s = N % 7, m = (GI + 1) & 3;
    waitv<imin(V_PRE + 2 * N, 28) - 1 - N>();
    if constexpr (N == 0) STK(0, 8);
    mmav<s == 0>(g.acc[0][m], WO[s][0], g.v[GI][s]); mmav<s == 0>(g.acc[1][m], WO[s][1], g.v[GI][s]);
    ld_v<V_PRE + 2 * N, V_PRE + 2 * N + 1>(g, vo, vo6, vb);
    mmav<s == 0>(g.acc[2][m], WO[s][2], g.v[GI][s]); mmav<s == 0>(g.acc[3][m], WO[s][3], g.v[GI][s]);
    ld_v<V_PRE + 2 * N + 1, V_PRE + 2 * N + 2>(g, vo, vo6, vb);
    mmav<s == 0>(g.acc[4][m], WO[s][4], g.v[GI][s]);
    if constexpr (GI > 0 && s >= 1 && s < 6) ldsw16<(s - 1) * 1024>(slot[GI - 1], g.acc[s - 1][GI]);
    g3_loop<N + 1>(g, WO, slot, vo, vo6, vb, st_);
  }
}

struct Late { f4 acc[4]; float r[4], n[4]; unsigned o[4], rd[2]; u4 cs; };
DEVI void z1_block(const Late& t, char* sm) {
  const f2 c0 = {__uint_as_float(t.cs.x), __uint_as_float(t.cs.y)}, c1 = {__uint_as_float(t.cs.z), __uint_as_float(t.cs.w)};
  u2 y[4];
#pragma unroll
  for (int m = 0; m < 4; ++m) {
    const f2 y0 = gelu2((f2){t.acc[m][0], t.acc[m][1]} * t.r[m] + c0 * t.n[m]), y1 = gelu2((f2){t.acc[m][2], t.acc[m][3]} * t.r[m] + c1 * t.n[m]);
    y[m] = (u2){pk_bf(y0.x, y0.y), pk_bf(y1.x, y1.y)};
  }
#pragma unroll
  for (int m = 0; m < 4; ++m) *(u2*)(sm + t.o[m]) = y[m];
}
template <int NB> DEVI void phase1(char* sm, char* ws, const int i, const int j, const int w, const int gs, const int cnt, u4 (&W)[16][4], Late& late, const u16* wsp, u64 ta, u64 tb_, u64* st_) {
  const int lane = fresh_lane();
  const int c = lane & 15, g4 = lane >> 4;
  const bool liveA = 2 * w < cnt;
  const int ga = gs + (liveA ? 2 * w : cnt - 1), gb = ga + 1;
  const unsigned sb = lds_addr(sm);
  G1 g;
  g.st_ = st_;
  g.vb[0] = (unsigned)ga * (NKS1 * 1024) + lane * 16; g.vb[1] = g.vb[0] + NKS1 * 1024;
  g.b2 = ws + OFF_WF; g.b1 = ws + OFF_WF + (size_t)NGB * NKS1 * 1024;
  unsigned cs2[2]; u4 cs1[2];
  {
    const int r8 = lane >> 3, sg = (r8 >> 1) & 1;
    g.va[0] = r8 * 128 + (((lane & 7) ^ (2 * sg)) * 16); g.va[1] = r8 * 128 + (((lane & 7) ^ (2 * sg + 4)) * 16);
    g.sb = sb; g.hsrc = ws + OFF_HT + (size_t)i * 81920;
    const unsigned vo = w & 1 ? g.va[1] : g.va[0];
#pragma unroll
    for (int n = 0; n < 4; ++n) dma16(sb + (w + 4 * n) * 1024, vo, g.hsrc + (w + 4 * n) * 1024);
#pragma unroll
    for (int x = 0; x < NB; ++x) { gld4<0>(cs2[x], (FH + 16 * (ga + x) + c) * 4, ws + OFF_CS); gld16<0>(cs1[x], (16 * (ga + x) + 4 * g4) * 4, ws + OFF_CS); }
    g1_pro<NB, 0, 4>(g);
    STK(5, 1);
#pragma unroll
    for (int n = 4; n < 10; ++n) dma16(sb + (w + 4 * n) * 1024, vo, g.hsrc + (w + 4 * n) * 1024);
    g1_pro<NB, 4, 8>(g);
    if constexpr (NB == 1) {
      ta += (lane >> 3) * (NKS3 * 1024) + (lane & 7) * 128; tb_ += (lane & 31) * (4 * D);
      const u64 tp = tb_ + ((ta - tb_) & (0ull - (lane < 32)));
      asm volatile("global_load_dword %0, %1, off sc1" : "=v"(g.touch[3]) : "v"(tp));
    }
  }
  STK(5, 4);
  WS x;
  w_setup(x, lane, w, i, sb, wsp);
  {
    const int qq = c >> 2, p = c & 3, sh = (qq >> 1) | ((g4 & 1) << 1);
#pragma unroll
    for (int m = 0; m < 4; ++m) {
      g.pa[m] = sb + (8 * g4 + qq) * 128 + (p >> 1) * 16 + 8 * (p & 1) + (m ^ w ^ sh) * 32;
    }
  }
  g.ones = (u4){0x3f803f80u, 0x3f803f80u, 0x3f803f80u, 0x3f803f80u};
  unsigned aq[4], al[4], zo[2], z1o[4], po, so;
#pragma unroll
  for (int m = 0; m < 4; ++m) { aq[m] = KB_S2 + (m ^ w) * 64 + g4 * 16; al[m] = KB_S2 + (m ^ w) * 64 + c * 4; z1o[m] = KB_Z1 + ((m ^ w) * 7 + 2 * w) * 512 + lane * 8; }
  zo[0] = (unsigned)(w >> 1) * (NGB * 1024) + lane * 16; zo[1] = (unsigned)(1 ^ (w >> 1)) * (NGB * 1024) + lane * 16;
  po = KB_Z1 + (w * 64 + 16 * ((c >> 2) ^ w) + 4 * g4) * 4; so = KB_S2 + (16 * w + c) * 4;
  const char* zb = ws + OFF_Z2 + ((size_t)2 * i * NGB + ga) * 1024;
  u64* st = (u64*)(ws + OFF_ST) + (size_t)i * 512;
  asm volatile("" : "+v"(aq[0]), "+v"(aq[1]), "+v"(aq[2]), "+v"(aq[3]), "+v"(al[0]), "+v"(al[1]), "+v"(al[2]), "+v"(al[3]), "+v"(zo[0]), "+v"(zo[1]),
               "+v"(z1o[0]), "+v"(z1o[1]), "+v"(z1o[2]), "+v"(z1o[3]), "+v"(po), "+v"(so), "+v"(x.vw[0]), "+v"(x.vw[1]), "+v"(x.rd[0]), "+v"(x.rd[1]));
  asm volatile("" : "+s"(x.src[0]), "+s"(x.src[1]), "+s"(x.src[2]), "+s"(x.src[3]), "+s"(x.ring), "+s"(zb), "+s"(st));
  if (w == 0 && lane < 4) *(unsigned*)(sm + KB_CNT + 4 * lane) = 0;
  if constexpr (NB == 2) asm volatile("s_waitcnt vmcnt(22)" : "+v"(cs2[0]), "+v"(cs2[1]), "+v"(cs1[0]), "+v"(cs1[1]), "+v"(g.ones)::"memory");
  else asm volatile("s_waitcnt vmcnt(14)" : "+v"(cs2[0]), "+v"(cs1[0]), "+v"(g.ones)::"memory");
  STK(5, 5);
  bar();
  STK(0, 1); STK(1, 1); STK(5, 6);
  rd_lo<0, 0>(g); rd_hi<0, 0>(g); rd_lo<0, 1>(g); rd_hi<0, 1>(g); rd_lo<0, 2>(g); rd_hi<0, 2>(g); rd_lo<0, 3>(g); rd_hi<0, 3>(g);
  g1_first<NB, 0>(g);
  asm volatile("s_nop 1" : "+v"(g.sx), "+v"(g.sq));
  {
    const float sx = c & 2 ? (c & 1 ? g.sx[3] : g.sx[2]) : (c & 1 ? g.sx[1] : g.sx[0]);
    const float sq = c & 2 ? (c & 1 ? g.sq[3] : g.sq[2]) : (c & 1 ? g.sq[1] : g.sq[0]);
    const f2 st = ln_stats(sx, sq, 1.0f / D);
    if ((c >> 2) == g4) { *(float*)(sm + so) = st.y; *(float*)(sm + so + 256) = -st.x * st.y; }
  }
  bar();
  hold(g.acc[0][0], g.acc[0][1], g.acc[0][2], g.acc[0][3]);
  if constexpr (NB == 2) hold(g.acc[1][0], g.acc[1][1], g.acc[1][2], g.acc[1][3]);
  STK(0, 2); STK(1, 2);
  f2 z[2][8];
  float r1[4], n1[4];
  float* ps = (float*)(sm + KB_Z1); float* pq = ps + 256;
  {
    float sv[16], qv[16];
    f4 r4[4], n4[4];
#pragma unroll
    for (int m = 0; m < 4; ++m) { r4[m] = *(const f4*)(sm + aq[m]); n4[m] = *(const f4*)(sm + aq[m] + 256); }
#pragma unroll
    for (int m = 0; m < 4; ++m) { r1[m] = *(const float*)(sm + al[m]); n1[m] = *(const float*)(sm + al[m] + 256); }
#pragma unroll
    for (int m = 0; m < 4; ++m) {
#pragma unroll
      for (int e = 0; e < 2; ++e) {
        const f2 r = {r4[m][2 * e], r4[m][2 * e + 1]}, n = {n4[m][2 * e], n4[m][2 * e + 1]};
#pragma unroll
        for (int y = 0; y < NB; ++y) z[y][2 * m + e] = gelu2((f2){g.acc[y][m][2 * e], g.acc[y][m][2 * e + 1]} * r + n * __uint_as_float(cs2[y]));
        f2 s2 = z[0][2 * m + e], q2 = s2 * s2;
        if constexpr (NB == 2) { s2 += z[1][2 * m + e]; q2 += z[1][2 * m + e] * z[1][2 * m + e]; }
        sv[4 * m + 2 * e] = s2.x; sv[4 * m + 2 * e + 1] = s2.y; qv[4 * m + 2 * e] = q2.x; qv[4 * m + 2 * e + 1] = q2.y;
        __builtin_amdgcn_sched_barrier(0);
        w_dma_rt(x, (2 * m + e) >> 2, (2 * m + e) & 3);
        __builtin_amdgcn_sched_barrier(0);
      }
    }
    fold16(sv); fold16(qv);
    if ((c & 3) == 0) {
      *(f4*)(sm + po) = liveA ? (f4){sv[0], sv[1], sv[2], sv[3]} : (f4){0.f, 0.f, 0.f, 0.f};
      *(f4*)(sm + po + 1024) = liveA ? (f4){qv[0], qv[1], qv[2], qv[3]} : (f4){0.f, 0.f, 0.f, 0.f};
    }
    if (last_of_four(sb + KB_CNT)) {
      const float ts = (ps[lane] + ps[64 + lane]) + (ps[128 + lane] + ps[192 + lane]), tq = (pq[lane] + pq[64 + lane]) + (pq[128 + lane] + pq[192 + lane]);
      __hip_atomic_store(st + j * 64 + lane, ((u64)(__float_as_uint(tq) | 0x80000000u) << 32) | __float_as_uint(ts), __ATOMIC_RELAXED, __HIP_MEMORY_SCOPE_AGENT);
    }
  }
  asm volatile("" : "+v"(r1[0]), "+v"(r1[1]), "+v"(r1[2]), "+v"(r1[3]), "+v"(n1[0]), "+v"(n1[1]), "+v"(n1[2]), "+v"(n1[3]));
  ST(3);
  const unsigned tv = (lane < 45 ? lane : 44) * 128;
  const char* tb = ws + OFF_WO + (size_t)5 * j * NKS3 * 1024 + (i & 7) * 135 * 128;
  g1_second<NB, 0, KSPLIT>(g, x, W, tv, tb);
  if constexpr (NB == 1) asm volatile("" : "+v"(g.touch[0]), "+v"(g.touch[1]), "+v"(g.touch[2]), "+v"(g.touch[3]));
  float* rs3 = (float*)(sm + KB_S3); float* nm3 = rs3 + 64;
  STK(2, 8);
  if constexpr (NB == 1) {
    u2 gv[8]; unsigned n;
    asm volatile(
        "s_mov_b32 %8, 0x40000\n"
        "1:\n\tglobal_load_dwordx2 %0, %9, %10 sc1\n\tglobal_load_dwordx2 %1, %9, %10 offset:512 sc1\n\tglobal_load_dwordx2 %2, %9, %10 offset:1024 sc1\n\t"
        "global_load_dwordx2 %3, %9, %10 offset:1536 sc1\n\tglobal_load_dwordx2 %4, %9, %10 offset:2048 sc1\n\tglobal_load_dwordx2 %5, %9, %10 offset:2560 sc1\n\t"
        "global_load_dwordx2 %6, %9, %10 offset:3072 sc1\n\tglobal_load_dwordx2 %7, %9, %10 offset:3584 sc1\n\ts_waitcnt vmcnt(0)\n\t"
        "v_cmp_gt_i64 vcc, 0, %0\n\tv_cmp_gt_i64 s[44:45], 0, %1\n\tv_cmp_gt_i64 s[46:47], 0, %2\n\tv_cmp_gt_i64 s[48:49], 0, %3\n\t"
        "s_and_b64 vcc, vcc, s[44:45]\n\ts_and_b64 s[46:47], s[46:47], s[48:49]\n\ts_and_b64 s[50:51], vcc, s[46:47]\n\t"
        "v_cmp_gt_i64 vcc, 0, %4\n\tv_cmp_gt_i64 s[44:45], 0, %5\n\tv_cmp_gt_i64 s[46:47], 0, %6\n\tv_cmp_gt_i64 s[48:49], 0, %7\n\t"
        "s_and_b64 vcc, vcc, s[44:45]\n\ts_and_b64 s[46:47], s[46:47], s[48:49]\n\ts_and_b64 vcc, vcc, s[46:47]\n\ts_and_b64 vcc, vcc, s[50:51]\n\t"
        "s_sub_u32 %8, %8, 1\n\ts_cbranch_scc1 2f\n\ts_cmp_eq_u64 vcc, -1\n\ts_cbranch_scc1 2f\n\ts_sleep 1\n\ts_branch 1b\n2:"
        : "=&v"(gv[0]), "=&v"(gv[1]), "=&v"(gv[2]), "=&v"(gv[3]), "=&v"(gv[4]), "=&v"(gv[5]), "=&v"(gv[6]), "=&v"(gv[7]), "=&s"(n)
        : "v"(lane * 8), "s"(st)
        : "vcc", "scc", "s44", "s45", "s46", "s47", "s48", "s49", "s50", "s51", "memory");
    float ts = 0.f, tq = 0.f;
#pragma unroll
    for (int v = 0; v < 8; ++v) { ts += __uint_as_float(gv[v].x); tq += __uint_as_float(gv[v].y & 0x7fffffffu); }
    const f2 s3 = ln_stats(ts, tq, 1.0f / FH);
    rs3[lane] = s3.y; nm3[lane] = -s3.x * s3.y;
  }
  bar();
  STK(0, 4); STK(2, 4); STK(3, 4);
  if (liveA) {
    unsigned o[2][8];
#pragma unroll
    for (int m = 0; m < 4; ++m) {
      const f4 r4 = *(const f4*)(sm + aq[m] + 512), n4 = *(const f4*)(sm + aq[m] + 768);
#pragma unroll
      for (int e = 0; e < 2; ++e) {
        const f2 r = {r4[2 * e], r4[2 * e + 1]}, n = {n4[2 * e], n4[2 * e + 1]};
#pragma unroll
        for (int x = 0; x < NB; ++x) { const f2 a = z[x][2 * m + e] * r + n; o[x][2 * m + e] = pk_bf(a.x, a.y); }
      }
    }
    const unsigned r0 = zo[0], r1 = zo[1];
    if (w & 1) {
#pragma unroll
      for (int x = 0; x < NB; ++x) { gst16s(r0, (u4){o[x][2], o[x][3], o[x][0], o[x][1]}, zb + x * 1024); gst16s(r1, (u4){o[x][6], o[x][7], o[x][4], o[x][5]}, zb + x * 1024); }
    } else {
#pragma unroll
      for (int x = 0; x < NB; ++x) { gst16s(r0, (u4){o[x][0], o[x][1], o[x][2], o[x][3]}, zb + x * 1024); gst16s(r1, (u4){o[x][4], o[x][5], o[x][6], o[x][7]}, zb + x * 1024); }
    }
  }
  __builtin_amdgcn_sched_barrier(0);
  STK(3, 1);
  g1_second<NB, KSPLIT, 20>(g, x, W, tv, tb);
  STK(3, 2);
  waitv<ops_in(1, KSPLIT + 20, 40)>();
  if (last_of_four(sb + KB_CNT + 4)) {
    if (lane == 0) __hip_atomic_store((unsigned*)(ws + OFF_F2) + (32 * j + i) * 32, 1u, __ATOMIC_RELAXED, __HIP_MEMORY_SCOPE_AGENT);
  }
  STK(3, 5);
  settle(g.acc[0][0], g.acc[0][1], g.acc[0][2], g.acc[0][3]);
#pragma unroll
  for (int m = 0; m < 4; ++m) { late.acc[m] = g.acc[0][m]; late.r[m] = r1[m]; late.n[m] = n1[m]; late.o[m] = z1o[m]; }
  late.cs = cs1[0];
  if constexpr (NB == 2) {
    z1_block(late, sm);
    hold(g.acc[1][0], g.acc[1][1], g.acc[1][2], g.acc[1][3]);
#pragma unroll
    for (int m = 0; m < 4; ++m) { late.acc[m] = g.acc[1][m]; late.o[m] += 512; }
    late.cs = cs1[1];
  }
  __builtin_amdgcn_sched_barrier(0);
  STK(3, 6);
  waitv<0>();
  w_read_all<4>(x, W); w_read_all<5>(x, W);
  asm volatile("s_waitcnt lgkmcnt(8)" ::: "memory");
  w_dma_all<6>(x);
  asm volatile("s_waitcnt lgkmcnt(0)" ::: "memory");
  w_dma_all<7>(x);
  late.rd[0] = x.rd[0]; late.rd[1] = x.rd[1];
  STK(3, 7);
  poll8((lane & 7) * 128, ws + OFF_F2 + (32 * j + 8 * w) * 128);
  STK(3, 8);
}

__global__ __launch_bounds__(256, 1) void kb(const u16* __restrict__ wsp, const u16* __restrict__ bsp, char* __restrict__ ws, u16* __restrict__ out) {
  __shared__ __attribute__((aligned(16))) char sm[KB_LDS];
  const int w = __builtin_amdgcn_readfirstlane(threadIdx.x >> 6);
  int i, j; tile_of(blockIdx.x, i, j);
  const int gs = j < 7 ? 7 * j : 48, cnt = j < 6 ? 7 : 6;
  const unsigned sb = lds_addr(sm);
  const int hz = j < 4 ? 40960 : 0;
  ST_DECL
  u4 W[16][4];
  Late late;
  const char* vst = ws + OFF_V + ((size_t)4 * i * NKS3 + (gs >> 1)) * 1024;
  u64 ta = (u64)vst + (0x15 >> j & 1) * 3072, tb = (u64)(out + 64 * i * D + 80 * j);
  asm volatile("" : "+s"(ta), "+s"(tb));
  if (w < 3) phase1<2>(sm, ws, i, j, w, gs, cnt, W, late, wsp, ta, tb, st_); else phase1<1>(sm, ws, i, j, w, gs, cnt, W, late, wsp, ta, tb, st_);
  G2 h;
  unsigned bias;
  {
    const int ln = fresh_lane(), c = ln & 15;
    h.vz = ln * 16 + 3072; h.vz6 = cnt == 7 ? h.vz : h.vz - 1024;
    gld2(bias, (64 * i + 32 * (w >> 1) + 8 * (c >> 2) + 4 * (w & 1) + (c & 3)) * 2, bsp);
  }
  h.zsrc = ws + OFF_Z2 + ((size_t)16 * w * NGB + gs) * 1024;
  h.wo = ws + OFF_WO + ((size_t)5 * j * NKS3 + 7 * w) * 1024;
  h.wo6 = w == 3 ? ws + OFF_ZE - 3072 : nullptr;
  ld_z_all<0, 0>(h);
  {
    WS x;
    x.rd[0] = late.rd[0]; x.rd[1] = late.rd[1];
    w_read_all<6>(x, W); w_read_all<7>(x, W);
  }
  __builtin_amdgcn_sched_barrier(0);
  z1_block(late, sm);
  __builtin_amdgcn_sched_barrier(0);
  u4 WO[7][5];
  g2_main<0, 0>(h, W, WO, st_);
  const int l2 = fresh_lane();
  const char* rd[3];
#pragma unroll
  for (int m = 1; m < 4; ++m) {
    h.slot[m - 1] = sb + (m < 3 ? KB_RED + w * 16384 + (m - 1) * KB_SLOT : hz + w * KB_SLOT) + l2 * 16;
    rd[m - 1] = sm + (m < 3 ? KB_RED + (w ^ m) * 16384 + (m - 1) * KB_SLOT : hz + (w ^ m) * KB_SLOT) + l2 * 16;
  }
  bar();
  g2_tail<0, 0>(h, W, WO);
  asm volatile("s_nop 7" : "+v"(h.acc[0][0]), "+v"(h.acc[1][0]), "+v"(h.acc[2][0]), "+v"(h.acc[3][0]), "+v"(h.acc[4][0]), "+v"(h.acc[5][0]), "+v"(h.acc[6][0]));
  bar();
  STK(0, 6);
  {
    pin(bias);
    const float bb = bflo(bias);
    const unsigned vl = l2 * 16;
    u2 vv[7];
#pragma unroll
    for (int fb = 0; fb < 7; ++fb) {
      const f4 sum = ((h.acc[fb][0] + *(const f4*)(rd[0] + fb * 1024)) + *(const f4*)(rd[1] + fb * 1024)) + *(const f4*)(rd[2] + fb * 1024);
      const u2 z1 = *(const u2*)(sm + KB_Z1 + (w * 7 + fb) * 512 + l2 * 8);
      const unsigned s0 = pk_bf(sum[0], sum[1]), s1 = pk_bf(sum[2], sum[3]);
      const unsigned g0 = pk_bf(bflo(s0) + bb, bfhi(s0) + bb), g1 = pk_bf(bflo(s1) + bb, bfhi(s1) + bb);
      vv[fb] = (u2){pk_bf(bflo(g0) * bflo(z1.x), bfhi(g0) * bfhi(z1.x)), pk_bf(bflo(g1) * bflo(z1.y), bfhi(g1) * bfhi(z1.y))};
    }
    const char* vb = vst + w * (NKS3 * 1024);
    if (gs & 1) {
      asm volatile("global_store_dwordx2 %0, %1, %2 offset:8 sc1" ::"v"(vl), "v"(vv[0]), "s"(vb) : "memory");
#pragma unroll
      for (int p = 0; p < 3; ++p) gst16s(vl + 1024 * (p + 1), cat(vv[2 * p + 1], vv[2 * p + 2]), vb);
    } else {
#pragma unroll
      for (int p = 0; p < 3; ++p) gst16s(vl + 1024 * p, cat(vv[2 * p], vv[2 * p + 1]), vb);
      if (cnt == 7) asm volatile("global_store_dwordx2 %0, %1, %2 offset:3072 sc1" ::"v"(vl), "v"(vv[6]), "s"(vb) : "memory");
    }
  }
  waitv<0>();
  if (last_of_four(sb + KB_CNT + 8)) {
    if (l2 == 0) __hip_atomic_store((unsigned*)(ws + OFF_F3) + (8 * i + j) * 32, 1u, __ATOMIC_RELAXED, __HIP_MEMORY_SCOPE_AGENT);
  }
  STK(0, 7);
  G3 o;
  const char* vb[4];
#pragma unroll
  for (int m = 0; m < 4; ++m) vb[m] = ws + OFF_V + ((size_t)(4 * i + (m ^ w)) * NKS3 + 7 * w) * 1024;
  poll8((l2 & 7) * 128, ws + OFF_F3 + i * 1024);
  const unsigned vz6 = w == 3 ? h.vz - 1024 : h.vz;
  ld_v<0, V_PRE>(o, h.vz, vz6, vb);
  g3_loop<0>(o, WO, h.slot, h.vz, vz6, vb, st_);
  asm volatile("s_nop 7" : "+v"(o.acc[0][0]), "+v"(o.acc[1][0]), "+v"(o.acc[2][0]), "+v"(o.acc[3][0]), "+v"(o.acc[4][0]));
  bar();
  const int c3 = l2 & 15, g3 = l2 >> 4;
  const int trow3 = 64 * i + 32 * (w >> 1) + 8 * (c3 >> 2) + 4 * (w & 1) + (c3 & 3);
  const char* hp = sm + (80 * j + 4 * g3 + (c3 >> 2)) * 128 + (((2 * w + ((c3 >> 1) & 1)) ^ (2 * (((c3 >> 3) & 1) | ((g3 >> 1) << 1)))) * 16) + 8 * (c3 & 1);
#pragma unroll
  for (int db = 0; db < 5; ++db) {
    const f4 sum = ((o.acc[db][0] + *(const f4*)(rd[0] + db * 1024)) + *(const f4*)(rd[1] + db * 1024)) + *(const f4*)(rd[2] + db * 1024);
    const u2 hv = __builtin_bit_cast(u2, __builtin_amdgcn_ds_read_tr16_b64_v4i16((LDSP s4*)(hp + db * 2048)));
    const unsigned s0 = pk_bf(sum[0], sum[1]), s1 = pk_bf(sum[2], sum[3]);
    *(u2*)(out + (size_t)trow3 * D + 80 * j + 16 * db + 4 * g3) =
        (u2){pk_bf(bflo(hv.x) + bflo(s0), bfhi(hv.x) + bfhi(s0)), pk_bf(bflo(hv.y) + bflo(s1), bfhi(hv.y) + bfhi(s1))};
  }
  ST_END(1);
}

extern "C" void kernel_launch(void* const* d_in, const int* in_sizes, int n_in, void* d_out, int out_size, void* d_ws, size_t ws_size,
                              hipStream_t stream) {
  char* ws = (char*)d_ws;
  ka<<<256, 1024, 0, stream>>>((const u16*)d_in[0], (const u16*)d_in[1], (const u16*)d_in[2], (const u16*)d_in[3], (const u16*)d_in[4],
                              (const unsigned*)d_in[5], (const unsigned*)d_in[6], (const u16*)d_in[8], (const u16*)d_in[9], (const u16*)d_in[11], ws);
  kb<<<256, 256, 0, stream>>>((const u16*)d_in[9], (const u16*)d_in[10], ws, (u16*)d_out);
}
```

```cpp
#include <hip/hip_runtime.h>

typedef unsigned short u16;
typedef unsigned long long u64;
typedef __attribute__((ext_vector_type(8))) __bf16 bf8;
typedef __attribute__((ext_vector_type(2))) __bf16 bf2;
typedef __attribute__((ext_vector_type(8))) _Float16 h8;
typedef __attribute__((ext_vector_type(2))) _Float16 h2;
typedef __attribute__((ext_vector_type(4))) float f4;
typedef __attribute__((ext_vector_type(2))) float f2;
typedef __attribute__((ext_vector_type(4))) short s4;
typedef __attribute__((ext_vector_type(4))) unsigned u4;
typedef __attribute__((ext_vector_type(2))) unsigned u2;
#define LDSP __attribute__((address_space(3)))
#define DEVI __device__ __forceinline__
#define ST_DECL u64* st_ = nullptr;
#define ST(i)
#define ST_END(kid)

constexpr int L = 2048, D = 640, NM = 128, F = 1728, FH = 864;
constexpr int NGB = 54;
constexpr int NKS1 = 20;
constexpr int NKS3 = 27;

constexpr size_t OFF_UT = 0;
constexpr size_t OFF_XT = OFF_UT + (size_t)D * L * 2;
constexpr size_t OFF_KR = OFF_XT + (size_t)D * L * 2;
constexpr size_t OFF_HT = OFF_KR + (size_t)D * L * 4;
constexpr size_t OFF_WF = OFF_HT + (size_t)D * L * 2;
constexpr size_t OFF_WO = OFF_WF + (size_t)2 * NGB * NKS1 * 1024;
constexpr size_t OFF_Z1 = OFF_WO + (size_t)40 * NKS3 * 1024;
constexpr size_t OFF_Z2 = OFF_Z1 + (size_t)128 * NGB * 512;
constexpr size_t OFF_V = OFF_Z2 + (size_t)64 * NGB * 1024;
constexpr size_t OFF_ST = OFF_V + (size_t)128 * NKS3 * 1024;
constexpr size_t OFF_CSP = OFF_ST + (size_t)32 * 8 * 64 * 8;
constexpr size_t OFF_CS = OFF_CSP + (size_t)5 * F * 4;
constexpr size_t OFF_KSC = OFF_CS + (size_t)F * 4;
constexpr size_t OFF_G1 = 32u << 20;
constexpr size_t OFF_F1 = OFF_G1 + 32768;
constexpr size_t OFF_EP = OFF_G1 + 65536;
constexpr size_t OFF_F2 = OFF_G1 + 131072;
constexpr size_t OFF_F3 = OFF_F2 + 32768;
constexpr size_t OFF_ZE = OFF_F3 + 32768;

DEVI float bflo(unsigned v) { return __uint_as_float(v << 16); }
DEVI float bfhi(unsigned v) { return __uint_as_float(v & 0xffff0000u); }
DEVI unsigned pk_bf(float a, float b) { f2 p = {a, b}; return __builtin_bit_cast(unsigned, __builtin_convertvector(p, bf2)); }
DEVI unsigned pk_h(float a, float b) { f2 p = {a, b}; return __builtin_bit_cast(unsigned, __builtin_convertvector(p, h2)); }
DEVI float rbf(float a) { return bflo(pk_bf(a, 0.f)); }
DEVI float hlo(unsigned v) { return (float)__builtin_bit_cast(h2, v).x; }
DEVI float hhi(unsigned v) { return (float)__builtin_bit_cast(h2, v).y; }
DEVI f4 mfma_bf(u4 a, u4 b, f4 c) { return __builtin_amdgcn_mfma_f32_16x16x32_bf16(__builtin_bit_cast(bf8, a), __builtin_bit_cast(bf8, b), c, 0, 0, 0); }
DEVI f4 mfma_h(u4 a, u4 b, f4 c) { return __builtin_amdgcn_mfma_f32_16x16x32_f16(__builtin_bit_cast(h8, a), __builtin_bit_cast(h8, b), c, 0, 0, 0); }
template <int CTRL> DEVI float dpp(float v) { return __int_as_float(__builtin_amdgcn_update_dpp(0, __float_as_int(v), CTRL, 0xf, 0xf, true)); }
constexpr int DPP_ROR1 = 0x121, DPP_ROR2 = 0x122, DPP_ROR4 = 0x124, DPP_ROR8 = 0x128, DPP_MIRROR = 0x140, DPP_HMIRROR = 0x141, DPP_Q3210 = 0x1B, DPP_Q1032 = 0xB1;

DEVI f2 ln_stats(float s, float q, float inv_n) {
  float m = s * inv_n;
  float v = fmaxf(q * inv_n - m * m, 0.f);
  float sd = rbf(__builtin_amdgcn_sqrtf(rbf(rbf(v) + 1e-5f)));
  f2 r = {rbf(m), __builtin_amdgcn_rcpf(sd)};
  return r;
}
DEVI float gelu_t(float x) {
  float t = __builtin_fmaf(x * x, -0.1029432397f, -2.3022081985f);
  float e = __builtin_amdgcn_exp2f(x * t);
  return x * __builtin_amdgcn_rcpf(1.0f + e);
}

template <int IMM> DEVI void gld16(u4& d, unsigned voff, const void* sb) { asm volatile("global_load_dwordx4 %0, %1, %2 offset:%3" : "=v"(d) : "v"(voff), "s"(sb), "i"(IMM)); }
template <int IMM> DEVI void gld8(u2& d, unsigned voff, const void* sb) { asm volatile("global_load_dwordx2 %0, %1, %2 offset:%3" : "=v"(d) : "v"(voff), "s"(sb), "i"(IMM)); }
template <int IMM> DEVI void gld4(unsigned& d, unsigned voff, const void* sb) { asm volatile("global_load_dword %0, %1, %2 offset:%3" : "=v"(d) : "v"(voff), "s"(sb), "i"(IMM)); }
DEVI void gld2(unsigned& d, unsigned voff, const void* sb) { asm volatile("global_load_ushort %0, %1, %2" : "=v"(d) : "v"(voff), "s"(sb)); }
DEVI void dma16(unsigned ldsdst, unsigned voff, const void* sb) {
  asm volatile("s_mov_b32 m0, %0\n\ts_nop 0\n\tglobal_load_lds_dwordx4 %1, %2" ::"s"(ldsdst), "v"(voff), "s"(sb) : "memory");
}
template <int N> DEVI void waitv() { asm volatile("s_waitcnt vmcnt(%0)" ::"i"(N) : "memory"); }
template <int N> DEVI void waitv(u4& a) { asm volatile("s_waitcnt vmcnt(%1)" : "+v"(a) : "i"(N) : "memory"); }
DEVI void pin(u4& a) { asm volatile("" : "+v"(a)); }
DEVI void pin(u2& a) { asm volatile("" : "+v"(a)); }
DEVI void pin(unsigned& a) { asm volatile("" : "+v"(a)); }
DEVI void bar() { asm volatile("s_waitcnt lgkmcnt(0)\n\ts_barrier" ::: "memory"); }
DEVI unsigned lds_addr(const void* p) { return (unsigned)(size_t)(LDSP const char*)p; }

DEVI void tr8x8(const u4 (&r)[8], u4 (&o)[8]) {
#pragma unroll
  for (int c2 = 0; c2 < 4; ++c2)
#pragma unroll
    for (int m = 0; m < 4; ++m) {
      o[2 * c2][m] = __builtin_amdgcn_perm(r[2 * m + 1][c2], r[2 * m][c2], 0x05040100u);
      o[2 * c2 + 1][m] = __builtin_amdgcn_perm(r[2 * m + 1][c2], r[2 * m][c2], 0x07060302u);
    }
}

constexpr int KG_PITCH = 528;
constexpr int KC_PITCH = 4160;
constexpr int RC_SZ = 30208;
constexpr int KA_K = 3 * RC_SZ, KA_U = KA_K + 12 * KC_PITCH, U_PITCH = 4608, KA_T = KA_U + 3 * U_PITCH, T_PITCH = 1536;
constexpr int KA_S = KA_T + 3 * T_PITCH, KA_LDS = KA_S + 96;

struct c2 { f2 x, y; };
DEVI c2 cmul(c2 a, c2 b) { c2 r; r.x = a.x * b.x - a.y * b.y; r.y = a.x * b.y + a.y * b.x; return r; }
DEVI c2 cexpp(f2 a2, f2 br, float p) {
  const f2 ea = a2 * p, ph = br * p;
  const f2 e = {__builtin_amdgcn_exp2f(ea.x), __builtin_amdgcn_exp2f(ea.y)};
  const float p0 = __builtin_amdgcn_fractf(ph.x), p1 = __builtin_amdgcn_fractf(ph.y);
  c2 r;
  r.x = e * (f2){__builtin_amdgcn_cosf(p0), __builtin_amdgcn_cosf(p1)};
  r.y = e * (f2){__builtin_amdgcn_sinf(p0), __builtin_amdgcn_sinf(p1)};
  return r;
}
DEVI unsigned pk_h2(f2 p) { return __builtin_bit_cast(unsigned, __builtin_convertvector(p, h2)); }
template <int CTRL, int RM> DEVI float dppm(float v) { return __int_as_float(__builtin_amdgcn_update_dpp(0, __float_as_int(v), CTRL, RM, 0xf, false)); }
DEVI float wave_sum63(float v) {
  v += dpp<DPP_ROR8>(v); v += dpp<DPP_ROR4>(v); v += dpp<DPP_ROR2>(v); v += dpp<DPP_ROR1>(v);
  v += dppm<0x142, 0xa>(v); v += dppm<0x143, 0xc>(v);
  return v;
}
template <int IMM> DEVI void lds8(u2& d, unsigned a) { asm volatile("ds_read_b64 %0, %1 offset:%2" : "=v"(d) : "v"(a), "i"(IMM)); }
template <int IMM> DEVI void lds16(u4& d, unsigned a) { asm volatile("ds_read_b128 %0, %1 offset:%2" : "=v"(d) : "v"(a), "i"(IMM)); }
template <int N> DEVI void waitl(u2& a, u2& b, u4& c) { asm volatile("s_waitcnt lgkmcnt(%3)" : "+v"(a), "+v"(b), "+v"(c) : "i"(N) : "memory"); }
template <int IMM> DEVI void gld16s(u4& d, unsigned voff, const void* sb) { asm volatile("global_load_dwordx4 %0, %1, %2 offset:%3 sc1" : "=v"(d) : "v"(voff), "s"(sb), "i"(IMM)); }
DEVI void gld4s(unsigned& d, unsigned voff, const void* sb) { asm volatile("global_load_dword %0, %1, %2 sc1" : "=v"(d) : "v"(voff), "s"(sb)); }
DEVI unsigned perm(unsigned hi, unsigned lo, unsigned sel) { return __builtin_amdgcn_perm(hi, lo, sel); }

template <int RR, int P> DEVI void conv_ld(u2 (&al)[8], u2 (&ah)[8], u4 (&bb)[8], unsigned pa, unsigned pb) {
  if constexpr (P < 8) {
    lds8<64 * (1 - RR) + 512 * (7 - P)>(al[P], pa);
    lds8<64 * (1 - RR) + 512 * (7 - P) + 8>(ah[P], pa);
    lds16<64 * (1 - RR) + 512 * P>(bb[P], pb);
    conv_ld<RR, P + 1>(al, ah, bb, pa, pb);
  }
}
template <int S> DEVI void conv_mm(u2 (&al)[8], u2 (&ah)[8], u4 (&bb)[8], f4 (&acc)[8]) {
  if constexpr (S < 8) {
    waitl<(3 * (7 - S) > 15 ? 15 : 3 * (7 - S))>(al[S], ah[S], bb[S]);
#pragma unroll
    for (int p = 0; p <= S; ++p) acc[S] = mfma_h((u4){al[p].x, al[p].y, ah[p].x, ah[p].y}, bb[S - p], acc[S]);
    __builtin_amdgcn_sched_barrier(0);
    conv_mm<S + 1>(al, ah, bb, acc);
  }
}
template <int IMM> DEVI void gld8s(u2& d, unsigned voff, const void* sb) { asm volatile("global_load_dwordx2 %0, %1, %2 offset:%3 sc1" : "=v"(d) : "v"(voff), "s"(sb), "i"(IMM)); }
DEVI void gst16s(unsigned voff, u4 v, const void* sb) { asm volatile("global_store_dwordx4 %0, %1, %2 sc1\n\ts_nop 1" ::"v"(voff), "v"(v), "s"(sb) : "memory"); }
DEVI u4 tr2(const char* p, int step) {
  const u2 a = __builtin_bit_cast(u2, __builtin_amdgcn_ds_read_tr16_b64_v4i16((LDSP s4*)p));
  const u2 b = __builtin_bit_cast(u2, __builtin_amdgcn_ds_read_tr16_b64_v4i16((LDSP s4*)(p + step)));
  return (u4){a.x, a.y, b.x, b.y};
}
template <bool TWO, int K> DEVI void tbl_ld(u4 (&a)[8], u4 (&c)[8], u4 (&e)[8], unsigned pa, unsigned pb) {
  if constexpr (K < 8) {
    lds16<64 * K>(a[K], pa); lds16<64 * K>(c[K], pb);
    if constexpr (TWO) lds16<64 * K + 16 * KG_PITCH>(e[K], pb);
    tbl_ld<TWO, K + 1>(a, c, e, pa, pb);
  }
}
template <bool TWO, int K> DEVI void tbl_mm(u4 (&a)[8], u4 (&c)[8], u4 (&e)[8], f4& acc, f4& ac0) {
  if constexpr (K < 8) {
    constexpr int N = (TWO ? 3 : 2) * (7 - K);
    if constexpr (TWO) asm volatile("s_waitcnt lgkmcnt(%3)" : "+v"(a[K]), "+v"(c[K]), "+v"(e[K]) : "i"(N > 15 ? 15 : N) : "memory");
    else asm volatile("s_waitcnt lgkmcnt(%2)" : "+v"(a[K]), "+v"(c[K]) : "i"(N) : "memory");
    acc = mfma_h(a[K], c[K], acc);
    if constexpr (TWO) ac0 = mfma_h(e[K], c[K], ac0);
    __builtin_amdgcn_sched_barrier(0);
    tbl_mm<TWO, K + 1>(a, c, e, acc, ac0);
  }
}
template <bool TWO> DEVI void tbl_tiles(unsigned pa, unsigned pb, f4& acc, f4& ac0) {
  u4 a[8], c[8], e[8];
  tbl_ld<TWO, 0>(a, c, e, pa, pb);
  tbl_mm<TWO, 0>(a, c, e, acc, ac0);
}
DEVI void chsync(unsigned fl, int wq, unsigned phase) {
  asm volatile("s_waitcnt lgkmcnt(0)\n\tds_write_b32 %0, %1" ::"v"(fl + 4 * wq), "v"(phase) : "memory");
  for (;;) {
    u4 v;
    asm volatile("ds_read_b128 %0, %1\n\ts_waitcnt lgkmcnt(0)" : "=v"(v) : "v"(fl) : "memory");
    if (__builtin_amdgcn_readfirstlane(min(min(v.x, v.y), min(v.z, v.w))) >= phase) break;
    __builtin_amdgcn_s_sleep(1);
  }
}
DEVI void rawbar() { asm volatile("s_barrier" ::: "memory"); }
template <int I> DEVI void poll_ld(u4 (&g)[4], unsigned go, const char* base) {
  if constexpr (I < 4) { gld16s<16 * I>(g[I], go, base); poll_ld<I + 1>(g, go, base); }
}
DEVI void wait4(u4 (&g)[4]) { asm volatile("s_waitcnt vmcnt(0)" : "+v"(g[0]), "+v"(g[1]), "+v"(g[2]), "+v"(g[3])::"memory"); }

constexpr int KA_CODE_LINES = 118, KB_CODE_LINES = 290;
__global__ __launch_bounds__(1024, 4) void ka(const u16* __restrict__ x, const u16* __restrict__ logA, const u16* __restrict__ Aim,
                                          const u16* __restrict__ Cre, const u16* __restrict__ Cim, const unsigned* __restrict__ logdt,
                                          const unsigned* __restrict__ dskip, const u16* __restrict__ w_in, const u16* __restrict__ wsp,
                                          const u16* __restrict__ w_out, char* __restrict__ ws) {
  __shared__ __attribute__((aligned(16))) char sm[KA_LDS];
  const int tid = threadIdx.x, lane = tid & 63;
  const int w = __builtin_amdgcn_readfirstlane(tid >> 6);
  const int b = blockIdx.x;
  ST_DECL
  const int g = b & 7, m = b >> 3;
  const bool isconv = m < 27;
  const int d0 = 80 * g + (3 * m < 77 ? 3 * m : 77);
  if (tid < 16) ((int*)(sm + KA_S + 32))[tid] = 0;
  unsigned tag = 0;
  if (w < 4) {
    u4 r0, r1; u2 r2;
    unsigned ep;
    const int row = 256 * g + 8 * m + 2 * w + (lane >> 5), tm = 4 * g + (m >> 3), tg = m & 7;
    const unsigned ro = (unsigned)row * 1280 + (lane & 31) * 16;
    gld4s(ep, 0, ws + OFF_EP);
    gld16<0>(r0, ro, x); gld16<512>(r1, ro, x); gld8<1024>(r2, ro - (lane & 31) * 8, x);
    bar();
    {
      const char* tsrc = (const char*)x + (size_t)tm * 64 * 1280 + tg * 160;
      const unsigned dst = lds_addr(sm) + KA_U + w * 1024;
#pragma unroll
      for (int i = 0; i < 3; ++i) { const unsigned pc = tid + 256 * i < 640 ? tid + 256 * i : 639; dma16(dst + i * 4096, (pc / 10) * 1280 + (pc % 10) * 16, tsrc); }
    }
    __builtin_amdgcn_s_setprio(2);
    asm volatile("s_waitcnt vmcnt(3)" : "+v"(ep), "+v"(r0), "+v"(r1), "+v"(r2)::"memory");
    ST(1);
    tag = ep + 0x9E3779B9u;
    f2 s0 = {bflo(r2.x), bfhi(r2.x)}, s1 = {bflo(r2.y), bfhi(r2.y)}, q0 = s0 * s0, q1 = s1 * s1;
#pragma unroll
    for (int c = 0; c < 4; ++c) {
      const unsigned v = r0[c], v2 = r1[c];
      const f2 a = {bflo(v), bfhi(v)}, a2 = {bflo(v2), bfhi(v2)};
      s0 += a; q0 += a * a; s1 += a2; q1 += a2 * a2;
    }
    s0 += s1; q0 += q1;
    float ss = s0.x + s0.y, qq = q0.x + q0.y;
    ss += dpp<DPP_ROR8>(ss); qq += dpp<DPP_ROR8>(qq); ss += dpp<DPP_ROR4>(ss); qq += dpp<DPP_ROR4>(qq);
    ss += dpp<DPP_ROR2>(ss); qq += dpp<DPP_ROR2>(qq); ss += dpp<DPP_ROR1>(ss); qq += dpp<DPP_ROR1>(qq);
    ss += dppm<0x142, 0xa>(ss); qq += dppm<0x142, 0xa>(qq);
    const float mean = ss * (1.0f / D);
    const float var = fmaxf(qq * (1.0f / D) - mean * mean, 0.f);
    unsigned pay = pk_bf(mean, __builtin_amdgcn_sqrtf(rbf(rbf(var) + 1e-5f)));
    pin(pay);
    u4 gv = {(unsigned)__builtin_amdgcn_readlane(pay, 31), tag, pay, tag};
    pin(gv);
    waitv<0>();
    if (lane == 63) gst16s((row - 1) * 8, gv, ws + OFF_G1);
    chsync(lds_addr(sm) + KA_S + 80, w, 1);
    ST(2);
#pragma unroll 1
    for (int bl = w; bl < 5; bl += 4) {
      const int l = lane & 15, gm = lane >> 4;
      const char* tp = sm + KA_U + (8 * gm + (l >> 2)) * 160 + bl * 32 + (l & 3) * 8;
      const u4 A = tr2(tp, 640), B = tr2(tp + 32 * 160, 640);
      u4 s1v, s2v;
#pragma unroll
      for (int e = 0; e < 4; ++e) {
        const unsigned ae = A[e], be = B[e];
        s1v[e] = __builtin_amdgcn_update_dpp(ae, be, DPP_ROR8, 0xf, 0xc, false);
        s2v[e] = __builtin_amdgcn_update_dpp(be, ae, DPP_ROR8, 0xf, 0x3, false);
      }
      const unsigned vo = (unsigned)(80 * tg + 16 * bl + (l & 7)) * 4096 + tm * 128 + (gm + 4 * (l >> 3)) * 16;
      gst16s(vo, s1v, ws + OFF_XT);
      gst16s(vo + 8 * 4096, s2v, ws + OFF_XT);
    }
    waitv<0>();
    chsync(lds_addr(sm) + KA_S + 80, w, 2);
    if (tid == 0) __hip_atomic_store((unsigned*)(ws + OFF_F1) + (32 * tg + tm) * 32, tag, __ATOMIC_RELAXED, __HIP_MEMORY_SCOPE_AGENT);
    ST(3);
    if (isconv) {
      u4 gr[4], xr[3];
      poll_ld<0>(gr, tid * 64, ws + OFF_G1);
      if (w == 0) {
#pragma unroll 1
        for (int spin = 0; spin < (1 << 16); ++spin) {
          unsigned v;
          asm volatile("global_load_dword %0, %1, %2 sc1\n\ts_waitcnt vmcnt(0)" : "=v"(v) : "v"((lane & 31) * 128), "s"(ws + OFF_F1 + g * 4096) : "memory");
          if (__all(v == tag)) break;
          __builtin_amdgcn_s_sleep(2);
        }
      }
      wait4(gr);
      chsync(lds_addr(sm) + KA_S + 80, w, 3);
      ST(4);
      const char* xsrc = ws + OFF_XT + (size_t)d0 * 4096;
      gld16s<0>(xr[0], tid * 16, xsrc); gld16s<0>(xr[1], tid * 16, xsrc + 4096); gld16s<0>(xr[2], tid * 16, xsrc + 8192);
#pragma unroll 1
      for (int spin = 0; spin < (1 << 16); ++spin) {
        bool ok = true;
#pragma unroll
        for (int i = 0; i < 4; ++i) ok = ok && gr[i].y == tag && gr[i].w == tag;
        if (__all(ok)) break;
        poll_ld<0>(gr, tid * 64, ws + OFF_G1);
        wait4(gr);
      }
      f2 mm[4], rs[4];
#pragma unroll
      for (int i = 0; i < 4; ++i) { mm[i] = (f2){bflo(gr[i].x), bflo(gr[i].z)}; rs[i] = (f2){__builtin_amdgcn_rcpf(bfhi(gr[i].x)), __builtin_amdgcn_rcpf(bfhi(gr[i].z))}; }
      if (tid < 90) *(u4*)(sm + KA_U + (tid / 30) * U_PITCH + (tid % 30) * 16) = (u4){0, 0, 0, 0};
      ST(7);
      asm volatile("s_waitcnt vmcnt(0)" : "+v"(xr[0]), "+v"(xr[1]), "+v"(xr[2])::"memory");
      ST(8);
#pragma unroll
      for (int c = 0; c < 3; ++c) {
        u4 o;
#pragma unroll
        for (int i = 0; i < 4; ++i) { const unsigned v = xr[c][i]; o[i] = pk_h2(((f2){bflo(v), bfhi(v)} - mm[i]) * rs[i]); }
        *(u4*)(sm + KA_U + c * U_PITCH + 480 + tid * 16) = o;
      }
      __builtin_amdgcn_s_setprio(0);
      bar();
      ST(6);
      if (b == 0 && tid == 0) __hip_atomic_store((unsigned*)(ws + OFF_EP), tag, __ATOMIC_RELAXED, __HIP_MEMORY_SCOPE_AGENT);
      __builtin_amdgcn_s_sleep(4);
      if (w != 0) {
        unsigned pw[4];
#pragma unroll
        for (int i = 0; i < 4; ++i) {
          const unsigned ln = (i * 81 + m * 3 + w - 1) * 64 + lane;
          gld4<0>(pw[i], (ln < 16384 ? ln : 16383) * 128, (const char*)wsp + (size_t)(g >> 1) * (16384 * 128));
        }
        if (m < 2) {
          const unsigned ln = (m * 3 + w - 1) * 64 + lane;
          const char* code;
          unsigned pc;
          asm volatile("s_getpc_b64 s[44:45]\n\ts_add_u32 s44, s44, _Z2kbPKtS0_PcPt@rel32@lo+4\n\ts_addc_u32 s45, s45, _Z2kbPKtS0_PcPt@rel32@hi+12\n\ts_mov_b64 %0, s[44:45]"
                       : "=s"(code)::"s44", "s45", "scc");
          gld4<0>(pc, (ln < KB_CODE_LINES ? ln : KB_CODE_LINES - 1) * 128, code);
          asm volatile("s_waitcnt vmcnt(0)" : "+v"(pc)::"memory");
        }
        asm volatile("s_waitcnt vmcnt(0)" : "+v"(pw[0]), "+v"(pw[1]), "+v"(pw[2]), "+v"(pw[3])::"memory");
      } else {
      const int item = b * 40 + lane;
      if (lane < 40) {
        const int oc = item % 80, kq = item / 80, kap = kq >> 2, qq = kq & 3;
        u4 r[8], o[8];
#pragma unroll
        for (int e = 0; e < 8; ++e) r[e] = *(const u4*)(w_out + (size_t)(32 * kap + 16 * (e >> 2) + 4 * qq + (e & 3)) * D + oc * 8);
        tr8x8(r, o);
        u4* dst = (u4*)(ws + OFF_WO + ((size_t)(oc >> 1) * NKS3 + kap) * 1024) + qq * 16 + (oc & 1) * 8;
#pragma unroll
        for (int c = 0; c < 8; ++c) dst[c] = o[c];
      }
      }
      ST_END(0);
      return;
    }
    __builtin_amdgcn_s_setprio(0);
  }
  if (!isconv) {
    unsigned pc = 0;
    if (w == 4) {
      const unsigned ln = (m - 27) * 64 + lane;
      const char* code;
      asm volatile("s_getpc_b64 s[44:45]\n\ts_add_u32 s44, s44, _Z2kaPKtS0_S0_S0_S0_PKjS2_S0_S0_S0_Pc@rel32@lo+4\n\t"
                   "s_addc_u32 s45, s45, _Z2kaPKtS0_S0_S0_S0_PKjS2_S0_S0_S0_Pc@rel32@hi+12\n\ts_mov_b64 %0, s[44:45]" : "=s"(code)::"s44", "s45", "scc");
      gld4<0>(pc, (ln < KA_CODE_LINES ? ln : KA_CODE_LINES - 1) * 128, code);
    }
    if (w >= 4) rawbar();
    rawbar();
    if (w == 4) asm volatile("s_waitcnt vmcnt(0)" : "+v"(pc)::"memory");
    const int si = b - 216;
    if (si < 27) {
      const int tt = tid - 384, co = tt & 7, ko = tt >> 3, c0 = 64 * si + 8 * co, k0 = 8 * ko;
      float* part = (float*)(sm + 98304);
      if (w >= 6) {
        u4 r[8], o[8];
#pragma unroll
        for (int e = 0; e < 8; ++e) r[e] = *(const u4*)(w_in + (size_t)(k0 + e) * F + c0);
        float cs[8];
#pragma unroll
        for (int c = 0; c < 4; ++c) {
          f2 s = {0.f, 0.f};
#pragma unroll
          for (int e = 0; e < 8; ++e) { const unsigned v = r[e][c]; s += (f2){bflo(v), bfhi(v)}; }
          cs[2 * c] = s.x + dpp<DPP_ROR8>(s.x); cs[2 * c + 1] = s.y + dpp<DPP_ROR8>(s.y);
        }
        if (!(lane & 8)) {
          f4* p = (f4*)(part + ((w - 6) * 4 + (lane >> 4)) * 64 + co * 8);
          p[0] = (f4){cs[0], cs[1], cs[2], cs[3]}; p[1] = (f4){cs[4], cs[5], cs[6], cs[7]};
        }
        tr8x8(r, o);
#pragma unroll
        for (int c = 0; c < 8; ++c) *(u4*)(sm + tt * 144 + c * 16) = o[c];
      }
      __syncthreads();
      if (w < 8) {
        float s = 0.f;
#pragma unroll
        for (int e = 0; e < 5; ++e) s += part[(w * 5 + e) * 64 + lane];
        part[2560 + w * 64 + lane] = s;
      }
#pragma unroll
      for (int i = 0; i < 5; ++i) {
        const int f = w * 5 + i, blk = f / 20, ks = f % 20, gc = 4 * si + blk;
        const u4 v = *(const u4*)(sm + ((4 * ks + (lane >> 4)) * 8 + 2 * blk + ((lane >> 3) & 1)) * 144 + (lane & 7) * 16);
        *(u4*)(ws + OFF_WF + ((size_t)(gc >= NGB ? gc - NGB : gc + NGB) * NKS1 + ks) * 1024 + lane * 16) = v;
      }
      __syncthreads();
      if (w == 0) {
        float s = 0.f;
#pragma unroll
        for (int e = 0; e < 8; ++e) s += part[2560 + e * 64 + lane];
        ((float*)(ws + OFF_CS))[64 * si + lane] = s;
      }
    } else if (si < 35) {
      ((u4*)(ws + OFF_ST))[(si - 27) * 1024 + tid] = (u4){0, 0, 0, 0};
    } else if (si == 35) {
      if (tid < 576) *(u4*)(ws + OFF_F2 + (tid < 512 ? tid * 128 : 65536 + (tid - 512) * 16)) = (u4){0, 0, 0, 0};
    }
    ST_END(0);
    return;
  }
  const int ch = w < 4 ? 0 : (w >> 2) - 1, role = (w + ch) & 3;
  const int d = d0 + ch;
  const int col = lane & 15, q = lane >> 4;
  char* rc = sm + ch * RC_SZ;
  float* slot = (float*)(sm + KA_S) + 2 * ch;
  const unsigned fl = lds_addr(sm) + KA_S + 32 + 16 * ch;
  if (w >= 4) {
    unsigned la, ai, cr, ci, ldw, dw;
    gld4<0>(ldw, 0, logdt + (d >> 1));
    gld4<0>(dw, 0, dskip + (d >> 1));
    gld4<0>(la, lane * 4, logA + d * NM);
    gld4<0>(ai, lane * 4, Aim + d * NM);
    gld4<0>(cr, lane * 4, Cre + d * NM);
    gld4<0>(ci, lane * 4, Cim + d * NM);
    rawbar();
    asm volatile("s_waitcnt vmcnt(0)" : "+v"(la), "+v"(ai), "+v"(cr), "+v"(ci), "+v"(ldw), "+v"(dw)::"memory");
    ST(1);
    const float dt = rbf(__expf((d & 1) ? bfhi(ldw) : bflo(ldw)));
    const unsigned ea = pk_bf(__expf(bflo(la)), __expf(bfhi(la)));
    const f2 a2 = (f2){bflo(ea), bfhi(ea)} * (-1.4426950408889634f * dt);
    f2 br = (f2){bflo(ai), bfhi(ai)} * (dt * 0.15915494309189535f);
    char* base = rc + lane * 8;
    auto put = [&](int row, c2 v) { *(u2*)(base + row * KG_PITCH) = (u2){pk_h2(v.x), pk_h2(v.y)}; };
    if (role == 0) {
      c2 v = {{bflo(cr), bfhi(cr)}, {bflo(ci), bfhi(ci)}};
      float mx = fmaxf(fmaxf(fabsf(v.x.x), fabsf(v.x.y)), fmaxf(fabsf(v.y.x), fabsf(v.y.y)));
      mx = fmaxf(mx, dpp<DPP_ROR8>(mx)); mx = fmaxf(mx, dpp<DPP_ROR4>(mx)); mx = fmaxf(mx, dpp<DPP_ROR2>(mx)); mx = fmaxf(mx, dpp<DPP_ROR1>(mx));
      mx = fmaxf(mx, dppm<0x142, 0xa>(mx)); mx = fmaxf(mx, dppm<0x143, 0xc>(mx));
      mx = __int_as_float(__builtin_amdgcn_readlane(__float_as_int(mx), 63));
      const float dsk = (d & 1) ? bfhi(dw) : bflo(dw);
      const float sc = fmaxf(fmaxf(2.0f * dt * mx, fabsf(dsk) * (1.0f / 16384.0f)), 1e-30f);
      const float isc = 1.0f / sc;
      if (lane == 0) { slot[0] = sc; slot[1] = dsk * isc; }
      v.x *= 2.0f * dt * isc; v.y *= 2.0f * dt * isc;
      const c2 s = cexpp(a2, br, 8.f);
#pragma unroll
      for (int j = 0; j < 16; ++j) { put(j, v); if (j < 15) v = cmul(v, s); }
    } else {
      br = -br;
      const c2 h = cexpp(a2, br, 128.f);
      if (role == 1) {
        const c2 s = cexpp(a2, br, 1.f);
        put(16, (c2){{1.f, 1.f}, {0.f, 0.f}}); put(24, h);
        c2 v = s;
#pragma unroll
        for (int c = 1; c < 8; ++c) { put(16 + c, v); put(24 + c, cmul(v, h)); if (c < 7) v = cmul(v, s); }
      } else if (role == 2) {
        const c2 s = cexpp(a2, br, 2.f);
        c2 v = cmul(h, h);
        const c2 q = cmul(s, s), y = cmul(v, v), z = cmul(y, h);
        put(40, y); put(41, cmul(y, q)); put(42, z); put(43, cmul(z, q));
#pragma unroll
        for (int c = 0; c < 4; ++c) { put(32 + c, v); put(36 + c, cmul(v, h)); if (c < 3) v = cmul(v, s); }
      } else {
        const c2 q = cexpp(a2, br, 4.f);
        c2 v = cexpp(a2, br, 768.f);
        put(44, v); put(45, cmul(v, q)); v = cmul(v, h);
        put(46, v); put(47, cmul(v, q));
#pragma unroll
        for (int i = 0; i < 9; ++i) { v = cmul(v, h); put(48 + i, v); }
      }
    }
  ST(2);
  chsync(fl, w & 3, 1);
  char* tb = sm + KA_T + ch * T_PITCH;
  {
    const int wq = w & 3;
    const unsigned pb = lds_addr(rc) + col * KG_PITCH + q * 16;
    f4 acc = {0.f, 0.f, 0.f, 0.f}, ac0 = {0.f, 0.f, 0.f, 0.f};
    const float dadd = slot[1];
    if (wq == 3) tbl_tiles<true>(pb + 32 * KG_PITCH, pb, acc, ac0); else tbl_tiles<false>(pb + (wq < 2 ? 48 : 40) * KG_PITCH, pb, acc, ac0);
    const unsigned p0 = pk_h(acc[0], acc[1]), p1 = pk_h(acc[2], acc[3]);
    if (wq < 2) {
      u16* t = (u16*)tb + 512 + 64 * q + col;
      t[0] = (u16)p0; t[16] = (u16)(p0 >> 16); t[32] = (u16)p1; t[48] = (u16)(p1 >> 16);
    } else if (wq == 2) {
      if (q < 2) { *(unsigned*)(tb + 2 * (384 + 64 * q + 2 * col)) = p0; *(unsigned*)(tb + 2 * (416 + 64 * q + 2 * col)) = p1; }
      else if (lane == 32) ((u16*)tb)[512] = (u16)p0;
    } else {
      if (q < 2) *(u2*)(tb + 2 * (256 + 64 * q + 4 * col)) = (u2){p0, p1};
      else if (lane == 32) ((u16*)tb)[384] = (u16)p0;
      if (lane == 0) ac0[0] += dadd;
      *(u2*)(tb + 2 * (128 * (q >> 1) + 4 * (q & 1) + 8 * col)) = (u2){pk_h(ac0[0], ac0[1]), pk_h(ac0[2], ac0[3])};
    }
  }
  asm volatile("s_waitcnt lgkmcnt(0)" ::: "memory");
  ST(3);
  {
    const int t8 = tid & 255, a = 255 - t8, wq = w & 3;
    u4 o0, o1;
    if (wq < 2) {
      const u16* t = (const u16*)tb + 384 + a;
      const unsigned lo = t[0], hi = t[1];
      const unsigned hh = perm(hi, hi, 0x01000100u), ll = perm(lo, lo, 0x01000100u), tie = (a & 1) ? hi : lo;
      o0 = (u4){hh, perm(tie, hi, 0x05040100u), ll, ll}; o1 = (u4){hh, perm(lo, tie, 0x05040100u), ll, ll};
    } else if (wq == 2) {
      const unsigned* t = (const unsigned*)(tb + 2 * (256 + 2 * a));
      const unsigned v01 = t[0], v2 = t[1];
      const unsigned t11 = perm(v01, v01, 0x03020302u), t00 = perm(v01, v01, 0x01000100u);
      o0 = (u4){perm(v2, v2, 0x01000100u), t11, perm(v01, v01, 0x01000302u), t00}; o1 = (u4){perm(v01, v2, 0x07060100u), t11, t00, t00};
    } else if (a >= 32) {
      const unsigned* t = (const unsigned*)(tb + 2 * (128 + 4 * a));
      const unsigned vm = t[-1], v01 = t[0], v23 = t[1], v4 = t[2];
      const unsigned t22 = perm(v23, v23, 0x01000100u);
      o0 = (u4){perm(v23, v4, 0x07060100u), t22, perm(v01, v23, 0x07060100u), perm(v01, v01, 0x01000100u)};
      o1 = (u4){perm(v23, v23, 0x01000302u), t22, perm(v01, v01, 0x01000302u), perm(vm, v01, a == 32 ? 0x07060100u : 0x01000100u)};
    } else {
      const u4 v = *(const u4*)(tb + 16 * a);
      const unsigned vm = a ? *(const unsigned*)(tb + 16 * a - 4) : 0u;
      o0 = (u4){perm(v.w, v.w, 0x01000302u), perm(v.z, v.z, 0x01000302u), perm(v.y, v.y, 0x01000302u), perm(v.x, v.x, 0x01000302u)};
      o1 = (u4){perm(v.z, v.w, 0x07060100u), perm(v.y, v.z, 0x07060100u), perm(v.x, v.y, 0x07060100u), perm(vm, v.x, 0x07060100u)};
    }
    char* kcb = sm + KA_K + ch * 4 * KC_PITCH;
    char* kc = kcb + 16 * t8;
    *(u4*)kc = o0; *(u4*)(kc + KC_PITCH) = o1;
    unsigned* k2 = (unsigned*)(kc + 2 * KC_PITCH - 4);
    unsigned* k3 = (unsigned*)(kc + 3 * KC_PITCH - 4);
    k2[0] = o0.x; k2[1] = o0.y; k2[2] = o0.z; k2[3] = o0.w;
    k3[0] = o1.x; k3[1] = o1.y; k3[2] = o1.z; k3[3] = o1.w;
    if (wq == 0 && lane < 36) { const int s = lane / 9; *(unsigned*)(kcb + s * KC_PITCH + 4096 - (s >> 1) * 4 + (lane - 9 * s) * 4) = 0; }
  }
  ST(4);
  }
  ST(5);
  bar();
  ST(6);
  const int wq = w & 3;
  u2 xv[2];
  gld8s<0>(xv[0], (512 * wq + 16 * col + 4 * q) * 2, ws + OFF_XT + (size_t)d * 4096);
  gld8s<512>(xv[1], (512 * wq + 16 * col + 4 * q) * 2, ws + OFF_XT + (size_t)d * 4096);
  f4 acc[8];
#pragma unroll
  for (int n = 0; n < 8; ++n) acc[n] = (f4){0.f, 0.f, 0.f, 0.f};
  {
    const int s = (3 - col) & 3;
    const unsigned pa = lds_addr(sm) + KA_K + (4 * ch + s) * KC_PITCH + 2 * (2031 - s - col + 8 * q) - 64 * (2 * wq + 1) - 512 * 7;
    const unsigned pb = lds_addr(sm) + KA_U + ch * U_PITCH + 448 + 32 * col + 16 * q - 64 * (2 * wq + 1);
    u2 al[8], ah[8]; u4 bb[8];
    conv_ld<0, 0>(al, ah, bb, pa, pb);
    conv_mm<0>(al, ah, bb, acc);
    __builtin_amdgcn_sched_barrier(0);
    conv_ld<1, 0>(al, ah, bb, pa, pb);
    conv_mm<0>(al, ah, bb, acc);
  }
  ST(7);
  f4 tot[2];
  {
    f4* red = (f4*)(rc + wq * 6144) + lane;
#pragma unroll
    for (int v = 0; v < 4; ++v) {
      if (v == wq) { tot[0] = acc[2 * v]; tot[1] = acc[2 * v + 1]; }
      else { const int sl = v > wq ? v - 1 : v; red[sl * 128] = acc[2 * v]; red[sl * 128 + 64] = acc[2 * v + 1]; }
    }
  }
  chsync(fl, w & 3, 2);
  ST(8);
#pragma unroll
  for (int v = 0; v < 3; ++v) {
    const int src = v < wq ? v : v + 1;
    const f4* rp = (const f4*)(rc + src * 6144) + (wq > src ? wq - 1 : wq) * 128 + lane;
    tot[0] += rp[0]; tot[1] += rp[64];
  }
  const float sc = slot[0];
  asm volatile("s_waitcnt vmcnt(0)" : "+v"(xv[0]), "+v"(xv[1])::"memory");
#pragma unroll
  for (int nn = 0; nn < 2; ++nn) {
    const int t = 512 * wq + 256 * nn + 16 * col + 4 * q;
    const int pos = (t & ~31) + 16 * ((t >> 2) & 1) + 4 * ((t >> 3) & 3);
    const float h[4] = {__builtin_fmaf(tot[nn][0], sc, bflo(xv[nn].x)), __builtin_fmaf(tot[nn][1], sc, bfhi(xv[nn].x)),
                        __builtin_fmaf(tot[nn][2], sc, bflo(xv[nn].y)), __builtin_fmaf(tot[nn][3], sc, bfhi(xv[nn].y))};
    *(u2*)(ws + OFF_HT + (size_t)(pos >> 6) * 81920 + d * 128 + (pos & 63) * 2) = (u2){pk_bf(h[0], h[1]), pk_bf(h[2], h[3])};
  }
  ST_END(0);
}

DEVI void tile_of(int b, int& i, int& j) { const int g = b & 7, r = b >> 3; i = (g >> 1) * 8 + (r >> 2); j = (g & 1) * 4 + (r & 3); }
constexpr int KB_RED = 81920, KB_SLOT = 7168, KB_Z1 = KB_RED + 65536, KB_S2 = KB_Z1 + 14336, KB_S3 = KB_S2 + 512,
              KB_CNT = KB_S3 + 512, KB_LDS = KB_CNT + 16;
#define KSP 12
constexpr int KSPLIT = KSP;

#define OSF ""
#define STSET 0
#define STK(set, i) do { if constexpr (STSET == set) ST(i); } while (0)
template <bool Z> DEVI void mmv(f4& d, const u4& a, const u4& b) {
  if constexpr (Z) asm volatile("v_mfma_f32_16x16x32_bf16 %0, %1, %2, 0" : "=v"(d) : "v"(a), "v"(b));
  else asm volatile("v_mfma_f32_16x16x32_bf16 %0, %1, %2, %0" : "+v"(d) : "v"(a), "v"(b));
}
template <bool Z> DEVI void mmva(f4& d, const u4& a, const u4& b) {
  if constexpr (Z) asm volatile("v_mfma_f32_16x16x32_bf16 %0, %1, %2, 0" : "=v"(d) : "v"(a), "a"(b));
  else asm volatile("v_mfma_f32_16x16x32_bf16 %0, %1, %2, %0" : "+v"(d) : "v"(a), "a"(b));
}
template <bool Z> DEVI void mmav(f4& d, const u4& a, const u4& b) {
  if constexpr (Z) asm volatile("v_mfma_f32_16x16x32_bf16 %0, %1, %2, 0" : "=v"(d) : "a"(a), "v"(b));
  else asm volatile("v_mfma_f32_16x16x32_bf16 %0, %1, %2, %0" : "+v"(d) : "a"(a), "v"(b));
}
template <int IMM> DEVI void gld16a(u4& d, unsigned voff, const void* sb) { asm volatile("global_load_dwordx4 %0, %1, %2 offset:%3" : "=a"(d) : "v"(voff), "s"(sb), "i"(IMM)); }
template <int IMM> DEVI void rld16(u4& d, unsigned voff, const void* sb) { asm volatile("global_load_dwordx4 %0, %1, %2 offset:%3" : "+v"(d) : "v"(voff), "s"(sb), "i"(IMM)); }
template <int IMM> DEVI void rld16s(u4& d, unsigned voff, const void* sb) { asm volatile("global_load_dwordx4 %0, %1, %2 offset:%3 sc1" : "+v"(d) : "v"(voff), "s"(sb), "i"(IMM)); }
template <int IMM> DEVI void trd(u2& d, unsigned a) { asm volatile("ds_read_b64_tr_b16 %0, %1 offset:%2" : "=v"(d) : "v"(a), "i"(IMM)); }
template <int IMM> DEVI void ldsw16(unsigned a, const f4& v) { asm volatile("ds_write_b128 %0, %1 offset:%2" ::"v"(a), "v"(v), "i"(IMM) : "memory"); }
DEVI void dot2(float& acc, unsigned a, unsigned b) { asm volatile("v_dot2_f32_bf16 %0, %1, %2, %0" : "+v"(acc) : "v"(a), "v"(b)); }
DEVI void dot2s(float& acc, unsigned a, unsigned b) { asm volatile("v_dot2_f32_bf16 %0, %1, %2, %0" : "+v"(acc) : "v"(a), "s"(b)); }
DEVI void settle(f4& a, f4& b, f4& c, f4& d) { asm volatile("s_nop 7" : "+v"(a), "+v"(b), "+v"(c), "+v"(d)); }
DEVI void hold(f4& a, f4& b, f4& c, f4& d) { asm volatile("" : "+v"(a), "+v"(b), "+v"(c), "+v"(d)); }
DEVI u4 cat(const u2& a, const u2& b) { return (u4){a.x, a.y, b.x, b.y}; }
DEVI f2 gelu2(f2 x) {
  const f2 u = x * (x * x * -0.1029432397f + -2.3022081985f);
  const f2 d = (f2){__builtin_amdgcn_exp2f(u.x), __builtin_amdgcn_exp2f(u.y)} + 1.0f;
  return x * (f2){__builtin_amdgcn_rcpf(d.x), __builtin_amdgcn_rcpf(d.y)};
}
template <int CTRL, int H> DEVI void bfly(float (&v)[16], bool up) {
#pragma unroll
  for (int k = 0; k < H; ++k) { const float send = up ? v[k] : v[k + H], keep = up ? v[k + H] : v[k]; v[k] = keep + dpp<CTRL>(send); }
}
DEVI float rows_sum(float v) {
  auto a = __builtin_amdgcn_permlane32_swap(__float_as_uint(v), __float_as_uint(v), false, false);
  v = __uint_as_float(a[0]) + __uint_as_float(a[1]);
  auto b = __builtin_amdgcn_permlane16_swap(__float_as_uint(v), __float_as_uint(v), false, false);
  return __uint_as_float(b[0]) + __uint_as_float(b[1]);
}
DEVI int fresh_lane() {
  int l;
  asm volatile("v_mbcnt_lo_u32_b32 %0, -1, 0\n\tv_mbcnt_hi_u32_b32 %0, -1, %0" : "=v"(l));
  return l;
}
DEVI bool last_of_four(unsigned cnt_addr) {
  unsigned o = 0;
  if (fresh_lane() == 0) asm volatile("ds_add_rtn_u32 %0, %1, %2\n\ts_waitcnt lgkmcnt(0)" : "=v"(o) : "v"(cnt_addr), "v"(1u) : "memory");
  return __builtin_amdgcn_readfirstlane(o) == 3;
}
DEVI void poll8(unsigned voff, const void* sb) {
  unsigned t, n;
  asm volatile(
      "s_mov_b32 %1, 0x40000\n"
      "1:\n\tglobal_load_dword %0, %2, %3 sc1\n\ts_waitcnt vmcnt(0)\n\tv_cmp_ne_u32 vcc, 0, %0\n\ts_sub_u32 %1, %1, 1\n\ts_cbranch_scc1 2f\n\t"
      "s_cmp_eq_u64 vcc, -1\n\ts_cbranch_scc1 2f\n\ts_sleep 1\n\ts_branch 1b\n2:"
      : "=&v"(t), "=&s"(n) : "v"(voff), "s"(sb) : "vcc", "scc", "memory");
}

struct G1 {
  u2 al[2][4], ah[2][4];
  u4 ring[8][2], ones;
  unsigned touch[4], va[2], sb;
  const char* hsrc;
  f4 acc[2][4], sx, sq;
  unsigned pa[4], vb[2];
  const char *b2, *b1;
  u64* st_;
};
struct WS { unsigned vw[2], rd[2], ring; const char* src[4]; };
template <int C, int M> DEVI void w_dma(const WS& x) {
  asm volatile("s_mov_b32 m0, %0\n\ts_nop 0\n\tglobal_load_lds_dwordx4 %1, %3\n\tglobal_load_lds_dwordx4 %2, %3 offset:1024"
               ::"s"(x.ring + (C & 1) * 8192 + M * 2048), "v"(x.vw[0]), "v"(x.vw[1]), "s"(x.src[M] + C * 128) : "memory");
}
DEVI void w_dma_rt(const WS& x, int C, int M) {
  asm volatile("s_mov_b32 m0, %0\n\ts_nop 0\n\tglobal_load_lds_dwordx4 %1, %3\n\tglobal_load_lds_dwordx4 %2, %3 offset:1024"
               ::"s"(x.ring + (C & 1) * 8192 + M * 2048), "v"(x.vw[0]), "v"(x.vw[1]), "s"(x.src[M] + C * 128) : "memory");
}
template <int C, int M, int KH> DEVI void w_read(const WS& x, u4 (&W)[16][4]) {
  asm volatile("ds_read_b128 %0, %1 offset:%2" : "=a"(W[2 * C + KH][M]) : "v"(x.rd[KH]), "i"((C & 1) * 8192 + M * 2048));
}
template <int C> DEVI void w_dma_all(const WS& x) { w_dma<C, 0>(x); w_dma<C, 1>(x); w_dma<C, 2>(x); w_dma<C, 3>(x); }
template <int C> DEVI void w_read_all(const WS& x, u4 (&W)[16][4]) {
  w_read<C, 0, 0>(x, W); w_read<C, 0, 1>(x, W); w_read<C, 1, 0>(x, W); w_read<C, 1, 1>(x, W);
  w_read<C, 2, 0>(x, W); w_read<C, 2, 1>(x, W); w_read<C, 3, 0>(x, W); w_read<C, 3, 1>(x, W);
}
DEVI void w_setup(WS& x, int lane, int w, int i, unsigned sb, const u16* wsp) {
  const int c = lane & 15, g4 = lane >> 4;
  x.ring = sb + KB_RED + w * 16384;
#pragma unroll
  for (int o = 0; o < 2; ++o) {
    x.vw[o] = (unsigned)(16 * o + 8 * (lane >> 5) + ((lane >> 3) & 3)) * 4096 + (((lane & 7) ^ ((4 * o + g4) & 7)) * 16) - 1024 * o;
    x.rd[o] = x.ring + c * 128 + (((4 * o + g4) ^ ((c >> 1) & 7)) * 16);
  }
#pragma unroll
  for (int m = 0; m < 4; ++m) x.src[m] = (const char*)wsp + (size_t)(64 * i + 32 * ((m ^ w) >> 1) + 4 * ((m ^ w) & 1)) * 4096 + w * 1024;
}
template <int KS> DEVI const char* wf_base(const char* b) { return b + (KS >> 3) * 8192 + 4096; }
template <int KS, int X> DEVI void ld_new(G1& g, u4& d, const char* b) { gld16<(KS & 7) * 1024 - 4096>(d, g.vb[X], wf_base<KS>(b)); }
template <int KS, int X> DEVI void ld_ring(G1& g, u4& d, const char* b) { rld16<(KS & 7) * 1024 - 4096>(d, g.vb[X], wf_base<KS>(b)); }
template <int S, int M> DEVI void rd_lo(G1& g) { trd<(S & 15) * 4096>(g.al[S & 1][M], g.pa[M]); }
template <int S, int M> DEVI void rd_hi(G1& g) { trd<(S & 15) * 4096 + 512>(g.ah[S & 1][M], g.pa[M]); }
template <int N, int LG> DEVI void g1_wait(G1& g, int c, int m) {
  asm volatile("s_waitcnt vmcnt(%4) lgkmcnt(%5)" : "+v"(g.al[c][m]), "+v"(g.ah[c][m]), "+v"(g.al[c][m + 1]), "+v"(g.ah[c][m + 1]) : "i"(N), "i"(LG) : "memory");
}
template <int S> DEVI void pa_step(G1& g) {
  if constexpr (S == 16) { for (int m = 0; m < 4; ++m) g.pa[m] += 65536; }
  if constexpr (S == 20) { for (int m = 0; m < 4; ++m) g.pa[m] -= 65536; }
}
template <int KS, int H> DEVI void a_dma(const G1& g) {
  asm volatile("s_mov_b32 m0, %0\n\ts_nop 0\n\tglobal_load_lds_dwordx4 %1, %3\n\tglobal_load_lds_dwordx4 %2, %3 offset:1024"
               ::"s"(g.sb + KS * 4096 + H * 2048), "v"(g.va[0]), "v"(g.va[1]), "s"(g.hsrc + KS * 4096 + H * 2048) : "memory");
}
template <int G, int X> DEVI void b_next(G1& g) {
  if constexpr (G + 8 < 20) ld_ring<G + 8, X>(g, g.ring[G & 7][X], g.b2);
  else if constexpr (G + 8 < 40) ld_ring<G + 8 - 20, X>(g, g.ring[G & 7][X], g.b1);
}
constexpr bool w_rd_at(int s) { return (s >= 0 && s < 8) || (s >= 10 && s < 18); }
constexpr bool w_dma_at(int s) { return w_rd_at(s - 1); }
constexpr int w_tile(int s) { return s < 8 ? s : s - 2; }
constexpr int ops_at(int nb, int k) { return (k < 32 ? nb : 0) + (nb == 1 && k < 10 ? 4 : 0) + (nb == 1 && k == 20 ? 3 : 0) + (w_dma_at(k - 20) ? 2 : 0); }
constexpr int ops_in(int nb, int a, int b) { int n = 0; for (int k = a; k < b; ++k) n += ops_at(nb, k); return n; }
constexpr int imin(int a, int b) { return a < b ? a : b; }
constexpr int g1_vm(int nb, int G) {
  int n = G < 4 ? nb * (7 - G) + 6 + ops_in(nb, 0, G) : G < 8 ? nb * (7 - G) + ops_in(nb, 0, G) : ops_in(nb, G - 7, G);
  if (G == 3) n = imin(n, 4 * nb + ops_in(nb, 0, 3));
  if (nb == 1 && G == 9) n = imin(n, 1 + ops_in(1, 5, 9));
  if (nb == 1 && G == 14) n = imin(n, 1 + ops_in(1, 10, 14));
  if (G >= 20 && G < 28) n = 2 * (27 - G) + ops_in(nb, 20, G);
  if (G >= 30 && G < 38) n = imin(n, (G - 9 < 32 ? nb - 1 : 0) + ops_in(nb, G - 8, G));
  return imin(n, 63);
}
template <int NB, int S> DEVI void g1_first(G1& g) {
  if constexpr (S < 20) {
    constexpr int c = S & 1, nx = (S + 1) % 20;
    constexpr bool Z = S == 0;
    g1_wait<g1_vm(NB, S), 4>(g, c, 0);
    if constexpr (S == 3 || S == 9 || S == 14) asm volatile("s_barrier" ::: "memory");
    { u64* st_ = g.st_; if constexpr (S == 1) STK(1, 4); if constexpr (S == 5) STK(1, 5); if constexpr (S == 10) STK(1, 6); if constexpr (S == 15) STK(1, 7); if constexpr (S == 19) STK(1, 8); }
    pa_step<S + 1>(g);
    const u4& b0 = g.ring[S & 7][0]; const u4& b1 = g.ring[S & 7][1];
    const u4 a0 = cat(g.al[c][0], g.ah[c][0]), a1 = cat(g.al[c][1], g.ah[c][1]);
    mmv<Z>(g.acc[0][0], a0, b0); rd_lo<nx, 0>(g);
    if constexpr (NB == 2) mmv<Z>(g.acc[1][0], a0, b1);
    rd_hi<nx, 0>(g);
    mmv<Z>(g.sx, a0, g.ones);
    if constexpr (NB == 1 && S < 10) a_dma<S + 10, 0>(g);
    mmv<Z>(g.acc[0][1], a1, b0); rd_lo<nx, 1>(g);
    if constexpr (NB == 2) mmv<Z>(g.acc[1][1], a1, b1);
    rd_hi<nx, 1>(g);
    mmv<Z>(g.sq, a0, a0);
    if constexpr (NB == 1 && S < 10) a_dma<S + 10, 1>(g);
    g1_wait<63, 4>(g, c, 2);
    const u4 a2 = cat(g.al[c][2], g.ah[c][2]), a3 = cat(g.al[c][3], g.ah[c][3]);
    mmv<Z>(g.acc[0][2], a2, b0); rd_lo<nx, 2>(g);
    if constexpr (NB == 2) mmv<Z>(g.acc[1][2], a2, b1);
    rd_hi<nx, 2>(g);
    mmv<Z>(g.acc[0][3], a3, b0); rd_lo<nx, 3>(g);
    b_next<S, 0>(g);
    if constexpr (NB == 2) mmv<Z>(g.acc[1][3], a3, b1);
    rd_hi<nx, 3>(g);
    if constexpr (NB == 2) b_next<S, 1>(g);
    g1_first<NB, S + 1>(g);
  }
}
template <int NB, int S, int X, int M> DEVI void mm1(G1& g) {
  if constexpr (X < NB) mmv<S == 0>(g.acc[X][M], g.ring[(S + 20) & 7][X], cat(g.al[S & 1][M], g.ah[S & 1][M]));
}
template <int NB, int S, int E> DEVI void g1_second(G1& g, const WS& x, u4 (&W)[16][4], unsigned tv, const char* tb) {
  if constexpr (S < E) {
    constexpr bool R = S < 19, WR = w_rd_at(S), WP = w_rd_at(S - 1);
    constexpr int T = w_tile(S), TD = w_tile(S - 1) + 8;
    g1_wait<g1_vm(NB, S + 20), (WP ? 5 : 4)>(g, S & 1, 0);
    { u64* st_ = g.st_; if constexpr (S == 0) STK(2, 1); if constexpr (S == 5) STK(2, 2); if constexpr (S == 10) STK(2, 5); if constexpr (S == 15) STK(2, 6); if constexpr (S == 19) STK(2, 7); }
    if constexpr (R) pa_step<S + 1>(g);
    if constexpr (NB == 1 && S == 0) { gld4<0>(g.touch[0], tv, tb); gld4<0>(g.touch[1], tv, tb + 45 * 128); gld4<0>(g.touch[2], tv, tb + 90 * 128); }
    mm1<NB, S, 0, 0>(g); if constexpr (R) rd_lo<S + 1, 0>(g);
    mm1<NB, S, 1, 0>(g); if constexpr (R) rd_hi<S + 1, 0>(g);
    if constexpr (WR) w_read<(T >> 2), (T & 3), 0>(x, W);
    mm1<NB, S, 0, 1>(g); if constexpr (R) rd_lo<S + 1, 1>(g);
    mm1<NB, S, 1, 1>(g); if constexpr (R) rd_hi<S + 1, 1>(g);
    g1_wait<63, (R ? (WR ? 5 : 4) : 0)>(g, S & 1, 2);
    mm1<NB, S, 0, 2>(g); if constexpr (R) rd_lo<S + 1, 2>(g);
    mm1<NB, S, 1, 2>(g); if constexpr (R) rd_hi<S + 1, 2>(g);
    if constexpr (WR) w_read<(T >> 2), (T & 3), 1>(x, W);
    mm1<NB, S, 0, 3>(g); if constexpr (R) rd_lo<S + 1, 3>(g);
    b_next<S + 20, 0>(g);
    if constexpr (w_dma_at(S)) w_dma<(TD >> 2), (TD & 3)>(x);
    mm1<NB, S, 1, 3>(g); if constexpr (R) rd_hi<S + 1, 3>(g);
    if constexpr (NB == 2) b_next<S + 20, 1>(g);
    g1_second<NB, S + 1, E>(g, x, W, tv, tb);
  }
}
template <int NB, int KS, int E> DEVI void g1_pro(G1& g) {
  if constexpr (KS < E) { ld_new<KS, 0>(g, g.ring[KS][0], g.b2); if constexpr (NB == 2) ld_new<KS, 1>(g, g.ring[KS][1], g.b2); g1_pro<NB, KS + 1, E>(g); }
}
#define FOLD2(CT, MA, MB, H) \
  "v_add_f32_dpp %0, %0, %0 " CT " row_mask:0xf bank_mask:" MA "\n\tv_add_f32_dpp %1, %1, %1 " CT " row_mask:0xf bank_mask:" MA "\n\t" \
  "v_add_f32_dpp %2, %2, %2 " CT " row_mask:0xf bank_mask:" MA "\n\tv_add_f32_dpp %3, %3, %3 " CT " row_mask:0xf bank_mask:" MA "\n\t" \
  "v_add_f32_dpp %0, %4, %4 " CT " row_mask:0xf bank_mask:" MB "\n\tv_add_f32_dpp %1, %5, %5 " CT " row_mask:0xf bank_mask:" MB "\n\t" \
  "v_add_f32_dpp %2, %6, %6 " CT " row_mask:0xf bank_mask:" MB "\n\tv_add_f32_dpp %3, %7, %7 " CT " row_mask:0xf bank_mask:" MB
DEVI void fold16(float (&v)[16]) {
  asm volatile("s_nop 1\n\t" FOLD2("row_mirror", "0x3", "0xc", 8) : "+v"(v[0]), "+v"(v[1]), "+v"(v[2]), "+v"(v[3]) : "v"(v[8]), "v"(v[9]), "v"(v[10]), "v"(v[11]));
  asm volatile("s_nop 1\n\t" FOLD2("row_mirror", "0x3", "0xc", 8) : "+v"(v[4]), "+v"(v[5]), "+v"(v[6]), "+v"(v[7]) : "v"(v[12]), "v"(v[13]), "v"(v[14]), "v"(v[15]));
  asm volatile(FOLD2("row_half_mirror", "0x5", "0xa", 4) "\n\t"
               "v_add_f32_dpp %0, %0, %0 quad_perm:[2,3,0,1] row_mask:0xf bank_mask:0xf\n\tv_add_f32_dpp %1, %1, %1 quad_perm:[2,3,0,1] row_mask:0xf bank_mask:0xf\n\t"
               "v_add_f32_dpp %2, %2, %2 quad_perm:[2,3,0,1] row_mask:0xf bank_mask:0xf\n\tv_add_f32_dpp %3, %3, %3 quad_perm:[2,3,0,1] row_mask:0xf bank_mask:0xf\n\t"
               "v_add_f32_dpp %0, %0, %0 quad_perm:[1,0,3,2] row_mask:0xf bank_mask:0xf\n\tv_add_f32_dpp %1, %1, %1 quad_perm:[1,0,3,2] row_mask:0xf bank_mask:0xf\n\t"
               "v_add_f32_dpp %2, %2, %2 quad_perm:[1,0,3,2] row_mask:0xf bank_mask:0xf\n\tv_add_f32_dpp %3, %3, %3 quad_perm:[1,0,3,2] row_mask:0xf bank_mask:0xf"
               : "+v"(v[0]), "+v"(v[1]), "+v"(v[2]), "+v"(v[3]) : "v"(v[4]), "v"(v[5]), "v"(v[6]), "v"(v[7]));
}

struct G2 {
  u4 z[4][7];
  f4 acc[7][4];
  unsigned vz, vz6, slot[3];
  const char *zsrc, *wo, *wo6;
};
template <int S, int FB> DEVI void ld_z(G2& g) {
  const char* sb = g.zsrc + (size_t)S * (NGB * 1024);
  if constexpr (S < 4) gld16s<(FB - 3) * 1024>(g.z[S][FB], FB == 6 ? g.vz6 : g.vz, sb);
  else rld16s<(FB - 3) * 1024>(g.z[S & 3][FB], FB == 6 ? g.vz6 : g.vz, sb);
}
template <int S, int FB> DEVI void ld_z_all(G2& g) { if constexpr (S < 4) { ld_z<S, FB>(g); if constexpr (FB < 6) ld_z_all<S, FB + 1>(g); else ld_z_all<S + 1, 0>(g); } }
template <int N> DEVI void ld_wo(G2& g, u4 (&WO)[7][5]) {
  constexpr int KS = N / 5, DB = N % 5;
  if constexpr (KS < 6) gld16a<(KS - 3) * 1024>(WO[KS][DB], g.vz, g.wo + DB * (NKS3 * 1024));
  else gld16a<0>(WO[KS][DB], g.vz, g.wo6 == nullptr ? g.wo + DB * (NKS3 * 1024) + 3072 : g.wo6);
}
template <int S, int FB> DEVI void g2_main(G2& g, u4 (&W)[16][4], u4 (&WO)[7][5], u64* st_) {
  if constexpr (S < 14) {
    if constexpr (S == 12 && FB == 0) asm volatile("s_waitcnt lgkmcnt(0)" ::: "memory");
    waitv<27>();
    if constexpr (FB == 0) { if constexpr (S == 0) { STK(0, 5); STK(4, 1); } if constexpr (S == 2) STK(4, 2); if constexpr (S == 4) STK(4, 4); if constexpr (S == 6) STK(4, 5); if constexpr (S == 8) STK(4, 6); if constexpr (S == 10) STK(4, 7); if constexpr (S == 13) STK(4, 8); }
    mmva<S == 0>(g.acc[FB][0], g.z[S & 3][FB], W[S][0]); mmva<S == 0>(g.acc[FB][1], g.z[S & 3][FB], W[S][1]);
    mmva<S == 0>(g.acc[FB][2], g.z[S & 3][FB], W[S][2]); mmva<S == 0>(g.acc[FB][3], g.z[S & 3][FB], W[S][3]);
    if constexpr (S < 12) ld_z<S + 4, FB>(g); else ld_wo<7 * (S - 12) + FB>(g, WO);
    if constexpr (FB < 6) g2_main<S, FB + 1>(g, W, WO, st_); else g2_main<S + 1, 0>(g, W, WO, st_);
  }
}
template <int GI, int K> DEVI void g2_tail(G2& g, u4 (&W)[16][4], u4 (&WO)[7][5]) {
  if constexpr (GI < 4) {
    constexpr int m = (GI + 1) & 3, s = 14 + K / 7, fb = K % 7;
    if constexpr (GI == 0 && K == 0) waitv<21>();
    if constexpr (GI == 0 && K == 7) waitv<14>();
    mmva<false>(g.acc[fb][m], g.z[s & 3][fb], W[s][m]);
    if constexpr (GI > 0 && K >= 1 && K < 8) ldsw16<(K - 1) * 1024>(g.slot[GI - 1], g.acc[K - 1][GI]);
    if constexpr (K >= 8 && 14 + 6 * GI + K - 8 < 35) ld_wo<14 + 6 * GI + K - 8>(g, WO);
    if constexpr (K < 13) g2_tail<GI, K + 1>(g, W, WO); else g2_tail<GI + 1, 0>(g, W, WO);
  }
}
struct G3 { u4 v[4][7]; f4 acc[5][4]; };
constexpr int V_PRE = 14;
template <int N, int E> DEVI void ld_v(G3& g, unsigned vo, unsigned vo6, const char* const (&vb)[4]) {
  if constexpr (N < E && N < 28) { gld16s<(N % 7 - 3) * 1024>(g.v[N / 7][N % 7], N % 7 == 6 ? vo6 : vo, vb[(N / 7 + 1) & 3]); ld_v<N + 1, E>(g, vo, vo6, vb); }
}
template <int N> DEVI void g3_loop(G3& g, u4 (&WO)[7][5], const unsigned (&slot)[3], unsigned vo, unsigned vo6, const char* const (&vb)[4], u64* st_) {
  if constexpr (N < 28) {
    constexpr int GI = N / 7, s = N % 7, m = (GI + 1) & 3;
    waitv<imin(V_PRE + 2 * N, 28) - 1 - N>();
    if constexpr (N == 0) STK(0, 8);
    mmav<s == 0>(g.acc[0][m], WO[s][0], g.v[GI][s]); mmav<s == 0>(g.acc[1][m], WO[s][1], g.v[GI][s]);
    ld_v<V_PRE + 2 * N, V_PRE + 2 * N + 1>(g, vo, vo6, vb);
    mmav<s == 0>(g.acc[2][m], WO[s][2], g.v[GI][s]); mmav<s == 0>(g.acc[3][m], WO[s][3], g.v[GI][s]);
    ld_v<V_PRE + 2 * N + 1, V_PRE + 2 * N + 2>(g, vo, vo6, vb);
    mmav<s == 0>(g.acc[4][m], WO[s][4], g.v[GI][s]);
    if constexpr (GI > 0 && s >= 1 && s < 6) ldsw16<(s - 1) * 1024>(slot[GI - 1], g.acc[s - 1][GI]);
    g3_loop<N + 1>(g, WO, slot, vo, vo6, vb, st_);
  }
}

struct Late { f4 acc[4]; float r[4], n[4]; unsigned o[4], rd[2]; u4 cs; };
DEVI void z1_block(const Late& t, char* sm) {
  const f2 c0 = {__uint_as_float(t.cs.x), __uint_as_float(t.cs.y)}, c1 = {__uint_as_float(t.cs.z), __uint_as_float(t.cs.w)};
  u2 y[4];
#pragma unroll
  for (int m = 0; m < 4; ++m) {
    const f2 y0 = gelu2((f2){t.acc[m][0], t.acc[m][1]} * t.r[m] + c0 * t.n[m]), y1 = gelu2((f2){t.acc[m][2], t.acc[m][3]} * t.r[m] + c1 * t.n[m]);
    y[m] = (u2){pk_bf(y0.x, y0.y), pk_bf(y1.x, y1.y)};
  }
#pragma unroll
  for (int m = 0; m < 4; ++m) *(u2*)(sm + t.o[m]) = y[m];
}
template <int NB> DEVI void phase1(char* sm, char* ws, const int i, const int j, const int w, const int gs, const int cnt, u4 (&W)[16][4], Late& late, const u16* wsp, u64 ta, u64 tb_, u64* st_) {
  const int lane = fresh_lane();
  const int c = lane & 15, g4 = lane >> 4;
  const bool liveA = 2 * w < cnt;
  const int ga = gs + (liveA ? 2 * w : cnt - 1), gb = ga + 1;
  const unsigned sb = lds_addr(sm);
  G1 g;
  g.st_ = st_;
  g.vb[0] = (unsigned)ga * (NKS1 * 1024) + lane * 16; g.vb[1] = g.vb[0] + NKS1 * 1024;
  g.b2 = ws + OFF_WF; g.b1 = ws + OFF_WF + (size_t)NGB * NKS1 * 1024;
  unsigned cs2[2]; u4 cs1[2];
  {
    const int r8 = lane >> 3, sg = (r8 >> 1) & 1;
    g.va[0] = r8 * 128 + (((lane & 7) ^ (2 * sg)) * 16); g.va[1] = r8 * 128 + (((lane & 7) ^ (2 * sg + 4)) * 16);
    g.sb = sb; g.hsrc = ws + OFF_HT + (size_t)i * 81920;
    const unsigned vo = w & 1 ? g.va[1] : g.va[0];
#pragma unroll
    for (int n = 0; n < 4; ++n) dma16(sb + (w + 4 * n) * 1024, vo, g.hsrc + (w + 4 * n) * 1024);
#pragma unroll
    for (int x = 0; x < NB; ++x) { gld4<0>(cs2[x], (FH + 16 * (ga + x) + c) * 4, ws + OFF_CS); gld16<0>(cs1[x], (16 * (ga + x) + 4 * g4) * 4, ws + OFF_CS); }
    g1_pro<NB, 0, 4>(g);
    STK(5, 1);
#pragma unroll
    for (int n = 4; n < 10; ++n) dma16(sb + (w + 4 * n) * 1024, vo, g.hsrc + (w + 4 * n) * 1024);
    g1_pro<NB, 4, 8>(g);
    if constexpr (NB == 1) {
      ta += (lane >> 3) * (NKS3 * 1024) + (lane & 7) * 128; tb_ += (lane & 31) * (4 * D);
      const u64 tp = tb_ + ((ta - tb_) & (0ull - (lane < 32)));
      asm volatile("global_load_dword %0, %1, off sc1" : "=v"(g.touch[3]) : "v"(tp));
    }
  }
  STK(5, 4);
  WS x;
  w_setup(x, lane, w, i, sb, wsp);
  {
    const int qq = c >> 2, p = c & 3, sh = (qq >> 1) | ((g4 & 1) << 1);
#pragma unroll
    for (int m = 0; m < 4; ++m) {
      g.pa[m] = sb + (8 * g4 + qq) * 128 + (p >> 1) * 16 + 8 * (p & 1) + (m ^ w ^ sh) * 32;
    }
  }
  g.ones = (u4){0x3f803f80u, 0x3f803f80u, 0x3f803f80u, 0x3f803f80u};
  unsigned aq[4], al[4], zo[2], z1o[4], po, so;
#pragma unroll
  for (int m = 0; m < 4; ++m) { aq[m] = KB_S2 + (m ^ w) * 64 + g4 * 16; al[m] = KB_S2 + (m ^ w) * 64 + c * 4; z1o[m] = KB_Z1 + ((m ^ w) * 7 + 2 * w) * 512 + lane * 8; }
  zo[0] = (unsigned)(w >> 1) * (NGB * 1024) + lane * 16; zo[1] = (unsigned)(1 ^ (w >> 1)) * (NGB * 1024) + lane * 16;
  po = KB_Z1 + (w * 64 + 16 * ((c >> 2) ^ w) + 4 * g4) * 4; so = KB_S2 + (16 * w + c) * 4;
  const char* zb = ws + OFF_Z2 + ((size_t)2 * i * NGB + ga) * 1024;
  u64* st = (u64*)(ws + OFF_ST) + (size_t)i * 512;
  asm volatile("" : "+v"(aq[0]), "+v"(aq[1]), "+v"(aq[2]), "+v"(aq[3]), "+v"(al[0]), "+v"(al[1]), "+v"(al[2]), "+v"(al[3]), "+v"(zo[0]), "+v"(zo[1]),
               "+v"(z1o[0]), "+v"(z1o[1]), "+v"(z1o[2]), "+v"(z1o[3]), "+v"(po), "+v"(so), "+v"(x.vw[0]), "+v"(x.vw[1]), "+v"(x.rd[0]), "+v"(x.rd[1]));
  asm volatile("" : "+s"(x.src[0]), "+s"(x.src[1]), "+s"(x.src[2]), "+s"(x.src[3]), "+s"(x.ring), "+s"(zb), "+s"(st));
  if (w == 0 && lane < 4) *(unsigned*)(sm + KB_CNT + 4 * lane) = 0;
  if constexpr (NB == 2) asm volatile("s_waitcnt vmcnt(22)" : "+v"(cs2[0]), "+v"(cs2[1]), "+v"(cs1[0]), "+v"(cs1[1]), "+v"(g.ones)::"memory");
  else asm volatile("s_waitcnt vmcnt(14)" : "+v"(cs2[0]), "+v"(cs1[0]), "+v"(g.ones)::"memory");
  STK(5, 5);
  bar();
  STK(0, 1); STK(1, 1); STK(5, 6);
  rd_lo<0, 0>(g); rd_hi<0, 0>(g); rd_lo<0, 1>(g); rd_hi<0, 1>(g); rd_lo<0, 2>(g); rd_hi<0, 2>(g); rd_lo<0, 3>(g); rd_hi<0, 3>(g);
  g1_first<NB, 0>(g);
  asm volatile("s_nop 1" : "+v"(g.sx), "+v"(g.sq));
  {
    const float sx = c & 2 ? (c & 1 ? g.sx[3] : g.sx[2]) : (c & 1 ? g.sx[1] : g.sx[0]);
    const float sq = c & 2 ? (c & 1 ? g.sq[3] : g.sq[2]) : (c & 1 ? g.sq[1] : g.sq[0]);
    const f2 st = ln_stats(sx, sq, 1.0f / D);
    if ((c >> 2) == g4) { *(float*)(sm + so) = st.y; *(float*)(sm + so + 256) = -st.x * st.y; }
  }
  bar();
  hold(g.acc[0][0], g.acc[0][1], g.acc[0][2], g.acc[0][3]);
  if constexpr (NB == 2) hold(g.acc[1][0], g.acc[1][1], g.acc[1][2], g.acc[1][3]);
  STK(0, 2); STK(1, 2);
  f2 z[2][8];
  float r1[4], n1[4];
  float* ps = (float*)(sm + KB_Z1); float* pq = ps + 256;
  {
    float sv[16], qv[16];
    f4 r4[4], n4[4];
#pragma unroll
    for (int m = 0; m < 4; ++m) { r4[m] = *(const f4*)(sm + aq[m]); n4[m] = *(const f4*)(sm + aq[m] + 256); }
#pragma unroll
    for (int m = 0; m < 4; ++m) { r1[m] = *(const float*)(sm + al[m]); n1[m] = *(const float*)(sm + al[m] + 256); }
#pragma unroll
    for (int m = 0; m < 4; ++m) {
#pragma unroll
      for (int e = 0; e < 2; ++e) {
        const f2 r = {r4[m][2 * e], r4[m][2 * e + 1]}, n = {n4[m][2 * e], n4[m][2 * e + 1]};
#pragma unroll
        for (int y = 0; y < NB; ++y) z[y][2 * m + e] = gelu2((f2){g.acc[y][m][2 * e], g.acc[y][m][2 * e + 1]} * r + n * __uint_as_float(cs2[y]));
        f2 s2 = z[0][2 * m + e], q2 = s2 * s2;
        if constexpr (NB == 2) { s2 += z[1][2 * m + e]; q2 += z[1][2 * m + e] * z[1][2 * m + e]; }
        sv[4 * m + 2 * e] = s2.x; sv[4 * m + 2 * e + 1] = s2.y; qv[4 * m + 2 * e] = q2.x; qv[4 * m + 2 * e + 1] = q2.y;
        __builtin_amdgcn_sched_barrier(0);
        w_dma_rt(x, (2 * m + e) >> 2, (2 * m + e) & 3);
        __builtin_amdgcn_sched_barrier(0);
      }
    }
    fold16(sv); fold16(qv);
    if ((c & 3) == 0) {
      *(f4*)(sm + po) = liveA ? (f4){sv[0], sv[1], sv[2], sv[3]} : (f4){0.f, 0.f, 0.f, 0.f};
      *(f4*)(sm + po + 1024) = liveA ? (f4){qv[0], qv[1], qv[2], qv[3]} : (f4){0.f, 0.f, 0.f, 0.f};
    }
    if (last_of_four(sb + KB_CNT)) {
      const float ts = (ps[lane] + ps[64 + lane]) + (ps[128 + lane] + ps[192 + lane]), tq = (pq[lane] + pq[64 + lane]) + (pq[128 + lane] + pq[192 + lane]);
      __hip_atomic_store(st + j * 64 + lane, ((u64)(__float_as_uint(tq) | 0x80000000u) << 32) | __float_as_uint(ts), __ATOMIC_RELAXED, __HIP_MEMORY_SCOPE_AGENT);
    }
  }
  asm volatile("" : "+v"(r1[0]), "+v"(r1[1]), "+v"(r1[2]), "+v"(r1[3]), "+v"(n1[0]), "+v"(n1[1]), "+v"(n1[2]), "+v"(n1[3]));
  ST(3);
  const unsigned tv = (lane < 45 ? lane : 44) * 128;
  const char* tb = ws + OFF_WO + (size_t)5 * j * NKS3 * 1024 + (i & 7) * 135 * 128;
  g1_second<NB, 0, KSPLIT>(g, x, W, tv, tb);
  if constexpr (NB == 1) asm volatile("" : "+v"(g.touch[0]), "+v"(g.touch[1]), "+v"(g.touch[2]), "+v"(g.touch[3]));
  float* rs3 = (float*)(sm + KB_S3); float* nm3 = rs3 + 64;
  STK(2, 8);
  if constexpr (NB == 1) {
    u2 gv[8]; unsigned n;
    asm volatile(
        "s_mov_b32 %8, 0x40000\n"
        "1:\n\tglobal_load_dwordx2 %0, %9, %10 sc1\n\tglobal_load_dwordx2 %1, %9, %10 offset:512 sc1\n\tglobal_load_dwordx2 %2, %9, %10 offset:1024 sc1\n\t"
        "global_load_dwordx2 %3, %9, %10 offset:1536 sc1\n\tglobal_load_dwordx2 %4, %9, %10 offset:2048 sc1\n\tglobal_load_dwordx2 %5, %9, %10 offset:2560 sc1\n\t"
        "global_load_dwordx2 %6, %9, %10 offset:3072 sc1\n\tglobal_load_dwordx2 %7, %9, %10 offset:3584 sc1\n\ts_waitcnt vmcnt(0)\n\t"
        "v_cmp_gt_i64 vcc, 0, %0\n\tv_cmp_gt_i64 s[44:45], 0, %1\n\tv_cmp_gt_i64 s[46:47], 0, %2\n\tv_cmp_gt_i64 s[48:49], 0, %3\n\t"
        "s_and_b64 vcc, vcc, s[44:45]\n\ts_and_b64 s[46:47], s[46:47], s[48:49]\n\ts_and_b64 s[50:51], vcc, s[46:47]\n\t"
        "v_cmp_gt_i64 vcc, 0, %4\n\tv_cmp_gt_i64 s[44:45], 0, %5\n\tv_cmp_gt_i64 s[46:47], 0, %6\n\tv_cmp_gt_i64 s[48:49], 0, %7\n\t"
        "s_and_b64 vcc, vcc, s[44:45]\n\ts_and_b64 s[46:47], s[46:47], s[48:49]\n\ts_and_b64 vcc, vcc, s[46:47]\n\ts_and_b64 vcc, vcc, s[50:51]\n\t"
        "s_sub_u32 %8, %8, 1\n\ts_cbranch_scc1 2f\n\ts_cmp_eq_u64 vcc, -1\n\ts_cbranch_scc1 2f\n\ts_sleep 1\n\ts_branch 1b\n2:"
        : "=&v"(gv[0]), "=&v"(gv[1]), "=&v"(gv[2]), "=&v"(gv[3]), "=&v"(gv[4]), "=&v"(gv[5]), "=&v"(gv[6]), "=&v"(gv[7]), "=&s"(n)
        : "v"(lane * 8), "s"(st)
        : "vcc", "scc", "s44", "s45", "s46", "s47", "s48", "s49", "s50", "s51", "memory");
    float ts = 0.f, tq = 0.f;
#pragma unroll
    for (int v = 0; v < 8; ++v) { ts += __uint_as_float(gv[v].x); tq += __uint_as_float(gv[v].y & 0x7fffffffu); }
    const f2 s3 = ln_stats(ts, tq, 1.0f / FH);
    rs3[lane] = s3.y; nm3[lane] = -s3.x * s3.y;
  }
  bar();
  STK(0, 4); STK(2, 4); STK(3, 4);
  if (liveA) {
    unsigned o[2][8];
#pragma unroll
    for (int m = 0; m < 4; ++m) {
      const f4 r4 = *(const f4*)(sm + aq[m] + 512), n4 = *(const f4*)(sm + aq[m] + 768);
#pragma unroll
      for (int e = 0; e < 2; ++e) {
        const f2 r = {r4[2 * e], r4[2 * e + 1]}, n = {n4[2 * e], n4[2 * e + 1]};
#pragma unroll
        for (int x = 0; x < NB; ++x) { const f2 a = z[x][2 * m + e] * r + n; o[x][2 * m + e] = pk_bf(a.x, a.y); }
      }
    }
    const unsigned r0 = zo[0], r1 = zo[1];
    if (w & 1) {
#pragma unroll
      for (int x = 0; x < NB; ++x) { gst16s(r0, (u4){o[x][2], o[x][3], o[x][0], o[x][1]}, zb + x * 1024); gst16s(r1, (u4){o[x][6], o[x][7], o[x][4], o[x][5]}, zb + x * 1024); }
    } else {
#pragma unroll
      for (int x = 0; x < NB; ++x) { gst16s(r0, (u4){o[x][0], o[x][1], o[x][2], o[x][3]}, zb + x * 1024); gst16s(r1, (u4){o[x][4], o[x][5], o[x][6], o[x][7]}, zb + x * 1024); }
    }
  }
  __builtin_amdgcn_sched_barrier(0);
  STK(3, 1);
  g1_second<NB, KSPLIT, 20>(g, x, W, tv, tb);
  STK(3, 2);
  waitv<ops_in(1, KSPLIT + 20, 40)>();
  if (last_of_four(sb + KB_CNT + 4)) {
    if (lane == 0) __hip_atomic_store((unsigned*)(ws + OFF_F2) + (32 * j + i) * 32, 1u, __ATOMIC_RELAXED, __HIP_MEMORY_SCOPE_AGENT);
  }
  STK(3, 5);
  settle(g.acc[0][0], g.acc[0][1], g.acc[0][2], g.acc[0][3]);
#pragma unroll
  for (int m = 0; m < 4; ++m) { late.acc[m] = g.acc[0][m]; late.r[m] = r1[m]; late.n[m] = n1[m]; late.o[m] = z1o[m]; }
  late.cs = cs1[0];
  if constexpr (NB == 2) {
    z1_block(late, sm);
    hold(g.acc[1][0], g.acc[1][1], g.acc[1][2], g.acc[1][3]);
#pragma unroll
    for (int m = 0; m < 4; ++m) { late.acc[m] = g.acc[1][m]; late.o[m] += 512; }
    late.cs = cs1[1];
  }
  __builtin_amdgcn_sched_barrier(0);
  STK(3, 6);
  waitv<0>();
  w_read_all<4>(x, W); w_read_all<5>(x, W);
  asm volatile("s_waitcnt lgkmcnt(8)" ::: "memory");
  w_dma_all<6>(x);
  asm volatile("s_waitcnt lgkmcnt(0)" ::: "memory");
  w_dma_all<7>(x);
  late.rd[0] = x.rd[0]; late.rd[1] = x.rd[1];
  STK(3, 7);
  poll8((lane & 7) * 128, ws + OFF_F2 + (32 * j + 8 * w) * 128);
  STK(3, 8);
}

__global__ __launch_bounds__(256, 1) void kb(const u16* __restrict__ wsp, const u16* __restrict__ bsp, char* __restrict__ ws, u16* __restrict__ out) {
  __shared__ __attribute__((aligned(16))) char sm[KB_LDS];
  const int w = __builtin_amdgcn_readfirstlane(threadIdx.x >> 6);
  int i, j; tile_of(blockIdx.x, i, j);
  const int gs = j < 7 ? 7 * j : 48, cnt = j < 6 ? 7 : 6;
  const unsigned sb = lds_addr(sm);
  const int hz = j < 4 ? 40960 : 0;
  ST_DECL
  u4 W[16][4];
  Late late;
  const char* vst = ws + OFF_V + ((size_t)4 * i * NKS3 + (gs >> 1)) * 1024;
  u64 ta = (u64)vst + (0x15 >> j & 1) * 3072, tb = (u64)(out + 64 * i * D + 80 * j);
  asm volatile("" : "+s"(ta), "+s"(tb));
  if (w < 3) phase1<2>(sm, ws, i, j, w, gs, cnt, W, late, wsp, ta, tb, st_); else phase1<1>(sm, ws, i, j, w, gs, cnt, W, late, wsp, ta, tb, st_);
  G2 h;
  unsigned bias;
  {
    const int ln = fresh_lane(), c = ln & 15;
    h.vz = ln * 16 + 3072; h.vz6 = cnt == 7 ? h.vz : h.vz - 1024;
    gld2(bias, (64 * i + 32 * (w >> 1) + 8 * (c >> 2) + 4 * (w & 1) + (c & 3)) * 2, bsp);
  }
  h.zsrc = ws + OFF_Z2 + ((size_t)16 * w * NGB + gs) * 1024;
  h.wo = ws + OFF_WO + ((size_t)5 * j * NKS3 + 7 * w) * 1024;
  h.wo6 = w == 3 ? ws + OFF_ZE - 3072 : nullptr;
  ld_z_all<0, 0>(h);
  {
    WS x;
    x.rd[0] = late.rd[0]; x.rd[1] = late.rd[1];
    w_read_all<6>(x, W); w_read_all<7>(x, W);
  }
  __builtin_amdgcn_sched_barrier(0);
  z1_block(late, sm);
  __builtin_amdgcn_sched_barrier(0);
  u4 WO[7][5];
  g2_main<0, 0>(h, W, WO, st_);
  const int l2 = fresh_lane();
  const char* rd[3];
#pragma unroll
  for (int m = 1; m < 4; ++m) {
    h.slot[m - 1] = sb + (m < 3 ? KB_RED + w * 16384 + (m - 1) * KB_SLOT : hz + w * KB_SLOT) + l2 * 16;
    rd[m - 1] = sm + (m < 3 ? KB_RED + (w ^ m) * 16384 + (m - 1) * KB_SLOT : hz + (w ^ m) * KB_SLOT) + l2 * 16;
  }
  bar();
  g2_tail<0, 0>(h, W, WO);
  asm volatile("s_nop 7" : "+v"(h.acc[0][0]), "+v"(h.acc[1][0]), "+v"(h.acc[2][0]), "+v"(h.acc[3][0]), "+v"(h.acc[4][0]), "+v"(h.acc[5][0]), "+v"(h.acc[6][0]));
  bar();
  STK(0, 6);
  {
    pin(bias);
    const float bb = bflo(bias);
    const unsigned vl = l2 * 16;
    u2 vv[7];
#pragma unroll
    for (int fb = 0; fb < 7; ++fb) {
      const f4 sum = ((h.acc[fb][0] + *(const f4*)(rd[0] + fb * 1024)) + *(const f4*)(rd[1] + fb * 1024)) + *(const f4*)(rd[2] + fb * 1024);
      const u2 z1 = *(const u2*)(sm + KB_Z1 + (w * 7 + fb) * 512 + l2 * 8);
      const unsigned s0 = pk_bf(sum[0], sum[1]), s1 = pk_bf(sum[2], sum[3]);
      const unsigned g0 = pk_bf(bflo(s0) + bb, bfhi(s0) + bb), g1 = pk_bf(bflo(s1) + bb, bfhi(s1) + bb);
      vv[fb] = (u2){pk_bf(bflo(g0) * bflo(z1.x), bfhi(g0) * bfhi(z1.x)), pk_bf(bflo(g1) * bflo(z1.y), bfhi(g1) * bfhi(z1.y))};
    }
    const char* vb = vst + w * (NKS3 * 1024);
    if (gs & 1) {
      asm volatile("global_store_dwordx2 %0, %1, %2 offset:8 sc1" ::"v"(vl), "v"(vv[0]), "s"(vb) : "memory");
#pragma unroll
      for (int p = 0; p < 3; ++p) gst16s(vl + 1024 * (p + 1), cat(vv[2 * p + 1], vv[2 * p + 2]), vb);
    } else {
#pragma unroll
      for (int p = 0; p < 3; ++p) gst16s(vl + 1024 * p, cat(vv[2 * p], vv[2 * p + 1]), vb);
      if (cnt == 7) asm volatile("global_store_dwordx2 %0, %1, %2 offset:3072 sc1" ::"v"(vl), "v"(vv[6]), "s"(vb) : "memory");
    }
  }
  waitv<0>();
  if (last_of_four(sb + KB_CNT + 8)) {
    if (l2 == 0) __hip_atomic_store((unsigned*)(ws + OFF_F3) + (8 * i + j) * 32, 1u, __ATOMIC_RELAXED, __HIP_MEMORY_SCOPE_AGENT);
  }
  STK(0, 7);
  G3 o;
  const char* vb[4];
#pragma unroll
  for (int m = 0; m < 4; ++m) vb[m] = ws + OFF_V + ((size_t)(4 * i + (m ^ w)) * NKS3 + 7 * w) * 1024;
  poll8((l2 & 7) * 128, ws + OFF_F3 + i * 1024);
  const unsigned vz6 = w == 3 ? h.vz - 1024 : h.vz;
  ld_v<0, V_PRE>(o, h.vz, vz6, vb);
  g3_loop<0>(o, WO, h.slot, h.vz, vz6, vb, st_);
  asm volatile("s_nop 7" : "+v"(o.acc[0][0]), "+v"(o.acc[1][0]), "+v"(o.acc[2][0]), "+v"(o.acc[3][0]), "+v"(o.acc[4][0]));
  bar();
  const int c3 = l2 & 15, g3 = l2 >> 4;
  const int trow3 = 64 * i + 32 * (w >> 1) + 8 * (c3 >> 2) + 4 * (w & 1) + (c3 & 3);
  const char* hp = sm + (80 * j + 4 * g3 + (c3 >> 2)) * 128 + (((2 * w + ((c3 >> 1) & 1)) ^ (2 * (((c3 >> 3) & 1) | ((g3 >> 1) << 1)))) * 16) + 8 * (c3 & 1);
  u2 y[5];
  {
    f4 p[3][5]; u2 hv[5];
#pragma unroll
    for (int db = 0; db < 5; ++db) {
      p[0][db] = *(const f4*)(rd[0] + db * 1024); p[1][db] = *(const f4*)(rd[1] + db * 1024); p[2][db] = *(const f4*)(rd[2] + db * 1024);
      hv[db] = __builtin_bit_cast(u2, __builtin_amdgcn_ds_read_tr16_b64_v4i16((LDSP s4*)(hp + db * 2048)));
    }
    __builtin_amdgcn_sched_barrier(0);
#pragma unroll
    for (int db = 0; db < 5; ++db) {
      const f4 sum = (o.acc[db][0] + p[0][db]) + (p[1][db] + p[2][db]);
      const unsigned s0 = pk_bf(sum[0], sum[1]), s1 = pk_bf(sum[2], sum[3]);
      y[db] = (u2){pk_bf(bflo(hv[db].x) + bflo(s0), bfhi(hv[db].x) + bfhi(s0)), pk_bf(bflo(hv[db].y) + bflo(s1), bfhi(hv[db].y) + bfhi(s1))};
    }
  }
  const unsigned oo = (unsigned)trow3 * (2 * D) + 160 * j;
#pragma unroll
  for (int p = 0; p < 2; ++p) {
    const auto a = __builtin_amdgcn_permlane16_swap(y[2 * p].x, y[2 * p + 1].x, false, false), b = __builtin_amdgcn_permlane16_swap(y[2 * p].y, y[2 * p + 1].y, false, false);
    asm volatile("global_store_dwordx4 %0, %1, %2 offset:%3" OSF "\n\ts_nop 1" ::"v"(oo + 32 * (g3 & 1) + 16 * (g3 >> 1)), "v"((u4){a[0], b[0], a[1], b[1]}), "s"(out), "i"(64 * p) : "memory");
  }
  asm volatile("global_store_dwordx2 %0, %1, %2 offset:128" OSF ::"v"(oo + 8 * g3), "v"(y[4]), "s"(out) : "memory");
  ST_END(1);
}

extern "C" void kernel_launch(void* const* d_in, const int* in_sizes, int n_in, void* d_out, int out_size, void* d_ws, size_t ws_size,
                              hipStream_t stream) {
  char* ws = (char*)d_ws;
  ka<<<256, 1024, 0, stream>>>((const u16*)d_in[0], (const u16*)d_in[1], (const u16*)d_in[2], (const u16*)d_in[3], (const u16*)d_in[4],
                              (const unsigned*)d_in[5], (const unsigned*)d_in[6], (const u16*)d_in[8], (const u16*)d_in[9], (const u16*)d_in[11], ws);
  kb<<<256, 256, 0, stream>>>((const u16*)d_in[9], (const u16*)d_in[10], ws, (u16*)d_out);
}
```

```cpp
#include <hip/hip_runtime.h>

typedef unsigned short u16;
typedef unsigned long long u64;
typedef __attribute__((ext_vector_type(8))) __bf16 bf8;
typedef __attribute__((ext_vector_type(2))) __bf16 bf2;
typedef __attribute__((ext_vector_type(8))) _Float16 h8;
typedef __attribute__((ext_vector_type(2))) _Float16 h2;
typedef __attribute__((ext_vector_type(4))) float f4;
typedef __attribute__((ext_vector_type(2))) float f2;
typedef __attribute__((ext_vector_type(4))) short s4;
typedef __attribute__((ext_vector_type(4))) unsigned u4;
typedef __attribute__((ext_vector_type(2))) unsigned u2;
#define LDSP __attribute__((address_space(3)))
#define DEVI __device__ __forceinline__
#define ST_DECL u64* st_ = nullptr;
#define ST(i)
#define ST_END(kid)

constexpr int L = 2048, D = 640, NM = 128, F = 1728, FH = 864;
constexpr int NGB = 54;
constexpr int NKS1 = 20;
constexpr int NKS3 = 27;

constexpr size_t OFF_UT = 0;
constexpr size_t OFF_XT = OFF_UT + (size_t)D * L * 2;
constexpr size_t OFF_KR = OFF_XT + (size_t)D * L * 2;
constexpr size_t OFF_HT = OFF_KR + (size_t)D * L * 4;
constexpr size_t OFF_WF = OFF_HT + (size_t)D * L * 2;
constexpr size_t OFF_WO = OFF_WF + (size_t)2 * NGB * NKS1 * 1024;
constexpr size_t OFF_Z1 = OFF_WO + (size_t)40 * NKS3 * 1024;
constexpr size_t OFF_Z2 = OFF_Z1 + (size_t)128 * NGB * 512;
constexpr size_t OFF_V = OFF_Z2 + (size_t)64 * NGB * 1024;
constexpr size_t OFF_ST = OFF_V + (size_t)128 * NKS3 * 1024;
constexpr size_t OFF_CSP = OFF_ST + (size_t)32 * 8 * 64 * 8;
constexpr size_t OFF_CS = OFF_CSP + (size_t)5 * F * 4;
constexpr size_t OFF_KSC = OFF_CS + (size_t)F * 4;
constexpr size_t OFF_G1 = 32u << 20;
constexpr size_t OFF_F1 = OFF_G1 + 32768;
constexpr size_t OFF_EP = OFF_G1 + 65536;
constexpr size_t OFF_F2 = OFF_G1 + 131072;
constexpr size_t OFF_F3 = OFF_F2 + 32768;
constexpr size_t OFF_ZE = OFF_F3 + 32768;

DEVI float bflo(unsigned v) { return __uint_as_float(v << 16); }
DEVI float bfhi(unsigned v) { return __uint_as_float(v & 0xffff0000u); }
DEVI unsigned pk_bf(float a, float b) { f2 p = {a, b}; return __builtin_bit_cast(unsigned, __builtin_convertvector(p, bf2)); }
DEVI unsigned pk_h(float a, float b) { f2 p = {a, b}; return __builtin_bit_cast(unsigned, __builtin_convertvector(p, h2)); }
DEVI float rbf(float a) { return bflo(pk_bf(a, 0.f)); }
DEVI float hlo(unsigned v) { return (float)__builtin_bit_cast(h2, v).x; }
DEVI float hhi(unsigned v) { return (float)__builtin_bit_cast(h2, v).y; }
DEVI f4 mfma_bf(u4 a, u4 b, f4 c) { return __builtin_amdgcn_mfma_f32_16x16x32_bf16(__builtin_bit_cast(bf8, a), __builtin_bit_cast(bf8, b), c, 0, 0, 0); }
DEVI f4 mfma_h(u4 a, u4 b, f4 c) { return __builtin_amdgcn_mfma_f32_16x16x32_f16(__builtin_bit_cast(h8, a), __builtin_bit_cast(h8, b), c, 0, 0, 0); }
template <int CTRL> DEVI float dpp(float v) { return __int_as_float(__builtin_amdgcn_update_dpp(0, __float_as_int(v), CTRL, 0xf, 0xf, true)); }
constexpr int DPP_ROR1 = 0x121, DPP_ROR2 = 0x122, DPP_ROR4 = 0x124, DPP_ROR8 = 0x128, DPP_MIRROR = 0x140, DPP_HMIRROR = 0x141, DPP_Q3210 = 0x1B, DPP_Q1032 = 0xB1;

DEVI f2 ln_stats(float s, float q, float inv_n) {
  float m = s * inv_n;
  float v = fmaxf(q * inv_n - m * m, 0.f);
  float sd = rbf(__builtin_amdgcn_sqrtf(rbf(rbf(v) + 1e-5f)));
  f2 r = {rbf(m), __builtin_amdgcn_rcpf(sd)};
  return r;
}
DEVI float gelu_t(float x) {
  float t = __builtin_fmaf(x * x, -0.1029432397f, -2.3022081985f);
  float e = __builtin_amdgcn_exp2f(x * t);
  return x * __builtin_amdgcn_rcpf(1.0f + e);
}

template <int IMM> DEVI void gld16(u4& d, unsigned voff, const void* sb) { asm volatile("global_load_dwordx4 %0, %1, %2 offset:%3" : "=v"(d) : "v"(voff), "s"(sb), "i"(IMM)); }
template <int IMM> DEVI void gld8(u2& d, unsigned voff, const void* sb) { asm volatile("global_load_dwordx2 %0, %1, %2 offset:%3" : "=v"(d) : "v"(voff), "s"(sb), "i"(IMM)); }
template <int IMM> DEVI void gld4(unsigned& d, unsigned voff, const void* sb) { asm volatile("global_load_dword %0, %1, %2 offset:%3" : "=v"(d) : "v"(voff), "s"(sb), "i"(IMM)); }
DEVI void gld2(unsigned& d, unsigned voff, const void* sb) { asm volatile("global_load_ushort %0, %1, %2" : "=v"(d) : "v"(voff), "s"(sb)); }
DEVI void dma16(unsigned ldsdst, unsigned voff, const void* sb) {
  asm volatile("s_mov_b32 m0, %0\n\ts_nop 0\n\tglobal_load_lds_dwordx4 %1, %2" ::"s"(ldsdst), "v"(voff), "s"(sb) : "memory");
}
template <int N> DEVI void waitv() { asm volatile("s_waitcnt vmcnt(%0)" ::"i"(N) : "memory"); }
template <int N> DEVI void waitv(u4& a) { asm volatile("s_waitcnt vmcnt(%1)" : "+v"(a) : "i"(N) : "memory"); }
DEVI void pin(u4& a) { asm volatile("" : "+v"(a)); }
DEVI void pin(u2& a) { asm volatile("" : "+v"(a)); }
DEVI void pin(unsigned& a) { asm volatile("" : "+v"(a)); }
DEVI void bar() { asm volatile("s_waitcnt lgkmcnt(0)\n\ts_barrier" ::: "memory"); }
DEVI unsigned lds_addr(const void* p) { return (unsigned)(size_t)(LDSP const char*)p; }

DEVI void tr8x8(const u4 (&r)[8], u4 (&o)[8]) {
#pragma unroll
  for (int c2 = 0; c2 < 4; ++c2)
#pragma unroll
    for (int m = 0; m < 4; ++m) {
      o[2 * c2][m] = __builtin_amdgcn_perm(r[2 * m + 1][c2], r[2 * m][c2], 0x05040100u);
      o[2 * c2 + 1][m] = __builtin_amdgcn_perm(r[2 * m + 1][c2], r[2 * m][c2], 0x07060302u);
    }
}

constexpr int KG_PITCH = 528;
constexpr int KC_PITCH = 4160;
constexpr int RC_SZ = 30208;
constexpr int KA_K = 3 * RC_SZ, KA_U = KA_K + 12 * KC_PITCH, U_PITCH = 4608, KA_T = KA_U + 3 * U_PITCH, T_PITCH = 1536;
constexpr int KA_S = KA_T + 3 * T_PITCH, KA_LDS = KA_S + 96;

struct c2 { f2 x, y; };
DEVI c2 cmul(c2 a, c2 b) { c2 r; r.x = a.x * b.x - a.y * b.y; r.y = a.x * b.y + a.y * b.x; return r; }
DEVI c2 cexpp(f2 a2, f2 br, float p) {
  const f2 ea = a2 * p, ph = br * p;
  const f2 e = {__builtin_amdgcn_exp2f(ea.x), __builtin_amdgcn_exp2f(ea.y)};
  const float p0 = __builtin_amdgcn_fractf(ph.x), p1 = __builtin_amdgcn_fractf(ph.y);
  c2 r;
  r.x = e * (f2){__builtin_amdgcn_cosf(p0), __builtin_amdgcn_cosf(p1)};
  r.y = e * (f2){__builtin_amdgcn_sinf(p0), __builtin_amdgcn_sinf(p1)};
  return r;
}
DEVI unsigned pk_h2(f2 p) { return __builtin_bit_cast(unsigned, __builtin_convertvector(p, h2)); }
template <int CTRL, int RM> DEVI float dppm(float v) { return __int_as_float(__builtin_amdgcn_update_dpp(0, __float_as_int(v), CTRL, RM, 0xf, false)); }
DEVI float wave_sum63(float v) {
  v += dpp<DPP_ROR8>(v); v += dpp<DPP_ROR4>(v); v += dpp<DPP_ROR2>(v); v += dpp<DPP_ROR1>(v);
  v += dppm<0x142, 0xa>(v); v += dppm<0x143, 0xc>(v);
  return v;
}
template <int IMM> DEVI void lds8(u2& d, unsigned a) { asm volatile("ds_read_b64 %0, %1 offset:%2" : "=v"(d) : "v"(a), "i"(IMM)); }
template <int IMM> DEVI void lds16(u4& d, unsigned a) { asm volatile("ds_read_b128 %0, %1 offset:%2" : "=v"(d) : "v"(a), "i"(IMM)); }
template <int N> DEVI void waitl(u2& a, u2& b, u4& c) { asm volatile("s_waitcnt lgkmcnt(%3)" : "+v"(a), "+v"(b), "+v"(c) : "i"(N) : "memory"); }
template <int IMM> DEVI void gld16s(u4& d, unsigned voff, const void* sb) { asm volatile("global_load_dwordx4 %0, %1, %2 offset:%3 sc1" : "=v"(d) : "v"(voff), "s"(sb), "i"(IMM)); }
DEVI void gld4s(unsigned& d, unsigned voff, const void* sb) { asm volatile("global_load_dword %0, %1, %2 sc1" : "=v"(d) : "v"(voff), "s"(sb)); }
DEVI unsigned perm(unsigned hi, unsigned lo, unsigned sel) { return __builtin_amdgcn_perm(hi, lo, sel); }

template <int RR, int P> DEVI void conv_ld(u2 (&al)[8], u2 (&ah)[8], u4 (&bb)[8], unsigned pa, unsigned pb) {
  if constexpr (P < 8) {
    lds8<64 * (1 - RR) + 512 * (7 - P)>(al[P], pa);
    lds8<64 * (1 - RR) + 512 * (7 - P) + 8>(ah[P], pa);
    lds16<64 * (1 - RR) + 512 * P>(bb[P], pb);
    conv_ld<RR, P + 1>(al, ah, bb, pa, pb);
  }
}
template <int S> DEVI void conv_mm(u2 (&al)[8], u2 (&ah)[8], u4 (&bb)[8], f4 (&acc)[8]) {
  if constexpr (S < 8) {
    waitl<(3 * (7 - S) > 15 ? 15 : 3 * (7 - S))>(al[S], ah[S], bb[S]);
#pragma unroll
    for (int p = 0; p <= S; ++p) acc[S] = mfma_h((u4){al[p].x, al[p].y, ah[p].x, ah[p].y}, bb[S - p], acc[S]);
    __builtin_amdgcn_sched_barrier(0);
    conv_mm<S + 1>(al, ah, bb, acc);
  }
}
template <int IMM> DEVI void gld8s(u2& d, unsigned voff, const void* sb) { asm volatile("global_load_dwordx2 %0, %1, %2 offset:%3 sc1" : "=v"(d) : "v"(voff), "s"(sb), "i"(IMM)); }
DEVI void gst16s(unsigned voff, u4 v, const void* sb) { asm volatile("global_store_dwordx4 %0, %1, %2 sc1\n\ts_nop 1" ::"v"(voff), "v"(v), "s"(sb) : "memory"); }
DEVI u4 tr2(const char* p, int step) {
  const u2 a = __builtin_bit_cast(u2, __builtin_amdgcn_ds_read_tr16_b64_v4i16((LDSP s4*)p));
  const u2 b = __builtin_bit_cast(u2, __builtin_amdgcn_ds_read_tr16_b64_v4i16((LDSP s4*)(p + step)));
  return (u4){a.x, a.y, b.x, b.y};
}
template <bool TWO, int K> DEVI void tbl_ld(u4 (&a)[8], u4 (&c)[8], u4 (&e)[8], unsigned pa, unsigned pb) {
  if constexpr (K < 8) {
    lds16<64 * K>(a[K], pa); lds16<64 * K>(c[K], pb);
    if constexpr (TWO) lds16<64 * K + 16 * KG_PITCH>(e[K], pb);
    tbl_ld<TWO, K + 1>(a, c, e, pa, pb);
  }
}
template <bool TWO, int K> DEVI void tbl_mm(u4 (&a)[8], u4 (&c)[8], u4 (&e)[8], f4& acc, f4& ac0) {
  if constexpr (K < 8) {
    constexpr int N = (TWO ? 3 : 2) * (7 - K);
    if constexpr (TWO) asm volatile("s_waitcnt lgkmcnt(%3)" : "+v"(a[K]), "+v"(c[K]), "+v"(e[K]) : "i"(N > 15 ? 15 : N) : "memory");
    else asm volatile("s_waitcnt lgkmcnt(%2)" : "+v"(a[K]), "+v"(c[K]) : "i"(N) : "memory");
    acc = mfma_h(a[K], c[K], acc);
    if constexpr (TWO) ac0 = mfma_h(e[K], c[K], ac0);
    __builtin_amdgcn_sched_barrier(0);
    tbl_mm<TWO, K + 1>(a, c, e, acc, ac0);
  }
}
template <bool TWO> DEVI void tbl_tiles(unsigned pa, unsigned pb, f4& acc, f4& ac0) {
  u4 a[8], c[8], e[8];
  tbl_ld<TWO, 0>(a, c, e, pa, pb);
  tbl_mm<TWO, 0>(a, c, e, acc, ac0);
}
DEVI void chsync(unsigned fl, int wq, unsigned phase) {
  asm volatile("s_waitcnt lgkmcnt(0)\n\tds_write_b32 %0, %1" ::"v"(fl + 4 * wq), "v"(phase) : "memory");
  for (;;) {
    u4 v;
    asm volatile("ds_read_b128 %0, %1\n\ts_waitcnt lgkmcnt(0)" : "=v"(v) : "v"(fl) : "memory");
    if (__builtin_amdgcn_readfirstlane(min(min(v.x, v.y), min(v.z, v.w))) >= phase) break;
    __builtin_amdgcn_s_sleep(1);
  }
}
DEVI void rawbar() { asm volatile("s_barrier" ::: "memory"); }
template <int I> DEVI void poll_ld(u4 (&g)[4], unsigned go, const char* base) {
  if constexpr (I < 4) { gld16s<16 * I>(g[I], go, base); poll_ld<I + 1>(g, go, base); }
}
DEVI void wait4(u4 (&g)[4]) { asm volatile("s_waitcnt vmcnt(0)" : "+v"(g[0]), "+v"(g[1]), "+v"(g[2]), "+v"(g[3])::"memory"); }

constexpr int KA_CODE_LINES = 118, KB_CODE_LINES = 290;
__global__ __launch_bounds__(1024, 4) void ka(const u16* __restrict__ x, const u16* __restrict__ logA, const u16* __restrict__ Aim,
                                          const u16* __restrict__ Cre, const u16* __restrict__ Cim, const unsigned* __restrict__ logdt,
                                          const unsigned* __restrict__ dskip, const u16* __restrict__ w_in, const u16* __restrict__ wsp,
                                          const u16* __restrict__ w_out, char* __restrict__ ws, const u16* __restrict__ bsp,
                                          const u16* __restrict__ out) {
  __shared__ __attribute__((aligned(16))) char sm[KA_LDS];
  const int tid = threadIdx.x, lane = tid & 63;
  const int w = __builtin_amdgcn_readfirstlane(tid >> 6);
  const int b = blockIdx.x;
  ST_DECL
  const int g = b & 7, m = b >> 3;
  const bool isconv = m < 27;
  const int d0 = 80 * g + (3 * m < 77 ? 3 * m : 77);
  if (tid < 16) ((int*)(sm + KA_S + 32))[tid] = 0;
  unsigned tag = 0;
  if (w < 4) {
    u4 r0, r1; u2 r2;
    unsigned ep;
    const int row = 256 * g + 8 * m + 2 * w + (lane >> 5), tm = 4 * g + (m >> 3), tg = m & 7;
    const unsigned ro = (unsigned)row * 1280 + (lane & 31) * 16;
    gld4s(ep, 0, ws + OFF_EP);
    gld16<0>(r0, ro, x); gld16<512>(r1, ro, x); gld8<1024>(r2, ro - (lane & 31) * 8, x);
    bar();
    {
      const char* tsrc = (const char*)x + (size_t)tm * 64 * 1280 + tg * 160;
      const unsigned dst = lds_addr(sm) + KA_U + w * 1024;
#pragma unroll
      for (int i = 0; i < 3; ++i) { const unsigned pc = tid + 256 * i < 640 ? tid + 256 * i : 639; dma16(dst + i * 4096, (pc / 10) * 1280 + (pc % 10) * 16, tsrc); }
    }
    __builtin_amdgcn_s_setprio(2);
    asm volatile("s_waitcnt vmcnt(3)" : "+v"(ep), "+v"(r0), "+v"(r1), "+v"(r2)::"memory");
    ST(1);
    tag = ep + 0x9E3779B9u;
    f2 s0 = {bflo(r2.x), bfhi(r2.x)}, s1 = {bflo(r2.y), bfhi(r2.y)}, q0 = s0 * s0, q1 = s1 * s1;
#pragma unroll
    for (int c = 0; c < 4; ++c) {
      const unsigned v = r0[c], v2 = r1[c];
      const f2 a = {bflo(v), bfhi(v)}, a2 = {bflo(v2), bfhi(v2)};
      s0 += a; q0 += a * a; s1 += a2; q1 += a2 * a2;
    }
    s0 += s1; q0 += q1;
    float ss = s0.x + s0.y, qq = q0.x + q0.y;
    ss += dpp<DPP_ROR8>(ss); qq += dpp<DPP_ROR8>(qq); ss += dpp<DPP_ROR4>(ss); qq += dpp<DPP_ROR4>(qq);
    ss += dpp<DPP_ROR2>(ss); qq += dpp<DPP_ROR2>(qq); ss += dpp<DPP_ROR1>(ss); qq += dpp<DPP_ROR1>(qq);
    ss += dppm<0x142, 0xa>(ss); qq += dppm<0x142, 0xa>(qq);
    const float mean = ss * (1.0f / D);
    const float var = fmaxf(qq * (1.0f / D) - mean * mean, 0.f);
    unsigned pay = pk_bf(mean, __builtin_amdgcn_sqrtf(rbf(rbf(var) + 1e-5f)));
    pin(pay);
    u4 gv = {(unsigned)__builtin_amdgcn_readlane(pay, 31), tag, pay, tag};
    pin(gv);
    waitv<0>();
    if (lane == 63) gst16s((row - 1) * 8, gv, ws + OFF_G1);
    chsync(lds_addr(sm) + KA_S + 80, w, 1);
    ST(2);
#pragma unroll 1
    for (int bl = w; bl < 5; bl += 4) {
      const int l = lane & 15, gm = lane >> 4;
      const char* tp = sm + KA_U + (8 * gm + (l >> 2)) * 160 + bl * 32 + (l & 3) * 8;
      const u4 A = tr2(tp, 640), B = tr2(tp + 32 * 160, 640);
      u4 s1v, s2v;
#pragma unroll
      for (int e = 0; e < 4; ++e) {
        const unsigned ae = A[e], be = B[e];
        s1v[e] = __builtin_amdgcn_update_dpp(ae, be, DPP_ROR8, 0xf, 0xc, false);
        s2v[e] = __builtin_amdgcn_update_dpp(be, ae, DPP_ROR8, 0xf, 0x3, false);
      }
      const unsigned vo = (unsigned)(80 * tg + 16 * bl + (l & 7)) * 4096 + tm * 128 + (gm + 4 * (l >> 3)) * 16;
      gst16s(vo, s1v, ws + OFF_XT);
      gst16s(vo + 8 * 4096, s2v, ws + OFF_XT);
    }
    waitv<0>();
    chsync(lds_addr(sm) + KA_S + 80, w, 2);
    if (tid == 0) __hip_atomic_store((unsigned*)(ws + OFF_F1) + (32 * tg + tm) * 32, tag, __ATOMIC_RELAXED, __HIP_MEMORY_SCOPE_AGENT);
    ST(3);
    if (isconv) {
      u4 gr[4], xr[3];
      poll_ld<0>(gr, tid * 64, ws + OFF_G1);
      if (w == 0) {
#pragma unroll 1
        for (int spin = 0; spin < (1 << 16); ++spin) {
          unsigned v;
          asm volatile("global_load_dword %0, %1, %2 sc1\n\ts_waitcnt vmcnt(0)" : "=v"(v) : "v"((lane & 31) * 128), "s"(ws + OFF_F1 + g * 4096) : "memory");
          if (__all(v == tag)) break;
          __builtin_amdgcn_s_sleep(2);
        }
      }
      wait4(gr);
      chsync(lds_addr(sm) + KA_S + 80, w, 3);
      ST(4);
      const char* xsrc = ws + OFF_XT + (size_t)d0 * 4096;
      gld16s<0>(xr[0], tid * 16, xsrc); gld16s<0>(xr[1], tid * 16, xsrc + 4096); gld16s<0>(xr[2], tid * 16, xsrc + 8192);
#pragma unroll 1
      for (int spin = 0; spin < (1 << 16); ++spin) {
        bool ok = true;
#pragma unroll
        for (int i = 0; i < 4; ++i) ok = ok && gr[i].y == tag && gr[i].w == tag;
        if (__all(ok)) break;
        poll_ld<0>(gr, tid * 64, ws + OFF_G1);
        wait4(gr);
      }
      f2 mm[4], rs[4];
#pragma unroll
      for (int i = 0; i < 4; ++i) { mm[i] = (f2){bflo(gr[i].x), bflo(gr[i].z)}; rs[i] = (f2){__builtin_amdgcn_rcpf(bfhi(gr[i].x)), __builtin_amdgcn_rcpf(bfhi(gr[i].z))}; }
      if (tid < 90) *(u4*)(sm + KA_U + (tid / 30) * U_PITCH + (tid % 30) * 16) = (u4){0, 0, 0, 0};
      ST(7);
      asm volatile("s_waitcnt vmcnt(0)" : "+v"(xr[0]), "+v"(xr[1]), "+v"(xr[2])::"memory");
      ST(8);
#pragma unroll
      for (int c = 0; c < 3; ++c) {
        u4 o;
#pragma unroll
        for (int i = 0; i < 4; ++i) { const unsigned v = xr[c][i]; o[i] = pk_h2(((f2){bflo(v), bfhi(v)} - mm[i]) * rs[i]); }
        *(u4*)(sm + KA_U + c * U_PITCH + 480 + tid * 16) = o;
      }
      __builtin_amdgcn_s_setprio(0);
      bar();
      ST(6);
      if (b == 0 && tid == 0) __hip_atomic_store((unsigned*)(ws + OFF_EP), tag, __ATOMIC_RELAXED, __HIP_MEMORY_SCOPE_AGENT);
      __builtin_amdgcn_s_sleep(4);
      if (w != 0) {
        unsigned pw[4];
#pragma unroll
        for (int i = 0; i < 4; ++i) {
          const unsigned ln = (i * 81 + m * 3 + w - 1) * 64 + lane;
          gld4<0>(pw[i], (ln < 16384 ? ln : 16383) * 128, (const char*)wsp + (size_t)(g >> 1) * (16384 * 128));
        }
        if (m < 2) {
          const unsigned ln = (m * 3 + w - 1) * 64 + lane;
          const char* code;
          unsigned pc;
          asm volatile("s_getpc_b64 s[44:45]\n\ts_add_u32 s44, s44, _Z2kbPKtS0_PcPt@rel32@lo+4\n\ts_addc_u32 s45, s45, _Z2kbPKtS0_PcPt@rel32@hi+12\n\ts_mov_b64 %0, s[44:45]"
                       : "=s"(code)::"s44", "s45", "scc");
          gld4<0>(pc, (ln < KB_CODE_LINES ? ln : KB_CODE_LINES - 1) * 128, code);
          asm volatile("s_waitcnt vmcnt(0)" : "+v"(pc)::"memory");
        }
        if (m >= 2 && m < 5) {
          const int q = (m - 2) * 3 + w - 1;
          const char* pb = q < 5 ? ws + OFF_HT + q * (64 * 55296) : q == 5 ? (const char*)bsp : q == 6 ? (const char*)out : ws + OFF_F2;
          unsigned pt;
          gld4s(pt, lane * (q < 5 ? 55296 : q == 5 ? 64 : q == 6 ? 40960 : 1024), pb);
          asm volatile("s_waitcnt vmcnt(0)" : "+v"(pt)::"memory");
        }
        asm volatile("s_waitcnt vmcnt(0)" : "+v"(pw[0]), "+v"(pw[1]), "+v"(pw[2]), "+v"(pw[3])::"memory");
      } else {
      const int item = b * 40 + lane;
      if (lane < 40) {
        const int oc = item % 80, kq = item / 80, kap = kq >> 2, qq = kq & 3;
        u4 r[8], o[8];
#pragma unroll
        for (int e = 0; e < 8; ++e) r[e] = *(const u4*)(w_out + (size_t)(32 * kap + 16 * (e >> 2) + 4 * qq + (e & 3)) * D + oc * 8);
        tr8x8(r, o);
        u4* dst = (u4*)(ws + OFF_WO + ((size_t)(oc >> 1) * NKS3 + kap) * 1024) + qq * 16 + (oc & 1) * 8;
#pragma unroll
        for (int c = 0; c < 8; ++c) dst[c] = o[c];
      }
      }
      ST_END(0);
      return;
    }
    __builtin_amdgcn_s_setprio(0);
  }
  if (!isconv) {
    unsigned pc = 0;
    if (w == 4) {
      const unsigned ln = (m - 27) * 64 + lane;
      const char* code;
      asm volatile("s_getpc_b64 s[44:45]\n\ts_add_u32 s44, s44, _Z2kaPKtS0_S0_S0_S0_PKjS2_S0_S0_S0_PcS0_S0_@rel32@lo+4\n\t"
                   "s_addc_u32 s45, s45, _Z2kaPKtS0_S0_S0_S0_PKjS2_S0_S0_S0_PcS0_S0_@rel32@hi+12\n\ts_mov_b64 %0, s[44:45]" : "=s"(code)::"s44", "s45", "scc");
      gld4<0>(pc, (ln < KA_CODE_LINES ? ln : KA_CODE_LINES - 1) * 128, code);
    }
    if (w >= 4) rawbar();
    rawbar();
    if (w == 4) asm volatile("s_waitcnt vmcnt(0)" : "+v"(pc)::"memory");
    const int si = b - 216;
    if (si < 27) {
      const int tt = tid - 384, co = tt & 7, ko = tt >> 3, c0 = 64 * si + 8 * co, k0 = 8 * ko;
      float* part = (float*)(sm + 98304);
      if (w >= 6) {
        u4 r[8], o[8];
#pragma unroll
        for (int e = 0; e < 8; ++e) r[e] = *(const u4*)(w_in + (size_t)(k0 + e) * F + c0);
        float cs[8];
#pragma unroll
        for (int c = 0; c < 4; ++c) {
          f2 s = {0.f, 0.f};
#pragma unroll
          for (int e = 0; e < 8; ++e) { const unsigned v = r[e][c]; s += (f2){bflo(v), bfhi(v)}; }
          cs[2 * c] = s.x + dpp<DPP_ROR8>(s.x); cs[2 * c + 1] = s.y + dpp<DPP_ROR8>(s.y);
        }
        if (!(lane & 8)) {
          f4* p = (f4*)(part + ((w - 6) * 4 + (lane >> 4)) * 64 + co * 8);
          p[0] = (f4){cs[0], cs[1], cs[2], cs[3]}; p[1] = (f4){cs[4], cs[5], cs[6], cs[7]};
        }
        tr8x8(r, o);
#pragma unroll
        for (int c = 0; c < 8; ++c) *(u4*)(sm + tt * 144 + c * 16) = o[c];
      }
      __syncthreads();
      if (w < 8) {
        float s = 0.f;
#pragma unroll
        for (int e = 0; e < 5; ++e) s += part[(w * 5 + e) * 64 + lane];
        part[2560 + w * 64 + lane] = s;
      }
#pragma unroll
      for (int i = 0; i < 5; ++i) {
        const int f = w * 5 + i, blk = f / 20, ks = f % 20, gc = 4 * si + blk;
        const u4 v = *(const u4*)(sm + ((4 * ks + (lane >> 4)) * 8 + 2 * blk + ((lane >> 3) & 1)) * 144 + (lane & 7) * 16);
        *(u4*)(ws + OFF_WF + ((size_t)(gc >= NGB ? gc - NGB : gc + NGB) * NKS1 + ks) * 1024 + lane * 16) = v;
      }
      __syncthreads();
      if (w == 0) {
        float s = 0.f;
#pragma unroll
        for (int e = 0; e < 8; ++e) s += part[2560 + e * 64 + lane];
        ((float*)(ws + OFF_CS))[64 * si + lane] = s;
      }
    } else if (si < 35) {
      ((u4*)(ws + OFF_ST))[(si - 27) * 1024 + tid] = (u4){0, 0, 0, 0};
    } else if (si == 35) {
      if (tid < 576) *(u4*)(ws + OFF_F2 + (tid < 512 ? tid * 128 : 65536 + (tid - 512) * 16)) = (u4){0, 0, 0, 0};
    }
    ST_END(0);
    return;
  }
  const int ch = w < 4 ? 0 : (w >> 2) - 1, role = (w + ch) & 3;
  const int d = d0 + ch;
  const int col = lane & 15, q = lane >> 4;
  char* rc = sm + ch * RC_SZ;
  float* slot = (float*)(sm + KA_S) + 2 * ch;
  const unsigned fl = lds_addr(sm) + KA_S + 32 + 16 * ch;
  if (w >= 4) {
    unsigned la, ai, cr, ci, ldw, dw;
    gld4<0>(ldw, 0, logdt + (d >> 1));
    gld4<0>(dw, 0, dskip + (d >> 1));
    gld4<0>(la, lane * 4, logA + d * NM);
    gld4<0>(ai, lane * 4, Aim + d * NM);
    gld4<0>(cr, lane * 4, Cre + d * NM);
    gld4<0>(ci, lane * 4, Cim + d * NM);
    rawbar();
    asm volatile("s_waitcnt vmcnt(0)" : "+v"(la), "+v"(ai), "+v"(cr), "+v"(ci), "+v"(ldw), "+v"(dw)::"memory");
    ST(1);
    const float dt = rbf(__expf((d & 1) ? bfhi(ldw) : bflo(ldw)));
    const unsigned ea = pk_bf(__expf(bflo(la)), __expf(bfhi(la)));
    const f2 a2 = (f2){bflo(ea), bfhi(ea)} * (-1.4426950408889634f * dt);
    f2 br = (f2){bflo(ai), bfhi(ai)} * (dt * 0.15915494309189535f);
    char* base = rc + lane * 8;
    auto put = [&](int row, c2 v) { *(u2*)(base + row * KG_PITCH) = (u2){pk_h2(v.x), pk_h2(v.y)}; };
    if (role == 0) {
      c2 v = {{bflo(cr), bfhi(cr)}, {bflo(ci), bfhi(ci)}};
      float mx = fmaxf(fmaxf(fabsf(v.x.x), fabsf(v.x.y)), fmaxf(fabsf(v.y.x), fabsf(v.y.y)));
      mx = fmaxf(mx, dpp<DPP_ROR8>(mx)); mx = fmaxf(mx, dpp<DPP_ROR4>(mx)); mx = fmaxf(mx, dpp<DPP_ROR2>(mx)); mx = fmaxf(mx, dpp<DPP_ROR1>(mx));
      mx = fmaxf(mx, dppm<0x142, 0xa>(mx)); mx = fmaxf(mx, dppm<0x143, 0xc>(mx));
      mx = __int_as_float(__builtin_amdgcn_readlane(__float_as_int(mx), 63));
      const float dsk = (d & 1) ? bfhi(dw) : bflo(dw);
      const float sc = fmaxf(fmaxf(2.0f * dt * mx, fabsf(dsk) * (1.0f / 16384.0f)), 1e-30f);
      const float isc = 1.0f / sc;
      if (lane == 0) { slot[0] = sc; slot[1] = dsk * isc; }
      v.x *= 2.0f * dt * isc; v.y *= 2.0f * dt * isc;
      const c2 s = cexpp(a2, br, 8.f);
#pragma unroll
      for (int j = 0; j < 16; ++j) { put(j, v); if (j < 15) v = cmul(v, s); }
    } else {
      br = -br;
      const c2 h = cexpp(a2, br, 128.f);
      if (role == 1) {
        const c2 s = cexpp(a2, br, 1.f);
        put(16, (c2){{1.f, 1.f}, {0.f, 0.f}}); put(24, h);
        c2 v = s;
#pragma unroll
        for (int c = 1; c < 8; ++c) { put(16 + c, v); put(24 + c, cmul(v, h)); if (c < 7) v = cmul(v, s); }
      } else if (role == 2) {
        const c2 s = cexpp(a2, br, 2.f);
        c2 v = cmul(h, h);
        const c2 q = cmul(s, s), y = cmul(v, v), z = cmul(y, h);
        put(40, y); put(41, cmul(y, q)); put(42, z); put(43, cmul(z, q));
#pragma unroll
        for (int c = 0; c < 4; ++c) { put(32 + c, v); put(36 + c, cmul(v, h)); if (c < 3) v = cmul(v, s); }
      } else {
        const c2 q = cexpp(a2, br, 4.f);
        c2 v = cexpp(a2, br, 768.f);
        put(44, v); put(45, cmul(v, q)); v = cmul(v, h);
        put(46, v); put(47, cmul(v, q));
#pragma unroll
        for (int i = 0; i < 9; ++i) { v = cmul(v, h); put(48 + i, v); }
      }
    }
  ST(2);
  chsync(fl, w & 3, 1);
  char* tb = sm + KA_T + ch * T_PITCH;
  {
    const int wq = w & 3;
    const unsigned pb = lds_addr(rc) + col * KG_PITCH + q * 16;
    f4 acc = {0.f, 0.f, 0.f, 0.f}, ac0 = {0.f, 0.f, 0.f, 0.f};
    const float dadd = slot[1];
    if (wq == 3) tbl_tiles<true>(pb + 32 * KG_PITCH, pb, acc, ac0); else tbl_tiles<false>(pb + (wq < 2 ? 48 : 40) * KG_PITCH, pb, acc, ac0);
    const unsigned p0 = pk_h(acc[0], acc[1]), p1 = pk_h(acc[2], acc[3]);
    if (wq < 2) {
      u16* t = (u16*)tb + 512 + 64 * q + col;
      t[0] = (u16)p0; t[16] = (u16)(p0 >> 16); t[32] = (u16)p1; t[48] = (u16)(p1 >> 16);
    } else if (wq == 2) {
      if (q < 2) { *(unsigned*)(tb + 2 * (384 + 64 * q + 2 * col)) = p0; *(unsigned*)(tb + 2 * (416 + 64 * q + 2 * col)) = p1; }
      else if (lane == 32) ((u16*)tb)[512] = (u16)p0;
    } else {
      if (q < 2) *(u2*)(tb + 2 * (256 + 64 * q + 4 * col)) = (u2){p0, p1};
      else if (lane == 32) ((u16*)tb)[384] = (u16)p0;
      if (lane == 0) ac0[0] += dadd;
      *(u2*)(tb + 2 * (128 * (q >> 1) + 4 * (q & 1) + 8 * col)) = (u2){pk_h(ac0[0], ac0[1]), pk_h(ac0[2], ac0[3])};
    }
  }
  asm volatile("s_waitcnt lgkmcnt(0)" ::: "memory");
  ST(3);
  {
    const int t8 = tid & 255, a = 255 - t8, wq = w & 3;
    u4 o0, o1;
    if (wq < 2) {
      const u16* t = (const u16*)tb + 384 + a;
      const unsigned lo = t[0], hi = t[1];
      const unsigned hh = perm(hi, hi, 0x01000100u), ll = perm(lo, lo, 0x01000100u), tie = (a & 1) ? hi : lo;
      o0 = (u4){hh, perm(tie, hi, 0x05040100u), ll, ll}; o1 = (u4){hh, perm(lo, tie, 0x05040100u), ll, ll};
    } else if (wq == 2) {
      const unsigned* t = (const unsigned*)(tb + 2 * (256 + 2 * a));
      const unsigned v01 = t[0], v2 = t[1];
      const unsigned t11 = perm(v01, v01, 0x03020302u), t00 = perm(v01, v01, 0x01000100u);
      o0 = (u4){perm(v2, v2, 0x01000100u), t11, perm(v01, v01, 0x01000302u), t00}; o1 = (u4){perm(v01, v2, 0x07060100u), t11, t00, t00};
    } else if (a >= 32) {
      const unsigned* t = (const unsigned*)(tb + 2 * (128 + 4 * a));
      const unsigned vm = t[-1], v01 = t[0], v23 = t[1], v4 = t[2];
      const unsigned t22 = perm(v23, v23, 0x01000100u);
      o0 = (u4){perm(v23, v4, 0x07060100u), t22, perm(v01, v23, 0x07060100u), perm(v01, v01, 0x01000100u)};
      o1 = (u4){perm(v23, v23, 0x01000302u), t22, perm(v01, v01, 0x01000302u), perm(vm, v01, a == 32 ? 0x07060100u : 0x01000100u)};
    } else {
      const u4 v = *(const u4*)(tb + 16 * a);
      const unsigned vm = a ? *(const unsigned*)(tb + 16 * a - 4) : 0u;
      o0 = (u4){perm(v.w, v.w, 0x01000302u), perm(v.z, v.z, 0x01000302u), perm(v.y, v.y, 0x01000302u), perm(v.x, v.x, 0x01000302u)};
      o1 = (u4){perm(v.z, v.w, 0x07060100u), perm(v.y, v.z, 0x07060100u), perm(v.x, v.y, 0x07060100u), perm(vm, v.x, 0x07060100u)};
    }
    char* kcb = sm + KA_K + ch * 4 * KC_PITCH;
    char* kc = kcb + 16 * t8;
    *(u4*)kc = o0; *(u4*)(kc + KC_PITCH) = o1;
    unsigned* k2 = (unsigned*)(kc + 2 * KC_PITCH - 4);
    unsigned* k3 = (unsigned*)(kc + 3 * KC_PITCH - 4);
    k2[0] = o0.x; k2[1] = o0.y; k2[2] = o0.z; k2[3] = o0.w;
    k3[0] = o1.x; k3[1] = o1.y; k3[2] = o1.z; k3[3] = o1.w;
    if (wq == 0 && lane < 36) { const int s = lane / 9; *(unsigned*)(kcb + s * KC_PITCH + 4096 - (s >> 1) * 4 + (lane - 9 * s) * 4) = 0; }
  }
  ST(4);
  }
  ST(5);
  bar();
  ST(6);
  const int wq = w & 3;
  u2 xv[2];
  gld8s<0>(xv[0], (512 * wq + 16 * col + 4 * q) * 2, ws + OFF_XT + (size_t)d * 4096);
  gld8s<512>(xv[1], (512 * wq + 16 * col + 4 * q) * 2, ws + OFF_XT + (size_t)d * 4096);
  f4 acc[8];
#pragma unroll
  for (int n = 0; n < 8; ++n) acc[n] = (f4){0.f, 0.f, 0.f, 0.f};
  {
    const int s = (3 - col) & 3;
    const unsigned pa = lds_addr(sm) + KA_K + (4 * ch + s) * KC_PITCH + 2 * (2031 - s - col + 8 * q) - 64 * (2 * wq + 1) - 512 * 7;
    const unsigned pb = lds_addr(sm) + KA_U + ch * U_PITCH + 448 + 32 * col + 16 * q - 64 * (2 * wq + 1);
    u2 al[8], ah[8]; u4 bb[8];
    conv_ld<0, 0>(al, ah, bb, pa, pb);
    conv_mm<0>(al, ah, bb, acc);
    __builtin_amdgcn_sched_barrier(0);
    conv_ld<1, 0>(al, ah, bb, pa, pb);
    conv_mm<0>(al, ah, bb, acc);
  }
  ST(7);
  f4 tot[2];
  {
    f4* red = (f4*)(rc + wq * 6144) + lane;
#pragma unroll
    for (int v = 0; v < 4; ++v) {
      if (v == wq) { tot[0] = acc[2 * v]; tot[1] = acc[2 * v + 1]; }
      else { const int sl = v > wq ? v - 1 : v; red[sl * 128] = acc[2 * v]; red[sl * 128 + 64] = acc[2 * v + 1]; }
    }
  }
  chsync(fl, w & 3, 2);
  ST(8);
#pragma unroll
  for (int v = 0; v < 3; ++v) {
    const int src = v < wq ? v : v + 1;
    const f4* rp = (const f4*)(rc + src * 6144) + (wq > src ? wq - 1 : wq) * 128 + lane;
    tot[0] += rp[0]; tot[1] += rp[64];
  }
  const float sc = slot[0];
  asm volatile("s_waitcnt vmcnt(0)" : "+v"(xv[0]), "+v"(xv[1])::"memory");
#pragma unroll
  for (int nn = 0; nn < 2; ++nn) {
    const int t = 512 * wq + 256 * nn + 16 * col + 4 * q;
    const int pos = (t & ~31) + 16 * ((t >> 2) & 1) + 4 * ((t >> 3) & 3);
    const float h[4] = {__builtin_fmaf(tot[nn][0], sc, bflo(xv[nn].x)), __builtin_fmaf(tot[nn][1], sc, bfhi(xv[nn].x)),
                        __builtin_fmaf(tot[nn][2], sc, bflo(xv[nn].y)), __builtin_fmaf(tot[nn][3], sc, bfhi(xv[nn].y))};
    *(u2*)(ws + OFF_HT + (size_t)(pos >> 6) * 81920 + d * 128 + (pos & 63) * 2) = (u2){pk_bf(h[0], h[1]), pk_bf(h[2], h[3])};
  }
  ST_END(0);
}

DEVI void tile_of(int b, int& i, int& j) { const int g = b & 7, r = b >> 3; i = (g >> 1) * 8 + (r >> 2); j = (g & 1) * 4 + (r & 3); }
constexpr int KB_RED = 81920, KB_SLOT = 7168, KB_Z1 = KB_RED + 65536, KB_S2 = KB_Z1 + 14336, KB_S3 = KB_S2 + 512,
              KB_CNT = KB_S3 + 512, KB_LDS = KB_CNT + 16;
#define KSP 10
#define KFL 15
constexpr int KFLAG = KFL;
constexpr int KSPLIT = KSP;

#define OSF ""
#define POLL_GAP "4"
#define WO_LATE 25
#define TCH 0
#define STSET 0
#define STK(set, i) do { if constexpr (STSET == set) ST(i); } while (0)
template <bool Z> DEVI void mmv(f4& d, const u4& a, const u4& b) {
  if constexpr (Z) asm volatile("v_mfma_f32_16x16x32_bf16 %0, %1, %2, 0" : "=v"(d) : "v"(a), "v"(b));
  else asm volatile("v_mfma_f32_16x16x32_bf16 %0, %1, %2, %0" : "+v"(d) : "v"(a), "v"(b));
}
template <bool Z> DEVI void mmva(f4& d, const u4& a, const u4& b) {
  if constexpr (Z) asm volatile("v_mfma_f32_16x16x32_bf16 %0, %1, %2, 0" : "=v"(d) : "v"(a), "a"(b));
  else asm volatile("v_mfma_f32_16x16x32_bf16 %0, %1, %2, %0" : "+v"(d) : "v"(a), "a"(b));
}
template <bool Z> DEVI void mmav(f4& d, const u4& a, const u4& b) {
  if constexpr (Z) asm volatile("v_mfma_f32_16x16x32_bf16 %0, %1, %2, 0" : "=v"(d) : "a"(a), "v"(b));
  else asm volatile("v_mfma_f32_16x16x32_bf16 %0, %1, %2, %0" : "+v"(d) : "a"(a), "v"(b));
}
template <int IMM> DEVI void gld16a(u4& d, unsigned voff, const void* sb) { asm volatile("global_load_dwordx4 %0, %1, %2 offset:%3" : "=a"(d) : "v"(voff), "s"(sb), "i"(IMM)); }
template <int IMM> DEVI void rld16(u4& d, unsigned voff, const void* sb) { asm volatile("global_load_dwordx4 %0, %1, %2 offset:%3" : "+v"(d) : "v"(voff), "s"(sb), "i"(IMM)); }
template <int IMM> DEVI void rld16s(u4& d, unsigned voff, const void* sb) { asm volatile("global_load_dwordx4 %0, %1, %2 offset:%3 sc1" : "+v"(d) : "v"(voff), "s"(sb), "i"(IMM)); }
template <int IMM> DEVI void trd(u2& d, unsigned a) { asm volatile("ds_read_b64_tr_b16 %0, %1 offset:%2" : "=v"(d) : "v"(a), "i"(IMM)); }
template <int IMM> DEVI void ldsw16(unsigned a, const f4& v) { asm volatile("ds_write_b128 %0, %1 offset:%2" ::"v"(a), "v"(v), "i"(IMM) : "memory"); }
DEVI void dot2(float& acc, unsigned a, unsigned b) { asm volatile("v_dot2_f32_bf16 %0, %1, %2, %0" : "+v"(acc) : "v"(a), "v"(b)); }
DEVI void dot2s(float& acc, unsigned a, unsigned b) { asm volatile("v_dot2_f32_bf16 %0, %1, %2, %0" : "+v"(acc) : "v"(a), "s"(b)); }
DEVI void settle(f4& a, f4& b, f4& c, f4& d) { asm volatile("s_nop 7" : "+v"(a), "+v"(b), "+v"(c), "+v"(d)); }
DEVI void hold(f4& a, f4& b, f4& c, f4& d) { asm volatile("" : "+v"(a), "+v"(b), "+v"(c), "+v"(d)); }
DEVI u4 cat(const u2& a, const u2& b) { return (u4){a.x, a.y, b.x, b.y}; }
DEVI f2 gelu2(f2 x) {
  const f2 u = x * (x * x * -0.1029432397f + -2.3022081985f);
  const f2 d = (f2){__builtin_amdgcn_exp2f(u.x), __builtin_amdgcn_exp2f(u.y)} + 1.0f;
  return x * (f2){__builtin_amdgcn_rcpf(d.x), __builtin_amdgcn_rcpf(d.y)};
}
template <int CTRL, int H> DEVI void bfly(float (&v)[16], bool up) {
#pragma unroll
  for (int k = 0; k < H; ++k) { const float send = up ? v[k] : v[k + H], keep = up ? v[k + H] : v[k]; v[k] = keep + dpp<CTRL>(send); }
}
DEVI float rows_sum(float v) {
  auto a = __builtin_amdgcn_permlane32_swap(__float_as_uint(v), __float_as_uint(v), false, false);
  v = __uint_as_float(a[0]) + __uint_as_float(a[1]);
  auto b = __builtin_amdgcn_permlane16_swap(__float_as_uint(v), __float_as_uint(v), false, false);
  return __uint_as_float(b[0]) + __uint_as_float(b[1]);
}
DEVI int fresh_lane() {
  int l;
  asm volatile("v_mbcnt_lo_u32_b32 %0, -1, 0\n\tv_mbcnt_hi_u32_b32 %0, -1, %0" : "=v"(l));
  return l;
}
DEVI bool last_of_four(unsigned cnt_addr) {
  unsigned o = 0;
  if (fresh_lane() == 0) asm volatile("ds_add_rtn_u32 %0, %1, %2\n\ts_waitcnt lgkmcnt(0)" : "=v"(o) : "v"(cnt_addr), "v"(1u) : "memory");
  return __builtin_amdgcn_readfirstlane(o) == 3;
}
#define POLL1(R) "s_waitcnt vmcnt(2)\n\tv_cmp_ne_u32 vcc, 0, " R "\n\ts_cmp_eq_u64 vcc, -1\n\ts_cbranch_scc1 2f\n\tglobal_load_dword " R ", %4, %5 sc1\n\t"
DEVI void poll8(unsigned (&t)[3], unsigned voff, const void* sb) {
  unsigned n;
  asm volatile(
      "s_mov_b32 %3, 0x20000\n\tglobal_load_dword %0, %4, %5 sc1\n\ts_sleep " POLL_GAP "\n\tglobal_load_dword %1, %4, %5 sc1\n\ts_sleep " POLL_GAP "\n\t"
      "global_load_dword %2, %4, %5 sc1\n1:\n\t" POLL1("%0") POLL1("%1") POLL1("%2") "s_sub_u32 %3, %3, 1\n\ts_cbranch_scc0 1b\n2:"
      : "=&v"(t[0]), "=&v"(t[1]), "=&v"(t[2]), "=&s"(n) : "v"(voff), "s"(sb) : "vcc", "scc", "memory");
}

struct G1 {
  u2 al[2][4], ah[2][4];
  u4 ring[8][2], ones;
  unsigned touch[4], va[2], sb;
  const char* hsrc;
  f4 acc[2][4], sx, sq;
  unsigned pa[4], vb[2];
  const char *b2, *b1;
  u64* st_;
};
struct WS { unsigned vw[2], rd[2], ring; const char* src[4]; };
template <int C, int M> DEVI void w_dma(const WS& x) {
  asm volatile("s_mov_b32 m0, %0\n\ts_nop 0\n\tglobal_load_lds_dwordx4 %1, %3\n\tglobal_load_lds_dwordx4 %2, %3 offset:1024"
               ::"s"(x.ring + (C & 1) * 8192 + M * 2048), "v"(x.vw[0]), "v"(x.vw[1]), "s"(x.src[M] + C * 128) : "memory");
}
DEVI void w_dma_rt(const WS& x, int C, int M) {
  asm volatile("s_mov_b32 m0, %0\n\ts_nop 0\n\tglobal_load_lds_dwordx4 %1, %3\n\tglobal_load_lds_dwordx4 %2, %3 offset:1024"
               ::"s"(x.ring + (C & 1) * 8192 + M * 2048), "v"(x.vw[0]), "v"(x.vw[1]), "s"(x.src[M] + C * 128) : "memory");
}
template <int C, int M, int KH> DEVI void w_read(const WS& x, u4 (&W)[16][4]) {
  asm volatile("ds_read_b128 %0, %1 offset:%2" : "=a"(W[2 * C + KH][M]) : "v"(x.rd[KH]), "i"((C & 1) * 8192 + M * 2048));
}
template <int C> DEVI void w_dma_all(const WS& x) { w_dma<C, 0>(x); w_dma<C, 1>(x); w_dma<C, 2>(x); w_dma<C, 3>(x); }
template <int C> DEVI void w_read_all(const WS& x, u4 (&W)[16][4]) {
  w_read<C, 0, 0>(x, W); w_read<C, 0, 1>(x, W); w_read<C, 1, 0>(x, W); w_read<C, 1, 1>(x, W);
  w_read<C, 2, 0>(x, W); w_read<C, 2, 1>(x, W); w_read<C, 3, 0>(x, W); w_read<C, 3, 1>(x, W);
}
DEVI void w_setup(WS& x, int lane, int w, int i, unsigned sb, const u16* wsp) {
  const int c = lane & 15, g4 = lane >> 4;
  x.ring = sb + KB_RED + w * 16384;
#pragma unroll
  for (int o = 0; o < 2; ++o) {
    x.vw[o] = (unsigned)(16 * o + 8 * (lane >> 5) + ((lane >> 3) & 3)) * 4096 + (((lane & 7) ^ ((4 * o + g4) & 7)) * 16) - 1024 * o;
    x.rd[o] = x.ring + c * 128 + (((4 * o + g4) ^ ((c >> 1) & 7)) * 16);
  }
#pragma unroll
  for (int m = 0; m < 4; ++m) x.src[m] = (const char*)wsp + (size_t)(64 * i + 32 * ((m ^ w) >> 1) + 4 * ((m ^ w) & 1)) * 4096 + w * 1024;
}
template <int KS> DEVI const char* wf_base(const char* b) { return b + (KS >> 3) * 8192 + 4096; }
template <int KS, int X> DEVI void ld_new(G1& g, u4& d, const char* b) { gld16<(KS & 7) * 1024 - 4096>(d, g.vb[X], wf_base<KS>(b)); }
template <int KS, int X> DEVI void ld_ring(G1& g, u4& d, const char* b) { rld16<(KS & 7) * 1024 - 4096>(d, g.vb[X], wf_base<KS>(b)); }
template <int S, int M> DEVI void rd_lo(G1& g) { trd<(S & 15) * 4096>(g.al[S & 1][M], g.pa[M]); }
template <int S, int M> DEVI void rd_hi(G1& g) { trd<(S & 15) * 4096 + 512>(g.ah[S & 1][M], g.pa[M]); }
template <int N, int LG> DEVI void g1_wait(G1& g, int c, int m) {
  asm volatile("s_waitcnt vmcnt(%4) lgkmcnt(%5)" : "+v"(g.al[c][m]), "+v"(g.ah[c][m]), "+v"(g.al[c][m + 1]), "+v"(g.ah[c][m + 1]) : "i"(N), "i"(LG) : "memory");
}
template <int S> DEVI void pa_step(G1& g) {
  if constexpr (S == 16) { for (int m = 0; m < 4; ++m) g.pa[m] += 65536; }
  if constexpr (S == 20) { for (int m = 0; m < 4; ++m) g.pa[m] -= 65536; }
}
template <int KS, int H> DEVI void a_dma(const G1& g) {
  asm volatile("s_mov_b32 m0, %0\n\ts_nop 0\n\tglobal_load_lds_dwordx4 %1, %3\n\tglobal_load_lds_dwordx4 %2, %3 offset:1024"
               ::"s"(g.sb + KS * 4096 + H * 2048), "v"(g.va[0]), "v"(g.va[1]), "s"(g.hsrc + KS * 4096 + H * 2048) : "memory");
}
template <int G, int X> DEVI void b_next(G1& g) {
  if constexpr (G + 8 < 20) ld_ring<G + 8, X>(g, g.ring[G & 7][X], g.b2);
  else if constexpr (G + 8 < 40) ld_ring<G + 8 - 20, X>(g, g.ring[G & 7][X], g.b1);
}
constexpr bool w_rd_at(int s) { return (s >= 0 && s < 8) || (s >= 10 && s < 18); }
constexpr bool w_dma_at(int s) { return w_rd_at(s - 1); }
constexpr int w_tile(int s) { return s < 8 ? s : s - 2; }
constexpr int ops_at(int nb, int k) { return (k < 32 ? nb : 0) + (nb == 1 && k < 10 ? 4 : 0) + (nb == 1 && k == 20 ? 3 : 0) + (w_dma_at(k - 20) ? 2 : 0); }
constexpr int ops_in(int nb, int a, int b) { int n = 0; for (int k = a; k < b; ++k) n += ops_at(nb, k); return n; }
constexpr int imin(int a, int b) { return a < b ? a : b; }
constexpr int g1_vm(int nb, int G) {
  int n = G < 4 ? nb * (7 - G) + 6 + ops_in(nb, 0, G) : G < 8 ? nb * (7 - G) + ops_in(nb, 0, G) : ops_in(nb, G - 7, G);
  if (G == 3) n = imin(n, 4 * nb + ops_in(nb, 0, 3));
  if (nb == 1 && G == 9) n = imin(n, 1 + ops_in(1, 5, 9));
  if (nb == 1 && G == 14) n = imin(n, 1 + ops_in(1, 10, 14));
  if (G >= 20 && G < 28) n = 2 * (27 - G) + ops_in(nb, 20, G);
  if (G >= 30 && G < 38) n = imin(n, (G - 9 < 32 ? nb - 1 : 0) + ops_in(nb, G - 8, G));
  return imin(n, 63);
}
template <int NB, int S> DEVI void g1_first(G1& g) {
  if constexpr (S < 20) {
    constexpr int c = S & 1, nx = (S + 1) % 20;
    constexpr bool Z = S == 0;
    g1_wait<g1_vm(NB, S), 4>(g, c, 0);
    if constexpr (S == 3 || S == 9 || S == 14) asm volatile("s_barrier" ::: "memory");
    { u64* st_ = g.st_; if constexpr (S == 1) STK(1, 4); if constexpr (S == 5) STK(1, 5); if constexpr (S == 10) STK(1, 6); if constexpr (S == 15) STK(1, 7); if constexpr (S == 19) STK(1, 8); }
    pa_step<S + 1>(g);
    const u4& b0 = g.ring[S & 7][0]; const u4& b1 = g.ring[S & 7][1];
    const u4 a0 = cat(g.al[c][0], g.ah[c][0]), a1 = cat(g.al[c][1], g.ah[c][1]);
    mmv<Z>(g.acc[0][0], a0, b0); rd_lo<nx, 0>(g);
    if constexpr (NB == 2) mmv<Z>(g.acc[1][0], a0, b1);
    rd_hi<nx, 0>(g);
    mmv<Z>(g.sx, a0, g.ones);
    if constexpr (NB == 1 && S < 10) a_dma<S + 10, 0>(g);
    mmv<Z>(g.acc[0][1], a1, b0); rd_lo<nx, 1>(g);
    if constexpr (NB == 2) mmv<Z>(g.acc[1][1], a1, b1);
    rd_hi<nx, 1>(g);
    mmv<Z>(g.sq, a0, a0);
    if constexpr (NB == 1 && S < 10) a_dma<S + 10, 1>(g);
    g1_wait<63, 4>(g, c, 2);
    const u4 a2 = cat(g.al[c][2], g.ah[c][2]), a3 = cat(g.al[c][3], g.ah[c][3]);
    mmv<Z>(g.acc[0][2], a2, b0); rd_lo<nx, 2>(g);
    if constexpr (NB == 2) mmv<Z>(g.acc[1][2], a2, b1);
    rd_hi<nx, 2>(g);
    mmv<Z>(g.acc[0][3], a3, b0); rd_lo<nx, 3>(g);
    b_next<S, 0>(g);
    if constexpr (NB == 2) mmv<Z>(g.acc[1][3], a3, b1);
    rd_hi<nx, 3>(g);
    if constexpr (NB == 2) b_next<S, 1>(g);
    g1_first<NB, S + 1>(g);
  }
}
template <int NB, int S, int X, int M> DEVI void mm1(G1& g) {
  if constexpr (X < NB) mmv<S == 0>(g.acc[X][M], g.ring[(S + 20) & 7][X], cat(g.al[S & 1][M], g.ah[S & 1][M]));
}
template <int NB, int S, int E> DEVI void g1_second(G1& g, const WS& x, u4 (&W)[16][4], unsigned tv, const char* tb) {
  if constexpr (S < E) {
    constexpr bool R = S < 19, WR = w_rd_at(S), WP = w_rd_at(S - 1);
    constexpr int T = w_tile(S), TD = w_tile(S - 1) + 8;
    g1_wait<g1_vm(NB, S + 20), (WP ? 5 : 4)>(g, S & 1, 0);
    { u64* st_ = g.st_; if constexpr (S == 0) STK(2, 1); if constexpr (S == 5) STK(2, 2); if constexpr (S == 10) STK(2, 5); if constexpr (S == 15) STK(2, 6); if constexpr (S == 19) STK(2, 7); }
    if constexpr (R) pa_step<S + 1>(g);
    if constexpr (NB == 1 && S == 0) { gld4<0>(g.touch[0], tv, tb); gld4<0>(g.touch[1], tv, tb + 45 * 128); gld4<0>(g.touch[2], tv, tb + 90 * 128); }
    mm1<NB, S, 0, 0>(g); if constexpr (R) rd_lo<S + 1, 0>(g);
    mm1<NB, S, 1, 0>(g); if constexpr (R) rd_hi<S + 1, 0>(g);
    if constexpr (WR) w_read<(T >> 2), (T & 3), 0>(x, W);
    mm1<NB, S, 0, 1>(g); if constexpr (R) rd_lo<S + 1, 1>(g);
    mm1<NB, S, 1, 1>(g); if constexpr (R) rd_hi<S + 1, 1>(g);
    g1_wait<63, (R ? (WR ? 5 : 4) : 0)>(g, S & 1, 2);
    mm1<NB, S, 0, 2>(g); if constexpr (R) rd_lo<S + 1, 2>(g);
    mm1<NB, S, 1, 2>(g); if constexpr (R) rd_hi<S + 1, 2>(g);
    if constexpr (WR) w_read<(T >> 2), (T & 3), 1>(x, W);
    mm1<NB, S, 0, 3>(g); if constexpr (R) rd_lo<S + 1, 3>(g);
    b_next<S + 20, 0>(g);
    if constexpr (w_dma_at(S)) w_dma<(TD >> 2), (TD & 3)>(x);
    mm1<NB, S, 1, 3>(g); if constexpr (R) rd_hi<S + 1, 3>(g);
    if constexpr (NB == 2) b_next<S + 20, 1>(g);
    g1_second<NB, S + 1, E>(g, x, W, tv, tb);
  }
}
template <int NB, int KS, int E> DEVI void g1_pro(G1& g) {
  if constexpr (KS < E) { ld_new<KS, 0>(g, g.ring[KS][0], g.b2); if constexpr (NB == 2) ld_new<KS, 1>(g, g.ring[KS][1], g.b2); g1_pro<NB, KS + 1, E>(g); }
}
#define FOLD2(CT, MA, MB, H) \
  "v_add_f32_dpp %0, %0, %0 " CT " row_mask:0xf bank_mask:" MA "\n\tv_add_f32_dpp %1, %1, %1 " CT " row_mask:0xf bank_mask:" MA "\n\t" \
  "v_add_f32_dpp %2, %2, %2 " CT " row_mask:0xf bank_mask:" MA "\n\tv_add_f32_dpp %3, %3, %3 " CT " row_mask:0xf bank_mask:" MA "\n\t" \
  "v_add_f32_dpp %0, %4, %4 " CT " row_mask:0xf bank_mask:" MB "\n\tv_add_f32_dpp %1, %5, %5 " CT " row_mask:0xf bank_mask:" MB "\n\t" \
  "v_add_f32_dpp %2, %6, %6 " CT " row_mask:0xf bank_mask:" MB "\n\tv_add_f32_dpp %3, %7, %7 " CT " row_mask:0xf bank_mask:" MB
DEVI void fold16(float (&v)[16]) {
  asm volatile("s_nop 1\n\t" FOLD2("row_mirror", "0x3", "0xc", 8) : "+v"(v[0]), "+v"(v[1]), "+v"(v[2]), "+v"(v[3]) : "v"(v[8]), "v"(v[9]), "v"(v[10]), "v"(v[11]));
  asm volatile("s_nop 1\n\t" FOLD2("row_mirror", "0x3", "0xc", 8) : "+v"(v[4]), "+v"(v[5]), "+v"(v[6]), "+v"(v[7]) : "v"(v[12]), "v"(v[13]), "v"(v[14]), "v"(v[15]));
  asm volatile(FOLD2("row_half_mirror", "0x5", "0xa", 4) "\n\t"
               "v_add_f32_dpp %0, %0, %0 quad_perm:[2,3,0,1] row_mask:0xf bank_mask:0xf\n\tv_add_f32_dpp %1, %1, %1 quad_perm:[2,3,0,1] row_mask:0xf bank_mask:0xf\n\t"
               "v_add_f32_dpp %2, %2, %2 quad_perm:[2,3,0,1] row_mask:0xf bank_mask:0xf\n\tv_add_f32_dpp %3, %3, %3 quad_perm:[2,3,0,1] row_mask:0xf bank_mask:0xf\n\t"
               "v_add_f32_dpp %0, %0, %0 quad_perm:[1,0,3,2] row_mask:0xf bank_mask:0xf\n\tv_add_f32_dpp %1, %1, %1 quad_perm:[1,0,3,2] row_mask:0xf bank_mask:0xf\n\t"
               "v_add_f32_dpp %2, %2, %2 quad_perm:[1,0,3,2] row_mask:0xf bank_mask:0xf\n\tv_add_f32_dpp %3, %3, %3 quad_perm:[1,0,3,2] row_mask:0xf bank_mask:0xf"
               : "+v"(v[0]), "+v"(v[1]), "+v"(v[2]), "+v"(v[3]) : "v"(v[4]), "v"(v[5]), "v"(v[6]), "v"(v[7]));
}

struct G2 {
  u4 z[4][7];
  f4 acc[7][4];
  unsigned vz, vz6, slot[3];
  const char *zsrc, *wo, *wo6;
};
template <int S, int FB> DEVI void ld_z(G2& g) {
  const char* sb = g.zsrc + (size_t)S * (NGB * 1024);
  if constexpr (S < 4) gld16s<(FB - 3) * 1024>(g.z[S][FB], FB == 6 ? g.vz6 : g.vz, sb);
  else rld16s<(FB - 3) * 1024>(g.z[S & 3][FB], FB == 6 ? g.vz6 : g.vz, sb);
}
template <int S, int FB> DEVI void ld_z_step(G2& g) { ld_z<S, FB>(g); if constexpr (FB < 6) ld_z_step<S, FB + 1>(g); }
template <int N> DEVI void ld_wo(G2& g, u4 (&WO)[7][5]) {
  constexpr int KS = N / 5, DB = N % 5;
  if constexpr (KS < 6) gld16a<(KS - 3) * 1024>(WO[KS][DB], g.vz, g.wo + DB * (NKS3 * 1024));
  else gld16a<0>(WO[KS][DB], g.vz, g.wo6 == nullptr ? g.wo + DB * (NKS3 * 1024) + 3072 : g.wo6);
}
template <int S, int FB> DEVI void g2_main(G2& g, u4 (&W)[16][4], u4 (&WO)[7][5], u64* st_) {
  if constexpr (S < 14) {
    if constexpr (S == 12 && FB == 0) asm volatile("s_waitcnt lgkmcnt(0)" ::: "memory");
    waitv<27>();
    if constexpr (FB == 0) { if constexpr (S == 0) { STK(0, 5); STK(4, 1); } if constexpr (S == 2) STK(4, 2); if constexpr (S == 4) STK(4, 4); if constexpr (S == 6) STK(4, 5); if constexpr (S == 8) STK(4, 6); if constexpr (S == 10) STK(4, 7); if constexpr (S == 13) STK(4, 8); }
    mmva<S == 0>(g.acc[FB][0], g.z[S & 3][FB], W[S][0]); mmva<S == 0>(g.acc[FB][1], g.z[S & 3][FB], W[S][1]);
    mmva<S == 0>(g.acc[FB][2], g.z[S & 3][FB], W[S][2]); mmva<S == 0>(g.acc[FB][3], g.z[S & 3][FB], W[S][3]);
    if constexpr (S < 12) ld_z<S + 4, FB>(g); else ld_wo<7 * (S - 12) + FB>(g, WO);
    if constexpr (FB < 6) g2_main<S, FB + 1>(g, W, WO, st_); else g2_main<S + 1, 0>(g, W, WO, st_);
  }
}
template <int GI, int K> DEVI void g2_tail(G2& g, u4 (&W)[16][4], u4 (&WO)[7][5]) {
  if constexpr (GI < 4) {
    constexpr int m = (GI + 1) & 3, s = 14 + K / 7, fb = K % 7;
    if constexpr (GI == 0 && K == 0) waitv<21>();
    if constexpr (GI == 0 && K == 7) waitv<14>();
    mmva<false>(g.acc[fb][m], g.z[s & 3][fb], W[s][m]);
    if constexpr (GI > 0 && K >= 1 && K < 8) ldsw16<(K - 1) * 1024>(g.slot[GI - 1], g.acc[K - 1][GI]);
    if constexpr (K >= 8 && 14 + 6 * GI + K - 8 < WO_LATE) ld_wo<14 + 6 * GI + K - 8>(g, WO);
    if constexpr (K < 13) g2_tail<GI, K + 1>(g, W, WO); else g2_tail<GI + 1, 0>(g, W, WO);
  }
}
template <int N> DEVI void ld_wo_rest(G2& g, u4 (&WO)[7][5]) { if constexpr (N < 35) { ld_wo<N>(g, WO); ld_wo_rest<N + 1>(g, WO); } }
struct G3 { u4 v[4][7]; f4 acc[5][4]; };
constexpr int V_PRE = 14;
template <int N, int E> DEVI void ld_v(G3& g, unsigned vo, unsigned vo6, const char* const (&vb)[4]) {
  if constexpr (N < E && N < 28) { gld16s<(N % 7 - 3) * 1024>(g.v[N / 7][N % 7], N % 7 == 6 ? vo6 : vo, vb[(N / 7 + 1) & 3]); ld_v<N + 1, E>(g, vo, vo6, vb); }
}
template <int N> DEVI void g3_loop(G3& g, u4 (&WO)[7][5], const unsigned (&slot)[3], unsigned vo, unsigned vo6, const char* const (&vb)[4], u64* st_) {
  if constexpr (N < 28) {
    constexpr int GI = N / 7, s = N % 7, m = (GI + 1) & 3;
    waitv<imin(V_PRE + 2 * N, 28) - 1 - N>();
    if constexpr (N == 0) STK(0, 8);
    mmav<s == 0>(g.acc[0][m], WO[s][0], g.v[GI][s]); mmav<s == 0>(g.acc[1][m], WO[s][1], g.v[GI][s]);
    ld_v<V_PRE + 2 * N, V_PRE + 2 * N + 1>(g, vo, vo6, vb);
    mmav<s == 0>(g.acc[2][m], WO[s][2], g.v[GI][s]); mmav<s == 0>(g.acc[3][m], WO[s][3], g.v[GI][s]);
    ld_v<V_PRE + 2 * N + 1, V_PRE + 2 * N + 2>(g, vo, vo6, vb);
    mmav<s == 0>(g.acc[4][m], WO[s][4], g.v[GI][s]);
    if constexpr (GI > 0 && s >= 1 && s < 6) ldsw16<(s - 1) * 1024>(slot[GI - 1], g.acc[s - 1][GI]);
    g3_loop<N + 1>(g, WO, slot, vo, vo6, vb, st_);
  }
}

struct Late { f4 acc[4]; float r[4], n[4]; unsigned o[4], rd[2], t[3]; u4 cs; };
DEVI void z1_tile(const Late& t, char* sm, int m) {
  const f2 c0 = {__uint_as_float(t.cs.x), __uint_as_float(t.cs.y)}, c1 = {__uint_as_float(t.cs.z), __uint_as_float(t.cs.w)};
  const f2 y0 = gelu2((f2){t.acc[m][0], t.acc[m][1]} * t.r[m] + c0 * t.n[m]), y1 = gelu2((f2){t.acc[m][2], t.acc[m][3]} * t.r[m] + c1 * t.n[m]);
  *(u2*)(sm + t.o[m]) = (u2){pk_bf(y0.x, y0.y), pk_bf(y1.x, y1.y)};
}
template <int NB> DEVI void phase1(char* sm, char* ws, const int i, const int j, const int w, const int gs, const int cnt, u4 (&W)[16][4], Late& late, const u16* wsp, u64 ta, u64 tb_, u64* st_) {
  const int lane = fresh_lane();
  const int c = lane & 15, g4 = lane >> 4;
  const bool liveA = 2 * w < cnt;
  const int ga = gs + (liveA ? 2 * w : cnt - 1), gb = ga + 1;
  const unsigned sb = lds_addr(sm);
  G1 g;
  g.st_ = st_;
  g.vb[0] = (unsigned)ga * (NKS1 * 1024) + lane * 16; g.vb[1] = g.vb[0] + NKS1 * 1024;
  g.b2 = ws + OFF_WF; g.b1 = ws + OFF_WF + (size_t)NGB * NKS1 * 1024;
  unsigned cs2[2]; u4 cs1[2];
  {
    const int r8 = lane >> 3, sg = (r8 >> 1) & 1;
    g.va[0] = r8 * 128 + (((lane & 7) ^ (2 * sg)) * 16); g.va[1] = r8 * 128 + (((lane & 7) ^ (2 * sg + 4)) * 16);
    g.sb = sb; g.hsrc = ws + OFF_HT + (size_t)i * 81920;
    const unsigned vo = w & 1 ? g.va[1] : g.va[0];
#pragma unroll
    for (int n = 0; n < 4; ++n) dma16(sb + (w + 4 * n) * 1024, vo, g.hsrc + (w + 4 * n) * 1024);
#pragma unroll
    for (int x = 0; x < NB; ++x) { gld4<0>(cs2[x], (FH + 16 * (ga + x) + c) * 4, ws + OFF_CS); gld16<0>(cs1[x], (16 * (ga + x) + 4 * g4) * 4, ws + OFF_CS); }
    g1_pro<NB, 0, 4>(g);
    STK(5, 1);
#pragma unroll
    for (int n = 4; n < 10; ++n) dma16(sb + (w + 4 * n) * 1024, vo, g.hsrc + (w + 4 * n) * 1024);
    g1_pro<NB, 4, 8>(g);
    if constexpr (NB == 1) {
      ta += ((lane >> 3) & 3) * (NKS3 * 1024) + (lane & 7) * 128; tb_ += (lane & 31) * (4 * D);
      const u64 tp = TCH == 1 ? ta : tb_ + ((ta - tb_) & (0ull - (lane < 32)));
      if (TCH != 2) asm volatile("global_load_dword %0, %1, off sc1" : "=v"(g.touch[3]) : "v"(tp)); else g.touch[3] = 0;
    }
  }
  STK(5, 4);
  WS x;
  w_setup(x, lane, w, i, sb, wsp);
  {
    const int qq = c >> 2, p = c & 3, sh = (qq >> 1) | ((g4 & 1) << 1);
#pragma unroll
    for (int m = 0; m < 4; ++m) {
      g.pa[m] = sb + (8 * g4 + qq) * 128 + (p >> 1) * 16 + 8 * (p & 1) + (m ^ w ^ sh) * 32;
    }
  }
  g.ones = (u4){0x3f803f80u, 0x3f803f80u, 0x3f803f80u, 0x3f803f80u};
  unsigned aq[4], al[4], zo[2], z1o[4], po, so;
#pragma unroll
  for (int m = 0; m < 4; ++m) { aq[m] = KB_S2 + (m ^ w) * 64 + g4 * 16; al[m] = KB_S2 + (m ^ w) * 64 + c * 4; z1o[m] = KB_Z1 + ((m ^ w) * 7 + 2 * w) * 512 + lane * 8; }
  zo[0] = (unsigned)(w >> 1) * (NGB * 1024) + lane * 16; zo[1] = (unsigned)(1 ^ (w >> 1)) * (NGB * 1024) + lane * 16;
  po = KB_Z1 + (w * 64 + 16 * ((c >> 2) ^ w) + 4 * g4) * 4; so = KB_S2 + (16 * w + c) * 4;
  const char* zb = ws + OFF_Z2 + ((size_t)2 * i * NGB + ga) * 1024;
  u64* st = (u64*)(ws + OFF_ST) + (size_t)i * 512;
  asm volatile("" : "+v"(aq[0]), "+v"(aq[1]), "+v"(aq[2]), "+v"(aq[3]), "+v"(al[0]), "+v"(al[1]), "+v"(al[2]), "+v"(al[3]), "+v"(zo[0]), "+v"(zo[1]),
               "+v"(z1o[0]), "+v"(z1o[1]), "+v"(z1o[2]), "+v"(z1o[3]), "+v"(po), "+v"(so), "+v"(x.vw[0]), "+v"(x.vw[1]), "+v"(x.rd[0]), "+v"(x.rd[1]));
  asm volatile("" : "+s"(x.src[0]), "+s"(x.src[1]), "+s"(x.src[2]), "+s"(x.src[3]), "+s"(x.ring), "+s"(zb), "+s"(st));
  if (w == 0 && lane < 4) *(unsigned*)(sm + KB_CNT + 4 * lane) = 0;
  if constexpr (NB == 2) asm volatile("s_waitcnt vmcnt(22)" : "+v"(cs2[0]), "+v"(cs2[1]), "+v"(cs1[0]), "+v"(cs1[1]), "+v"(g.ones)::"memory");
  else asm volatile("s_waitcnt vmcnt(14)" : "+v"(cs2[0]), "+v"(cs1[0]), "+v"(g.ones)::"memory");
  STK(5, 5);
  bar();
  STK(0, 1); STK(1, 1); STK(5, 6);
  rd_lo<0, 0>(g); rd_hi<0, 0>(g); rd_lo<0, 1>(g); rd_hi<0, 1>(g); rd_lo<0, 2>(g); rd_hi<0, 2>(g); rd_lo<0, 3>(g); rd_hi<0, 3>(g);
  g1_first<NB, 0>(g);
  asm volatile("s_nop 1" : "+v"(g.sx), "+v"(g.sq));
  {
    const float sx = c & 2 ? (c & 1 ? g.sx[3] : g.sx[2]) : (c & 1 ? g.sx[1] : g.sx[0]);
    const float sq = c & 2 ? (c & 1 ? g.sq[3] : g.sq[2]) : (c & 1 ? g.sq[1] : g.sq[0]);
    const f2 st = ln_stats(sx, sq, 1.0f / D);
    if ((c >> 2) == g4) { *(float*)(sm + so) = st.y; *(float*)(sm + so + 256) = -st.x * st.y; }
  }
  bar();
  hold(g.acc[0][0], g.acc[0][1], g.acc[0][2], g.acc[0][3]);
  if constexpr (NB == 2) hold(g.acc[1][0], g.acc[1][1], g.acc[1][2], g.acc[1][3]);
  STK(0, 2); STK(1, 2);
  f2 z[2][8];
  float r1[4], n1[4];
  float* ps = (float*)(sm + KB_Z1); float* pq = ps + 256;
  {
    float sv[16], qv[16];
    f4 r4[4], n4[4];
#pragma unroll
    for (int m = 0; m < 4; ++m) { r4[m] = *(const f4*)(sm + aq[m]); n4[m] = *(const f4*)(sm + aq[m] + 256); }
#pragma unroll
    for (int m = 0; m < 4; ++m) { r1[m] = *(const float*)(sm + al[m]); n1[m] = *(const float*)(sm + al[m] + 256); }
#pragma unroll
    for (int m = 0; m < 4; ++m) {
#pragma unroll
      for (int e = 0; e < 2; ++e) {
        const f2 r = {r4[m][2 * e], r4[m][2 * e + 1]}, n = {n4[m][2 * e], n4[m][2 * e + 1]};
#pragma unroll
        for (int y = 0; y < NB; ++y) z[y][2 * m + e] = gelu2((f2){g.acc[y][m][2 * e], g.acc[y][m][2 * e + 1]} * r + n * __uint_as_float(cs2[y]));
        f2 s2 = z[0][2 * m + e], q2 = s2 * s2;
        if constexpr (NB == 2) { s2 += z[1][2 * m + e]; q2 += z[1][2 * m + e] * z[1][2 * m + e]; }
        sv[4 * m + 2 * e] = s2.x; sv[4 * m + 2 * e + 1] = s2.y; qv[4 * m + 2 * e] = q2.x; qv[4 * m + 2 * e + 1] = q2.y;
        __builtin_amdgcn_sched_barrier(0);
        w_dma_rt(x, (2 * m + e) >> 2, (2 * m + e) & 3);
        __builtin_amdgcn_sched_barrier(0);
      }
    }
    fold16(sv); fold16(qv);
    if ((c & 3) == 0) {
      *(f4*)(sm + po) = liveA ? (f4){sv[0], sv[1], sv[2], sv[3]} : (f4){0.f, 0.f, 0.f, 0.f};
      *(f4*)(sm + po + 1024) = liveA ? (f4){qv[0], qv[1], qv[2], qv[3]} : (f4){0.f, 0.f, 0.f, 0.f};
    }
    if (last_of_four(sb + KB_CNT)) {
      const float ts = (ps[lane] + ps[64 + lane]) + (ps[128 + lane] + ps[192 + lane]), tq = (pq[lane] + pq[64 + lane]) + (pq[128 + lane] + pq[192 + lane]);
      __hip_atomic_store(st + j * 64 + lane, ((u64)(__float_as_uint(tq) | 0x80000000u) << 32) | __float_as_uint(ts), __ATOMIC_RELAXED, __HIP_MEMORY_SCOPE_AGENT);
    }
  }
  asm volatile("" : "+v"(r1[0]), "+v"(r1[1]), "+v"(r1[2]), "+v"(r1[3]), "+v"(n1[0]), "+v"(n1[1]), "+v"(n1[2]), "+v"(n1[3]));
  ST(3);
  const unsigned tv = (lane < 45 ? lane : 44) * 128;
  const char* tb = ws + OFF_WO + (size_t)5 * j * NKS3 * 1024 + (i & 7) * 135 * 128;
  g1_second<NB, 0, KSPLIT>(g, x, W, tv, tb);
  if constexpr (NB == 1) asm volatile("" : "+v"(g.touch[0]), "+v"(g.touch[1]), "+v"(g.touch[2]), "+v"(g.touch[3]));
  float* rs3 = (float*)(sm + KB_S3); float* nm3 = rs3 + 64;
  STK(2, 8);
  if constexpr (NB == 1) {
    u2 gv[8]; unsigned n;
    asm volatile(
        "s_mov_b32 %8, 0x40000\n"
        "1:\n\tglobal_load_dwordx2 %0, %9, %10 sc1\n\tglobal_load_dwordx2 %1, %9, %10 offset:512 sc1\n\tglobal_load_dwordx2 %2, %9, %10 offset:1024 sc1\n\t"
        "global_load_dwordx2 %3, %9, %10 offset:1536 sc1\n\tglobal_load_dwordx2 %4, %9, %10 offset:2048 sc1\n\tglobal_load_dwordx2 %5, %9, %10 offset:2560 sc1\n\t"
        "global_load_dwordx2 %6, %9, %10 offset:3072 sc1\n\tglobal_load_dwordx2 %7, %9, %10 offset:3584 sc1\n\ts_waitcnt vmcnt(0)\n\t"
        "v_cmp_gt_i64 vcc, 0, %0\n\tv_cmp_gt_i64 s[44:45], 0, %1\n\tv_cmp_gt_i64 s[46:47], 0, %2\n\tv_cmp_gt_i64 s[48:49], 0, %3\n\t"
        "s_and_b64 vcc, vcc, s[44:45]\n\ts_and_b64 s[46:47], s[46:47], s[48:49]\n\ts_and_b64 s[50:51], vcc, s[46:47]\n\t"
        "v_cmp_gt_i64 vcc, 0, %4\n\tv_cmp_gt_i64 s[44:45], 0, %5\n\tv_cmp_gt_i64 s[46:47], 0, %6\n\tv_cmp_gt_i64 s[48:49], 0, %7\n\t"
        "s_and_b64 vcc, vcc, s[44:45]\n\ts_and_b64 s[46:47], s[46:47], s[48:49]\n\ts_and_b64 vcc, vcc, s[46:47]\n\ts_and_b64 vcc, vcc, s[50:51]\n\t"
        "s_sub_u32 %8, %8, 1\n\ts_cbranch_scc1 2f\n\ts_cmp_eq_u64 vcc, -1\n\ts_cbranch_scc1 2f\n\ts_sleep 1\n\ts_branch 1b\n2:"
        : "=&v"(gv[0]), "=&v"(gv[1]), "=&v"(gv[2]), "=&v"(gv[3]), "=&v"(gv[4]), "=&v"(gv[5]), "=&v"(gv[6]), "=&v"(gv[7]), "=&s"(n)
        : "v"(lane * 8), "s"(st)
        : "vcc", "scc", "s44", "s45", "s46", "s47", "s48", "s49", "s50", "s51", "memory");
    float ts = 0.f, tq = 0.f;
#pragma unroll
    for (int v = 0; v < 8; ++v) { ts += __uint_as_float(gv[v].x); tq += __uint_as_float(gv[v].y & 0x7fffffffu); }
    const f2 s3 = ln_stats(ts, tq, 1.0f / FH);
    rs3[lane] = s3.y; nm3[lane] = -s3.x * s3.y;
  }
  bar();
  STK(0, 4); STK(2, 4); STK(3, 4);
  if (liveA) {
    unsigned o[2][8];
#pragma unroll
    for (int m = 0; m < 4; ++m) {
      const f4 r4 = *(const f4*)(sm + aq[m] + 512), n4 = *(const f4*)(sm + aq[m] + 768);
#pragma unroll
      for (int e = 0; e < 2; ++e) {
        const f2 r = {r4[2 * e], r4[2 * e + 1]}, n = {n4[2 * e], n4[2 * e + 1]};
#pragma unroll
        for (int x = 0; x < NB; ++x) { const f2 a = z[x][2 * m + e] * r + n; o[x][2 * m + e] = pk_bf(a.x, a.y); }
      }
    }
    const unsigned r0 = zo[0], r1 = zo[1];
    if (w & 1) {
#pragma unroll
      for (int x = 0; x < NB; ++x) { gst16s(r0, (u4){o[x][2], o[x][3], o[x][0], o[x][1]}, zb + x * 1024); gst16s(r1, (u4){o[x][6], o[x][7], o[x][4], o[x][5]}, zb + x * 1024); }
    } else {
#pragma unroll
      for (int x = 0; x < NB; ++x) { gst16s(r0, (u4){o[x][0], o[x][1], o[x][2], o[x][3]}, zb + x * 1024); gst16s(r1, (u4){o[x][4], o[x][5], o[x][6], o[x][7]}, zb + x * 1024); }
    }
  }
  __builtin_amdgcn_sched_barrier(0);
  STK(3, 1);
  g1_second<NB, KSPLIT, KFLAG>(g, x, W, tv, tb);
  waitv<ops_in(NB, KSPLIT + 20, KFLAG + 20)>();
  if (last_of_four(sb + KB_CNT + 4)) {
    if (lane == 0) __hip_atomic_store((unsigned*)(ws + OFF_F2) + (32 * j + i) * 32, 1u, __ATOMIC_RELAXED, __HIP_MEMORY_SCOPE_AGENT);
  }
  STK(3, 2);
  g1_second<NB, KFLAG, 20>(g, x, W, tv, tb);
  STK(3, 5);
  settle(g.acc[0][0], g.acc[0][1], g.acc[0][2], g.acc[0][3]);
#pragma unroll
  for (int m = 0; m < 4; ++m) { late.acc[m] = g.acc[0][m]; late.r[m] = r1[m]; late.n[m] = n1[m]; late.o[m] = z1o[m]; }
  late.cs = cs1[0];
  if constexpr (NB == 2) {
#pragma unroll
    for (int m = 0; m < 4; ++m) z1_tile(late, sm, m);
    hold(g.acc[1][0], g.acc[1][1], g.acc[1][2], g.acc[1][3]);
#pragma unroll
    for (int m = 0; m < 4; ++m) { late.acc[m] = g.acc[1][m]; late.o[m] += 512; }
    late.cs = cs1[1];
  }
  __builtin_amdgcn_sched_barrier(0);
  STK(3, 6);
  waitv<0>();
  w_read_all<4>(x, W); w_read_all<5>(x, W);
  asm volatile("s_waitcnt lgkmcnt(8)" ::: "memory");
  w_dma_all<6>(x);
  asm volatile("s_waitcnt lgkmcnt(0)" ::: "memory");
  w_dma_all<7>(x);
  late.rd[0] = x.rd[0]; late.rd[1] = x.rd[1];
  STK(3, 7);
  poll8(late.t, (lane & 7) * 128, ws + OFF_F2 + (32 * j + 8 * w) * 128);
  STK(3, 8);
}

__global__ __launch_bounds__(256, 1) void kb(const u16* __restrict__ wsp, const u16* __restrict__ bsp, char* __restrict__ ws, u16* __restrict__ out) {
  __shared__ __attribute__((aligned(16))) char sm[KB_LDS];
  const int w = __builtin_amdgcn_readfirstlane(threadIdx.x >> 6);
  int i, j; tile_of(blockIdx.x, i, j);
  const int gs = j < 7 ? 7 * j : 48, cnt = j < 6 ? 7 : 6;
  const unsigned sb = lds_addr(sm);
  const int hz = j < 4 ? 40960 : 0;
  ST_DECL
  u4 W[16][4];
  Late late;
  const char* vst = ws + OFF_V + ((size_t)4 * i * NKS3 + (gs >> 1)) * 1024;
  u64 ta = (u64)vst + (0x15 >> j & 1) * 3072, tb = (u64)(out + 64 * i * D + 80 * j);
  asm volatile("" : "+s"(ta), "+s"(tb));
  if (w < 3) phase1<2>(sm, ws, i, j, w, gs, cnt, W, late, wsp, ta, tb, st_); else phase1<1>(sm, ws, i, j, w, gs, cnt, W, late, wsp, ta, tb, st_);
  G2 h;
  unsigned bias;
  {
    const int ln = fresh_lane(), c = ln & 15;
    h.vz = ln * 16 + 3072; h.vz6 = cnt == 7 ? h.vz : h.vz - 1024;
    gld2(bias, (64 * i + 32 * (w >> 1) + 8 * (c >> 2) + 4 * (w & 1) + (c & 3)) * 2, bsp);
  }
  h.zsrc = ws + OFF_Z2 + ((size_t)16 * w * NGB + gs) * 1024;
  h.wo = ws + OFF_WO + ((size_t)5 * j * NKS3 + 7 * w) * 1024;
  h.wo6 = w == 3 ? ws + OFF_ZE - 3072 : nullptr;
  ld_z_step<0, 0>(h);
  {
    WS x;
    x.rd[0] = late.rd[0]; x.rd[1] = late.rd[1];
    w_read_all<6>(x, W); w_read_all<7>(x, W);
  }
  __builtin_amdgcn_sched_barrier(0);
  z1_tile(late, sm, 0);
  __builtin_amdgcn_sched_barrier(0);
  ld_z_step<1, 0>(h);
  __builtin_amdgcn_sched_barrier(0);
  z1_tile(late, sm, 1);
  __builtin_amdgcn_sched_barrier(0);
  ld_z_step<2, 0>(h);
  __builtin_amdgcn_sched_barrier(0);
  z1_tile(late, sm, 2); z1_tile(late, sm, 3);
  __builtin_amdgcn_sched_barrier(0);
  ld_z_step<3, 0>(h);
  __builtin_amdgcn_sched_barrier(0);
  u4 WO[7][5];
  g2_main<0, 0>(h, W, WO, st_);
  asm volatile("" : "+v"(late.t[0]), "+v"(late.t[1]), "+v"(late.t[2]));
  const int l2 = fresh_lane();
  const char* rd[3];
#pragma unroll
  for (int m = 1; m < 4; ++m) {
    h.slot[m - 1] = sb + (m < 3 ? KB_RED + w * 16384 + (m - 1) * KB_SLOT : hz + w * KB_SLOT) + l2 * 16;
    rd[m - 1] = sm + (m < 3 ? KB_RED + (w ^ m) * 16384 + (m - 1) * KB_SLOT : hz + (w ^ m) * KB_SLOT) + l2 * 16;
  }
  bar();
  g2_tail<0, 0>(h, W, WO);
  asm volatile("s_nop 7" : "+v"(h.acc[0][0]), "+v"(h.acc[1][0]), "+v"(h.acc[2][0]), "+v"(h.acc[3][0]), "+v"(h.acc[4][0]), "+v"(h.acc[5][0]), "+v"(h.acc[6][0]));
  bar();
  STK(0, 6);
  {
    pin(bias);
    const float bb = bflo(bias);
    const unsigned vl = l2 * 16;
    u2 vv[7], zz[7];
    f4 p[3][7];
#pragma unroll
    for (int fb = 0; fb < 7; ++fb) {
      p[0][fb] = *(const f4*)(rd[0] + fb * 1024); p[1][fb] = *(const f4*)(rd[1] + fb * 1024); p[2][fb] = *(const f4*)(rd[2] + fb * 1024);
      zz[fb] = *(const u2*)(sm + KB_Z1 + (w * 7 + fb) * 512 + l2 * 8);
    }
    __builtin_amdgcn_sched_barrier(0);
#pragma unroll
    for (int fb = 0; fb < 7; ++fb) {
      const f4 sum = (h.acc[fb][0] + p[0][fb]) + (p[1][fb] + p[2][fb]);
      const u2 z1 = zz[fb];
      const unsigned s0 = pk_bf(sum[0], sum[1]), s1 = pk_bf(sum[2], sum[3]);
      const unsigned g0 = pk_bf(bflo(s0) + bb, bfhi(s0) + bb), g1 = pk_bf(bflo(s1) + bb, bfhi(s1) + bb);
      vv[fb] = (u2){pk_bf(bflo(g0) * bflo(z1.x), bfhi(g0) * bfhi(z1.x)), pk_bf(bflo(g1) * bflo(z1.y), bfhi(g1) * bfhi(z1.y))};
    }
    const char* vb = vst + w * (NKS3 * 1024);
    if (gs & 1) {
      asm volatile("global_store_dwordx2 %0, %1, %2 offset:8 sc1" ::"v"(vl), "v"(vv[0]), "s"(vb) : "memory");
#pragma unroll
      for (int p = 0; p < 3; ++p) gst16s(vl + 1024 * (p + 1), cat(vv[2 * p + 1], vv[2 * p + 2]), vb);
    } else {
#pragma unroll
      for (int p = 0; p < 3; ++p) gst16s(vl + 1024 * p, cat(vv[2 * p], vv[2 * p + 1]), vb);
      if (cnt == 7) asm volatile("global_store_dwordx2 %0, %1, %2 offset:3072 sc1" ::"v"(vl), "v"(vv[6]), "s"(vb) : "memory");
    }
  }
  ld_wo_rest<WO_LATE>(h, WO);
  waitv<35 - WO_LATE>();
  if (last_of_four(sb + KB_CNT + 8)) {
    if (l2 == 0) __hip_atomic_store((unsigned*)(ws + OFF_F3) + (8 * i + j) * 32, 1u, __ATOMIC_RELAXED, __HIP_MEMORY_SCOPE_AGENT);
  }
  STK(0, 7);
  G3 o;
  const char* vb[4];
#pragma unroll
  for (int m = 0; m < 4; ++m) vb[m] = ws + OFF_V + ((size_t)(4 * i + (m ^ w)) * NKS3 + 7 * w) * 1024;
  unsigned pt[3];
  poll8(pt, (l2 & 7) * 128, ws + OFF_F3 + i * 1024);
  const unsigned vz6 = w == 3 ? h.vz - 1024 : h.vz;
  ld_v<0, V_PRE>(o, h.vz, vz6, vb);
  g3_loop<0>(o, WO, h.slot, h.vz, vz6, vb, st_);
  asm volatile("" : "+v"(pt[0]), "+v"(pt[1]), "+v"(pt[2]));
  asm volatile("s_nop 7" : "+v"(o.acc[0][0]), "+v"(o.acc[1][0]), "+v"(o.acc[2][0]), "+v"(o.acc[3][0]), "+v"(o.acc[4][0]));
  bar();
  const int c3 = l2 & 15, g3 = l2 >> 4;
  const int trow3 = 64 * i + 32 * (w >> 1) + 8 * (c3 >> 2) + 4 * (w & 1) + (c3 & 3);
  const char* hp = sm + (80 * j + 4 * g3 + (c3 >> 2)) * 128 + (((2 * w + ((c3 >> 1) & 1)) ^ (2 * (((c3 >> 3) & 1) | ((g3 >> 1) << 1)))) * 16) + 8 * (c3 & 1);
  u2 y[5];
  {
    f4 p[3][5]; u2 hv[5];
#pragma unroll
    for (int db = 0; db < 5; ++db) {
      p[0][db] = *(const f4*)(rd[0] + db * 1024); p[1][db] = *(const f4*)(rd[1] + db * 1024); p[2][db] = *(const f4*)(rd[2] + db * 1024);
      hv[db] = __builtin_bit_cast(u2, __builtin_amdgcn_ds_read_tr16_b64_v4i16((LDSP s4*)(hp + db * 2048)));
    }
    __builtin_amdgcn_sched_barrier(0);
#pragma unroll
    for (int db = 0; db < 5; ++db) {
      const f4 sum = (o.acc[db][0] + p[0][db]) + (p[1][db] + p[2][db]);
      const unsigned s0 = pk_bf(sum[0], sum[1]), s1 = pk_bf(sum[2], sum[3]);
      y[db] = (u2){pk_bf(bflo(hv[db].x) + bflo(s0), bfhi(hv[db].x) + bfhi(s0)), pk_bf(bflo(hv[db].y) + bflo(s1), bfhi(hv[db].y) + bfhi(s1))};
    }
  }
  const unsigned oo = (unsigned)trow3 * (2 * D) + 160 * j;
#pragma unroll
  for (int p = 0; p < 2; ++p) {
    const auto a = __builtin_amdgcn_permlane16_swap(y[2 * p].x, y[2 * p + 1].x, false, false), b = __builtin_amdgcn_permlane16_swap(y[2 * p].y, y[2 * p + 1].y, false, false);
    asm volatile("global_store_dwordx4 %0, %1, %2 offset:%3" OSF "\n\ts_nop 1" ::"v"(oo + 32 * (g3 & 1) + 16 * (g3 >> 1)), "v"((u4){a[0], b[0], a[1], b[1]}), "s"(out), "i"(64 * p) : "memory");
  }
  asm volatile("global_store_dwordx2 %0, %1, %2 offset:128" OSF ::"v"(oo + 8 * g3), "v"(y[4]), "s"(out) : "memory");
  ST_END(1);
}

extern "C" void kernel_launch(void* const* d_in, const int* in_sizes, int n_in, void* d_out, int out_size, void* d_ws, size_t ws_size,
                              hipStream_t stream) {
  char* ws = (char*)d_ws;
  ka<<<256, 1024, 0, stream>>>((const u16*)d_in[0], (const u16*)d_in[1], (const u16*)d_in[2], (const u16*)d_in[3], (const u16*)d_in[4],
                              (const unsigned*)d_in[5], (const unsigned*)d_in[6], (const u16*)d_in[8], (const u16*)d_in[9], (const u16*)d_in[11], ws,
                              (const u16*)d_in[10], (const u16*)d_out);
  kb<<<256, 256, 0, stream>>>((const u16*)d_in[9], (const u16*)d_in[10], ws, (u16*)d_out);
}
```

```cpp
#include <hip/hip_runtime.h>

typedef unsigned short u16;
typedef unsigned long long u64;
typedef __attribute__((ext_vector_type(8))) __bf16 bf8;
typedef __attribute__((ext_vector_type(2))) __bf16 bf2;
typedef __attribute__((ext_vector_type(8))) _Float16 h8;
typedef __attribute__((ext_vector_type(2))) _Float16 h2;
typedef __attribute__((ext_vector_type(4))) float f4;
typedef __attribute__((ext_vector_type(2))) float f2;
typedef __attribute__((ext_vector_type(4))) short s4;
typedef __attribute__((ext_vector_type(4))) unsigned u4;
typedef __attribute__((ext_vector_type(2))) unsigned u2;
#define LDSP __attribute__((address_space(3)))
#define DEVI __device__ __forceinline__
#define ST_DECL u64* st_ = nullptr;
#define ST(i)
#define ST_END(kid)

constexpr int L = 2048, D = 640, NM = 128, F = 1728, FH = 864;
constexpr int NGB = 54;
constexpr int NKS1 = 20;
constexpr int NKS3 = 27;

constexpr size_t OFF_UT = 0;
constexpr size_t OFF_XT = OFF_UT + (size_t)D * L * 2;
constexpr size_t OFF_KR = OFF_XT + (size_t)D * L * 2;
constexpr size_t OFF_HT = OFF_KR + (size_t)D * L * 4;
constexpr size_t OFF_WF = OFF_HT + (size_t)D * L * 2;
constexpr size_t OFF_WO = OFF_WF + (size_t)2 * NGB * NKS1 * 1024;
constexpr size_t OFF_Z1 = OFF_WO + (size_t)40 * NKS3 * 1024;
constexpr size_t OFF_Z2 = OFF_Z1 + (size_t)128 * NGB * 512;
constexpr size_t OFF_V = OFF_Z2 + (size_t)64 * NGB * 1024;
constexpr size_t OFF_ST = OFF_V + (size_t)128 * NKS3 * 1024;
constexpr size_t OFF_CSP = OFF_ST + (size_t)32 * 8 * 64 * 8;
constexpr size_t OFF_CS = OFF_CSP + (size_t)5 * F * 4;
constexpr size_t OFF_KSC = OFF_CS + (size_t)F * 4;
constexpr size_t OFF_G1 = 32u << 20;
constexpr size_t OFF_F1 = OFF_G1 + 32768;
constexpr size_t OFF_EP = OFF_G1 + 65536;
constexpr size_t OFF_F2 = OFF_G1 + 131072;
constexpr size_t OFF_F3 = OFF_F2 + 32768;
constexpr size_t OFF_ZE = OFF_F3 + 32768;

DEVI float bflo(unsigned v) { return __uint_as_float(v << 16); }
DEVI float bfhi(unsigned v) { return __uint_as_float(v & 0xffff0000u); }
DEVI unsigned pk_bf(float a, float b) { f2 p = {a, b}; return __builtin_bit_cast(unsigned, __builtin_convertvector(p, bf2)); }
DEVI unsigned pk_h(float a, float b) { f2 p = {a, b}; return __builtin_bit_cast(unsigned, __builtin_convertvector(p, h2)); }
DEVI float rbf(float a) { return bflo(pk_bf(a, 0.f)); }
DEVI float hlo(unsigned v) { return (float)__builtin_bit_cast(h2, v).x; }
DEVI float hhi(unsigned v) { return (float)__builtin_bit_cast(h2, v).y; }
DEVI f4 mfma_bf(u4 a, u4 b, f4 c) { return __builtin_amdgcn_mfma_f32_16x16x32_bf16(__builtin_bit_cast(bf8, a), __builtin_bit_cast(bf8, b), c, 0, 0, 0); }
DEVI f4 mfma_h(u4 a, u4 b, f4 c) { return __builtin_amdgcn_mfma_f32_16x16x32_f16(__builtin_bit_cast(h8, a), __builtin_bit_cast(h8, b), c, 0, 0, 0); }
template <int CTRL> DEVI float dpp(float v) { return __int_as_float(__builtin_amdgcn_update_dpp(0, __float_as_int(v), CTRL, 0xf, 0xf, true)); }
constexpr int DPP_ROR1 = 0x121, DPP_ROR2 = 0x122, DPP_ROR4 = 0x124, DPP_ROR8 = 0x128, DPP_MIRROR = 0x140, DPP_HMIRROR = 0x141, DPP_Q3210 = 0x1B, DPP_Q1032 = 0xB1;

DEVI f2 ln_stats(float s, float q, float inv_n) {
  float m = s * inv_n;
  float v = fmaxf(q * inv_n - m * m, 0.f);
  float sd = rbf(__builtin_amdgcn_sqrtf(rbf(rbf(v) + 1e-5f)));
  f2 r = {rbf(m), __builtin_amdgcn_rcpf(sd)};
  return r;
}
DEVI float gelu_t(float x) {
  float t = __builtin_fmaf(x * x, -0.1029432397f, -2.3022081985f);
  float e = __builtin_amdgcn_exp2f(x * t);
  return x * __builtin_amdgcn_rcpf(1.0f + e);
}

template <int IMM> DEVI void gld16(u4& d, unsigned voff, const void* sb) { asm volatile("global_load_dwordx4 %0, %1, %2 offset:%3" : "=v"(d) : "v"(voff), "s"(sb), "i"(IMM)); }
template <int IMM> DEVI void gld8(u2& d, unsigned voff, const void* sb) { asm volatile("global_load_dwordx2 %0, %1, %2 offset:%3" : "=v"(d) : "v"(voff), "s"(sb), "i"(IMM)); }
template <int IMM> DEVI void gld4(unsigned& d, unsigned voff, const void* sb) { asm volatile("global_load_dword %0, %1, %2 offset:%3" : "=v"(d) : "v"(voff), "s"(sb), "i"(IMM)); }
DEVI void gld2(unsigned& d, unsigned voff, const void* sb) { asm volatile("global_load_ushort %0, %1, %2" : "=v"(d) : "v"(voff), "s"(sb)); }
DEVI void dma16(unsigned ldsdst, unsigned voff, const void* sb) {
  asm volatile("s_mov_b32 m0, %0\n\ts_nop 0\n\tglobal_load_lds_dwordx4 %1, %2" ::"s"(ldsdst), "v"(voff), "s"(sb) : "memory");
}
template <int N> DEVI void waitv() { asm volatile("s_waitcnt vmcnt(%0)" ::"i"(N) : "memory"); }
template <int N> DEVI void waitv(u4& a) { asm volatile("s_waitcnt vmcnt(%1)" : "+v"(a) : "i"(N) : "memory"); }
DEVI void pin(u4& a) { asm volatile("" : "+v"(a)); }
DEVI void pin(u2& a) { asm volatile("" : "+v"(a)); }
DEVI void pin(unsigned& a) { asm volatile("" : "+v"(a)); }
DEVI void bar() { asm volatile("s_waitcnt lgkmcnt(0)\n\ts_barrier" ::: "memory"); }
DEVI unsigned lds_addr(const void* p) { return (unsigned)(size_t)(LDSP const char*)p; }

DEVI void tr8x8(const u4 (&r)[8], u4 (&o)[8]) {
#pragma unroll
  for (int c2 = 0; c2 < 4; ++c2)
#pragma unroll
    for (int m = 0; m < 4; ++m) {
      o[2 * c2][m] = __builtin_amdgcn_perm(r[2 * m + 1][c2], r[2 * m][c2], 0x05040100u);
      o[2 * c2 + 1][m] = __builtin_amdgcn_perm(r[2 * m + 1][c2], r[2 * m][c2], 0x07060302u);
    }
}

constexpr int KG_PITCH = 528;
constexpr int KC_PITCH = 4160;
constexpr int RC_SZ = 30208;
constexpr int KA_K = 3 * RC_SZ, KA_U = KA_K + 12 * KC_PITCH, U_PITCH = 4608, KA_T = KA_U + 3 * U_PITCH, T_PITCH = 1536;
constexpr int KA_S = KA_T + 3 * T_PITCH, KA_LDS = KA_S + 96;

struct c2 { f2 x, y; };
DEVI c2 cmul(c2 a, c2 b) { c2 r; r.x = a.x * b.x - a.y * b.y; r.y = a.x * b.y + a.y * b.x; return r; }
DEVI c2 cexpp(f2 a2, f2 br, float p) {
  const f2 ea = a2 * p, ph = br * p;
  const f2 e = {__builtin_amdgcn_exp2f(ea.x), __builtin_amdgcn_exp2f(ea.y)};
  const float p0 = __builtin_amdgcn_fractf(ph.x), p1 = __builtin_amdgcn_fractf(ph.y);
  c2 r;
  r.x = e * (f2){__builtin_amdgcn_cosf(p0), __builtin_amdgcn_cosf(p1)};
  r.y = e * (f2){__builtin_amdgcn_sinf(p0), __builtin_amdgcn_sinf(p1)};
  return r;
}
DEVI unsigned pk_h2(f2 p) { return __builtin_bit_cast(unsigned, __builtin_convertvector(p, h2)); }
template <int CTRL, int RM> DEVI float dppm(float v) { return __int_as_float(__builtin_amdgcn_update_dpp(0, __float_as_int(v), CTRL, RM, 0xf, false)); }
DEVI float wave_sum63(float v) {
  v += dpp<DPP_ROR8>(v); v += dpp<DPP_ROR4>(v); v += dpp<DPP_ROR2>(v); v += dpp<DPP_ROR1>(v);
  v += dppm<0x142, 0xa>(v); v += dppm<0x143, 0xc>(v);
  return v;
}
template <int IMM> DEVI void lds8(u2& d, unsigned a) { asm volatile("ds_read_b64 %0, %1 offset:%2" : "=v"(d) : "v"(a), "i"(IMM)); }
template <int IMM> DEVI void lds16(u4& d, unsigned a) { asm volatile("ds_read_b128 %0, %1 offset:%2" : "=v"(d) : "v"(a), "i"(IMM)); }
template <int N> DEVI void waitl(u2& a, u2& b, u4& c) { asm volatile("s_waitcnt lgkmcnt(%3)" : "+v"(a), "+v"(b), "+v"(c) : "i"(N) : "memory"); }
template <int IMM> DEVI void gld16s(u4& d, unsigned voff, const void* sb) { asm volatile("global_load_dwordx4 %0, %1, %2 offset:%3 sc1" : "=v"(d) : "v"(voff), "s"(sb), "i"(IMM)); }
DEVI void gld4s(unsigned& d, unsigned voff, const void* sb) { asm volatile("global_load_dword %0, %1, %2 sc1" : "=v"(d) : "v"(voff), "s"(sb)); }
DEVI unsigned perm(unsigned hi, unsigned lo, unsigned sel) { return __builtin_amdgcn_perm(hi, lo, sel); }

template <int RR, int P> DEVI void conv_ld(u2 (&al)[8], u2 (&ah)[8], u4 (&bb)[8], unsigned pa, unsigned pb) {
  if constexpr (P < 8) {
    lds8<64 * (1 - RR) + 512 * (7 - P)>(al[P], pa);
    lds8<64 * (1 - RR) + 512 * (7 - P) + 8>(ah[P], pa);
    lds16<64 * (1 - RR) + 512 * P>(bb[P], pb);
    conv_ld<RR, P + 1>(al, ah, bb, pa, pb);
  }
}
template <int S> DEVI void conv_mm(u2 (&al)[8], u2 (&ah)[8], u4 (&bb)[8], f4 (&acc)[8]) {
  if constexpr (S < 8) {
    waitl<(3 * (7 - S) > 15 ? 15 : 3 * (7 - S))>(al[S], ah[S], bb[S]);
#pragma unroll
    for (int p = 0; p <= S; ++p) acc[S] = mfma_h((u4){al[p].x, al[p].y, ah[p].x, ah[p].y}, bb[S - p], acc[S]);
    __builtin_amdgcn_sched_barrier(0);
    conv_mm<S + 1>(al, ah, bb, acc);
  }
}
template <int IMM> DEVI void gld8s(u2& d, unsigned voff, const void* sb) { asm volatile("global_load_dwordx2 %0, %1, %2 offset:%3 sc1" : "=v"(d) : "v"(voff), "s"(sb), "i"(IMM)); }
DEVI void gst16s(unsigned voff, u4 v, const void* sb) { asm volatile("global_store_dwordx4 %0, %1, %2 sc1\n\ts_nop 1" ::"v"(voff), "v"(v), "s"(sb) : "memory"); }
DEVI u4 tr2(const char* p, int step) {
  const u2 a = __builtin_bit_cast(u2, __builtin_amdgcn_ds_read_tr16_b64_v4i16((LDSP s4*)p));
  const u2 b = __builtin_bit_cast(u2, __builtin_amdgcn_ds_read_tr16_b64_v4i16((LDSP s4*)(p + step)));
  return (u4){a.x, a.y, b.x, b.y};
}
template <bool TWO, int K> DEVI void tbl_ld(u4 (&a)[8], u4 (&c)[8], u4 (&e)[8], unsigned pa, unsigned pb) {
  if constexpr (K < 8) {
    lds16<64 * K>(a[K], pa); lds16<64 * K>(c[K], pb);
    if constexpr (TWO) lds16<64 * K + 16 * KG_PITCH>(e[K], pb);
    tbl_ld<TWO, K + 1>(a, c, e, pa, pb);
  }
}
template <bool TWO, int K> DEVI void tbl_mm(u4 (&a)[8], u4 (&c)[8], u4 (&e)[8], f4& acc, f4& ac0) {
  if constexpr (K < 8) {
    constexpr int N = (TWO ? 3 : 2) * (7 - K);
    if constexpr (TWO) asm volatile("s_waitcnt lgkmcnt(%3)" : "+v"(a[K]), "+v"(c[K]), "+v"(e[K]) : "i"(N > 15 ? 15 : N) : "memory");
    else asm volatile("s_waitcnt lgkmcnt(%2)" : "+v"(a[K]), "+v"(c[K]) : "i"(N) : "memory");
    acc = mfma_h(a[K], c[K], acc);
    if constexpr (TWO) ac0 = mfma_h(e[K], c[K], ac0);
    __builtin_amdgcn_sched_barrier(0);
    tbl_mm<TWO, K + 1>(a, c, e, acc, ac0);
  }
}
template <bool TWO> DEVI void tbl_tiles(unsigned pa, unsigned pb, f4& acc, f4& ac0) {
  u4 a[8], c[8], e[8];
  tbl_ld<TWO, 0>(a, c, e, pa, pb);
  tbl_mm<TWO, 0>(a, c, e, acc, ac0);
}
DEVI void chsync(unsigned fl, int wq, unsigned phase) {
  asm volatile("s_waitcnt lgkmcnt(0)\n\tds_write_b32 %0, %1" ::"v"(fl + 4 * wq), "v"(phase) : "memory");
  for (;;) {
    u4 v;
    asm volatile("ds_read_b128 %0, %1\n\ts_waitcnt lgkmcnt(0)" : "=v"(v) : "v"(fl) : "memory");
    if (__builtin_amdgcn_readfirstlane(min(min(v.x, v.y), min(v.z, v.w))) >= phase) break;
    __builtin_amdgcn_s_sleep(1);
  }
}
DEVI void rawbar() { asm volatile("s_barrier" ::: "memory"); }
template <int I> DEVI void poll_ld(u4 (&g)[4], unsigned go, const char* base) {
  if constexpr (I < 4) { gld16s<16 * I>(g[I], go, base); poll_ld<I + 1>(g, go, base); }
}
DEVI void wait4(u4 (&g)[4]) { asm volatile("s_waitcnt vmcnt(0)" : "+v"(g[0]), "+v"(g[1]), "+v"(g[2]), "+v"(g[3])::"memory"); }

constexpr int KA_CODE_LINES = 118, KB_CODE_LINES = 290;
__global__ __launch_bounds__(1024, 4) void ka(const u16* __restrict__ x, const u16* __restrict__ logA, const u16* __restrict__ Aim,
                                          const u16* __restrict__ Cre, const u16* __restrict__ Cim, const unsigned* __restrict__ logdt,
                                          const unsigned* __restrict__ dskip, const u16* __restrict__ w_in, const u16* __restrict__ wsp,
                                          const u16* __restrict__ w_out, char* __restrict__ ws) {
  __shared__ __attribute__((aligned(16))) char sm[KA_LDS];
  const int tid = threadIdx.x, lane = tid & 63;
  const int w = __builtin_amdgcn_readfirstlane(tid >> 6);
  const int b = blockIdx.x;
  ST_DECL
  const int g = b & 7, m = b >> 3;
  const bool isconv = m < 27;
  const int d0 = 80 * g + (3 * m < 77 ? 3 * m : 77);
  if (tid < 16) ((int*)(sm + KA_S + 32))[tid] = 0;
  unsigned tag = 0;
  if (w < 4) {
    u4 r0, r1; u2 r2;
    unsigned ep;
    const int row = 256 * g + 8 * m + 2 * w + (lane >> 5), tm = 4 * g + (m >> 3), tg = m & 7;
    const unsigned ro = (unsigned)row * 1280 + (lane & 31) * 16;
    gld4s(ep, 0, ws + OFF_EP);
    gld16<0>(r0, ro, x); gld16<512>(r1, ro, x); gld8<1024>(r2, ro - (lane & 31) * 8, x);
    bar();
    {
      const char* tsrc = (const char*)x + (size_t)tm * 64 * 1280 + tg * 160;
      const unsigned dst = lds_addr(sm) + KA_U + w * 1024;
#pragma unroll
      for (int i = 0; i < 3; ++i) { const unsigned pc = tid + 256 * i < 640 ? tid + 256 * i : 639; dma16(dst + i * 4096, (pc / 10) * 1280 + (pc % 10) * 16, tsrc); }
    }
    __builtin_amdgcn_s_setprio(2);
    asm volatile("s_waitcnt vmcnt(3)" : "+v"(ep), "+v"(r0), "+v"(r1), "+v"(r2)::"memory");
    ST(1);
    tag = ep + 0x9E3779B9u;
    f2 s0 = {bflo(r2.x), bfhi(r2.x)}, s1 = {bflo(r2.y), bfhi(r2.y)}, q0 = s0 * s0, q1 = s1 * s1;
#pragma unroll
    for (int c = 0; c < 4; ++c) {
      const unsigned v = r0[c], v2 = r1[c];
      const f2 a = {bflo(v), bfhi(v)}, a2 = {bflo(v2), bfhi(v2)};
      s0 += a; q0 += a * a; s1 += a2; q1 += a2 * a2;
    }
    s0 += s1; q0 += q1;
    float ss = s0.x + s0.y, qq = q0.x + q0.y;
    ss += dpp<DPP_ROR8>(ss); qq += dpp<DPP_ROR8>(qq); ss += dpp<DPP_ROR4>(ss); qq += dpp<DPP_ROR4>(qq);
    ss += dpp<DPP_ROR2>(ss); qq += dpp<DPP_ROR2>(qq); ss += dpp<DPP_ROR1>(ss); qq += dpp<DPP_ROR1>(qq);
    ss += dppm<0x142, 0xa>(ss); qq += dppm<0x142, 0xa>(qq);
    const float mean = ss * (1.0f / D);
    const float var = fmaxf(qq * (1.0f / D) - mean * mean, 0.f);
    unsigned pay = pk_bf(mean, __builtin_amdgcn_sqrtf(rbf(rbf(var) + 1e-5f)));
    pin(pay);
    u4 gv = {(unsigned)__builtin_amdgcn_readlane(pay, 31), tag, pay, tag};
    pin(gv);
    waitv<0>();
    if (lane == 63) gst16s((row - 1) * 8, gv, ws + OFF_G1);
    chsync(lds_addr(sm) + KA_S + 80, w, 1);
    ST(2);
#pragma unroll 1
    for (int bl = w; bl < 5; bl += 4) {
      const int l = lane & 15, gm = lane >> 4;
      const char* tp = sm + KA_U + (8 * gm + (l >> 2)) * 160 + bl * 32 + (l & 3) * 8;
      const u4 A = tr2(tp, 640), B = tr2(tp + 32 * 160, 640);
      u4 s1v, s2v;
#pragma unroll
      for (int e = 0; e < 4; ++e) {
        const unsigned ae = A[e], be = B[e];
        s1v[e] = __builtin_amdgcn_update_dpp(ae, be, DPP_ROR8, 0xf, 0xc, false);
        s2v[e] = __builtin_amdgcn_update_dpp(be, ae, DPP_ROR8, 0xf, 0x3, false);
      }
      const unsigned vo = (unsigned)(80 * tg + 16 * bl + (l & 7)) * 4096 + tm * 128 + (gm + 4 * (l >> 3)) * 16;
      gst16s(vo, s1v, ws + OFF_XT);
      gst16s(vo + 8 * 4096, s2v, ws + OFF_XT);
    }
    waitv<0>();
    chsync(lds_addr(sm) + KA_S + 80, w, 2);
    if (tid == 0) __hip_atomic_store((unsigned*)(ws + OFF_F1) + (32 * tg + tm) * 32, tag, __ATOMIC_RELAXED, __HIP_MEMORY_SCOPE_AGENT);
    ST(3);
    if (isconv) {
      u4 gr[4], xr[3];
      poll_ld<0>(gr, tid * 64, ws + OFF_G1);
      if (w == 0) {
#pragma unroll 1
        for (int spin = 0; spin < (1 << 16); ++spin) {
          unsigned v;
          asm volatile("global_load_dword %0, %1, %2 sc1\n\ts_waitcnt vmcnt(0)" : "=v"(v) : "v"((lane & 31) * 128), "s"(ws + OFF_F1 + g * 4096) : "memory");
          if (__all(v == tag)) break;
          __builtin_amdgcn_s_sleep(2);
        }
      }
      wait4(gr);
      chsync(lds_addr(sm) + KA_S + 80, w, 3);
      ST(4);
      const char* xsrc = ws + OFF_XT + (size_t)d0 * 4096;
      gld16s<0>(xr[0], tid * 16, xsrc); gld16s<0>(xr[1], tid * 16, xsrc + 4096); gld16s<0>(xr[2], tid * 16, xsrc + 8192);
#pragma unroll 1
      for (int spin = 0; spin < (1 << 16); ++spin) {
        bool ok = true;
#pragma unroll
        for (int i = 0; i < 4; ++i) ok = ok && gr[i].y == tag && gr[i].w == tag;
        if (__all(ok)) break;
        poll_ld<0>(gr, tid * 64, ws + OFF_G1);
        wait4(gr);
      }
      f2 mm[4], rs[4];
#pragma unroll
      for (int i = 0; i < 4; ++i) { mm[i] = (f2){bflo(gr[i].x), bflo(gr[i].z)}; rs[i] = (f2){__builtin_amdgcn_rcpf(bfhi(gr[i].x)), __builtin_amdgcn_rcpf(bfhi(gr[i].z))}; }
      if (tid < 90) *(u4*)(sm + KA_U + (tid / 30) * U_PITCH + (tid % 30) * 16) = (u4){0, 0, 0, 0};
      ST(7);
      asm volatile("s_waitcnt vmcnt(0)" : "+v"(xr[0]), "+v"(xr[1]), "+v"(xr[2])::"memory");
      ST(8);
#pragma unroll
      for (int c = 0; c < 3; ++c) {
        u4 o;
#pragma unroll
        for (int i = 0; i < 4; ++i) { const unsigned v = xr[c][i]; o[i] = pk_h2(((f2){bflo(v), bfhi(v)} - mm[i]) * rs[i]); }
        *(u4*)(sm + KA_U + c * U_PITCH + 480 + tid * 16) = o;
      }
      __builtin_amdgcn_s_setprio(0);
      bar();
      ST(6);
      if (b == 0 && tid == 0) __hip_atomic_store((unsigned*)(ws + OFF_EP), tag, __ATOMIC_RELAXED, __HIP_MEMORY_SCOPE_AGENT);
      __builtin_amdgcn_s_sleep(4);
      if (w != 0) {
        unsigned pw[4];
#pragma unroll
        for (int i = 0; i < 4; ++i) {
          const unsigned ln = (i * 81 + m * 3 + w - 1) * 64 + lane;
          gld4<0>(pw[i], (ln < 16384 ? ln : 16383) * 128, (const char*)wsp + (size_t)(g >> 1) * (16384 * 128));
        }
        if (m < 2) {
          const unsigned ln = (m * 3 + w - 1) * 64 + lane;
          const char* code;
          unsigned pc;
          asm volatile("s_getpc_b64 s[44:45]\n\ts_add_u32 s44, s44, _Z2kbPKtS0_PcPt@rel32@lo+4\n\ts_addc_u32 s45, s45, _Z2kbPKtS0_PcPt@rel32@hi+12\n\ts_mov_b64 %0, s[44:45]"
                       : "=s"(code)::"s44", "s45", "scc");
          gld4<0>(pc, (ln < KB_CODE_LINES ? ln : KB_CODE_LINES - 1) * 128, code);
          asm volatile("" : "+v"(pc), "+v"(pw[0]), "+v"(pw[1]), "+v"(pw[2]), "+v"(pw[3]));
        }
        asm volatile("" : "+v"(pw[0]), "+v"(pw[1]), "+v"(pw[2]), "+v"(pw[3]));
      } else {
      const int item = b * 40 + lane;
      if (lane < 40) {
        const int oc = item % 80, kq = item / 80, kap = kq >> 2, qq = kq & 3;
        u4 r[8], o[8];
#pragma unroll
        for (int e = 0; e < 8; ++e) r[e] = *(const u4*)(w_out + (size_t)(32 * kap + 16 * (e >> 2) + 4 * qq + (e & 3)) * D + oc * 8);
        tr8x8(r, o);
        u4* dst = (u4*)(ws + OFF_WO + ((size_t)(oc >> 1) * NKS3 + kap) * 1024) + qq * 16 + (oc & 1) * 8;
#pragma unroll
        for (int c = 0; c < 8; ++c) dst[c] = o[c];
      }
      }
      ST_END(0);
      return;
    }
    __builtin_amdgcn_s_setprio(0);
  }
  if (!isconv) {
    unsigned pc = 0;
    if (w == 4) {
      const unsigned ln = (m - 27) * 64 + lane;
      const char* code;
      asm volatile("s_getpc_b64 s[44:45]\n\ts_add_u32 s44, s44, _Z2kaPKtS0_S0_S0_S0_PKjS2_S0_S0_S0_Pc@rel32@lo+4\n\t"
                   "s_addc_u32 s45, s45, _Z2kaPKtS0_S0_S0_S0_PKjS2_S0_S0_S0_Pc@rel32@hi+12\n\ts_mov_b64 %0, s[44:45]" : "=s"(code)::"s44", "s45", "scc");
      gld4<0>(pc, (ln < KA_CODE_LINES ? ln : KA_CODE_LINES - 1) * 128, code);
    }
    if (w >= 4) rawbar();
    rawbar();
    if (w == 4) asm volatile("s_waitcnt vmcnt(0)" : "+v"(pc)::"memory");
    const int si = b - 216;
    if (si < 27) {
      const int tt = tid - 384, co = tt & 7, ko = tt >> 3, c0 = 64 * si + 8 * co, k0 = 8 * ko;
      float* part = (float*)(sm + 98304);
      if (w >= 6) {
        u4 r[8], o[8];
#pragma unroll
        for (int e = 0; e < 8; ++e) r[e] = *(const u4*)(w_in + (size_t)(k0 + e) * F + c0);
        float cs[8];
#pragma unroll
        for (int c = 0; c < 4; ++c) {
          f2 s = {0.f, 0.f};
#pragma unroll
          for (int e = 0; e < 8; ++e) { const unsigned v = r[e][c]; s += (f2){bflo(v), bfhi(v)}; }
          cs[2 * c] = s.x + dpp<DPP_ROR8>(s.x); cs[2 * c + 1] = s.y + dpp<DPP_ROR8>(s.y);
        }
        if (!(lane & 8)) {
          f4* p = (f4*)(part + ((w - 6) * 4 + (lane >> 4)) * 64 + co * 8);
          p[0] = (f4){cs[0], cs[1], cs[2], cs[3]}; p[1] = (f4){cs[4], cs[5], cs[6], cs[7]};
        }
        tr8x8(r, o);
#pragma unroll
        for (int c = 0; c < 8; ++c) *(u4*)(sm + tt * 144 + c * 16) = o[c];
      }
      __syncthreads();
      if (w < 8) {
        float s = 0.f;
#pragma unroll
        for (int e = 0; e < 5; ++e) s += part[(w * 5 + e) * 64 + lane];
        part[2560 + w * 64 + lane] = s;
      }
#pragma unroll
      for (int i = 0; i < 5; ++i) {
        const int f = w * 5 + i, blk = f / 20, ks = f % 20, gc = 4 * si + blk;
        const u4 v = *(const u4*)(sm + ((4 * ks + (lane >> 4)) * 8 + 2 * blk + ((lane >> 3) & 1)) * 144 + (lane & 7) * 16);
        *(u4*)(ws + OFF_WF + ((size_t)(gc >= NGB ? gc - NGB : gc + NGB) * NKS1 + ks) * 1024 + lane * 16) = v;
      }
      __syncthreads();
      if (w == 0) {
        float s = 0.f;
#pragma unroll
        for (int e = 0; e < 8; ++e) s += part[2560 + e * 64 + lane];
        ((float*)(ws + OFF_CS))[64 * si + lane] = s;
      }
    } else if (si < 35) {
      ((u4*)(ws + OFF_ST))[(si - 27) * 1024 + tid] = (u4){0, 0, 0, 0};
    } else if (si == 35) {
      if (tid < 576) *(u4*)(ws + OFF_F2 + (tid < 512 ? tid * 128 : 65536 + (tid - 512) * 16)) = (u4){0, 0, 0, 0};
    }
    ST_END(0);
    return;
  }
  const int ch = w < 4 ? 0 : (w >> 2) - 1, role = (w + ch) & 3;
  const int d = d0 + ch;
  const int col = lane & 15, q = lane >> 4;
  char* rc = sm + ch * RC_SZ;
  float* slot = (float*)(sm + KA_S) + 2 * ch;
  const unsigned fl = lds_addr(sm) + KA_S + 32 + 16 * ch;
  if (w >= 4) {
    unsigned la, ai, cr, ci, ldw, dw;
    gld4<0>(ldw, 0, logdt + (d >> 1));
    gld4<0>(dw, 0, dskip + (d >> 1));
    gld4<0>(la, lane * 4, logA + d * NM);
    gld4<0>(ai, lane * 4, Aim + d * NM);
    gld4<0>(cr, lane * 4, Cre + d * NM);
    gld4<0>(ci, lane * 4, Cim + d * NM);
    rawbar();
    asm volatile("s_waitcnt vmcnt(0)" : "+v"(la), "+v"(ai), "+v"(cr), "+v"(ci), "+v"(ldw), "+v"(dw)::"memory");
    ST(1);
    const float dt = rbf(__expf((d & 1) ? bfhi(ldw) : bflo(ldw)));
    const unsigned ea = pk_bf(__expf(bflo(la)), __expf(bfhi(la)));
    const f2 a2 = (f2){bflo(ea), bfhi(ea)} * (-1.4426950408889634f * dt);
    f2 br = (f2){bflo(ai), bfhi(ai)} * (dt * 0.15915494309189535f);
    char* base = rc + lane * 8;
    auto put = [&](int row, c2 v) { *(u2*)(base + row * KG_PITCH) = (u2){pk_h2(v.x), pk_h2(v.y)}; };
    if (role == 0) {
      c2 v = {{bflo(cr), bfhi(cr)}, {bflo(ci), bfhi(ci)}};
      float mx = fmaxf(fmaxf(fabsf(v.x.x), fabsf(v.x.y)), fmaxf(fabsf(v.y.x), fabsf(v.y.y)));
      mx = fmaxf(mx, dpp<DPP_ROR8>(mx)); mx = fmaxf(mx, dpp<DPP_ROR4>(mx)); mx = fmaxf(mx, dpp<DPP_ROR2>(mx)); mx = fmaxf(mx, dpp<DPP_ROR1>(mx));
      mx = fmaxf(mx, dppm<0x142, 0xa>(mx)); mx = fmaxf(mx, dppm<0x143, 0xc>(mx));
      mx = __int_as_float(__builtin_amdgcn_readlane(__float_as_int(mx), 63));
      const float dsk = (d & 1) ? bfhi(dw) : bflo(dw);
      const float sc = fmaxf(fmaxf(2.0f * dt * mx, fabsf(dsk) * (1.0f / 16384.0f)), 1e-30f);
      const float isc = 1.0f / sc;
      if (lane == 0) { slot[0] = sc; slot[1] = dsk * isc; }
      v.x *= 2.0f * dt * isc; v.y *= 2.0f * dt * isc;
      const c2 s = cexpp(a2, br, 8.f);
#pragma unroll
      for (int j = 0; j < 16; ++j) { put(j, v); if (j < 15) v = cmul(v, s); }
    } else {
      br = -br;
      const c2 h = cexpp(a2, br, 128.f);
      if (role == 1) {
        const c2 s = cexpp(a2, br, 1.f);
        put(16, (c2){{1.f, 1.f}, {0.f, 0.f}}); put(24, h);
        c2 v = s;
#pragma unroll
        for (int c = 1; c < 8; ++c) { put(16 + c, v); put(24 + c, cmul(v, h)); if (c < 7) v = cmul(v, s); }
      } else if (role == 2) {
        const c2 s = cexpp(a2, br, 2.f);
        c2 v = cmul(h, h);
        const c2 q = cmul(s, s), y = cmul(v, v), z = cmul(y, h);
        put(40, y); put(41, cmul(y, q)); put(42, z); put(43, cmul(z, q));
#pragma unroll
        for (int c = 0; c < 4; ++c) { put(32 + c, v); put(36 + c, cmul(v, h)); if (c < 3) v = cmul(v, s); }
      } else {
        const c2 q = cexpp(a2, br, 4.f);
        c2 v = cexpp(a2, br, 768.f);
        put(44, v); put(45, cmul(v, q)); v = cmul(v, h);
        put(46, v); put(47, cmul(v, q));
#pragma unroll
        for (int i = 0; i < 9; ++i) { v = cmul(v, h); put(48 + i, v); }
      }
    }
  ST(2);
  chsync(fl, w & 3, 1);
  char* tb = sm + KA_T + ch * T_PITCH;
  {
    const int wq = w & 3;
    const unsigned pb = lds_addr(rc) + col * KG_PITCH + q * 16;
    f4 acc = {0.f, 0.f, 0.f, 0.f}, ac0 = {0.f, 0.f, 0.f, 0.f};
    const float dadd = slot[1];
    if (wq == 3) tbl_tiles<true>(pb + 32 * KG_PITCH, pb, acc, ac0); else tbl_tiles<false>(pb + (wq < 2 ? 48 : 40) * KG_PITCH, pb, acc, ac0);
    const unsigned p0 = pk_h(acc[0], acc[1]), p1 = pk_h(acc[2], acc[3]);
    if (wq < 2) {
      u16* t = (u16*)tb + 512 + 64 * q + col;
      t[0] = (u16)p0; t[16] = (u16)(p0 >> 16); t[32] = (u16)p1; t[48] = (u16)(p1 >> 16);
    } else if (wq == 2) {
      if (q < 2) { *(unsigned*)(tb + 2 * (384 + 64 * q + 2 * col)) = p0; *(unsigned*)(tb + 2 * (416 + 64 * q + 2 * col)) = p1; }
      else if (lane == 32) ((u16*)tb)[512] = (u16)p0;
    } else {
      if (q < 2) *(u2*)(tb + 2 * (256 + 64 * q + 4 * col)) = (u2){p0, p1};
      else if (lane == 32) ((u16*)tb)[384] = (u16)p0;
      if (lane == 0) ac0[0] += dadd;
      *(u2*)(tb + 2 * (128 * (q >> 1) + 4 * (q & 1) + 8 * col)) = (u2){pk_h(ac0[0], ac0[1]), pk_h(ac0[2], ac0[3])};
    }
  }
  asm volatile("s_waitcnt lgkmcnt(0)" ::: "memory");
  ST(3);
  {
    const int t8 = tid & 255, a = 255 - t8, wq = w & 3;
    u4 o0, o1;
    if (wq < 2) {
      const u16* t = (const u16*)tb + 384 + a;
      const unsigned lo = t[0], hi = t[1];
      const unsigned hh = perm(hi, hi, 0x01000100u), ll = perm(lo, lo, 0x01000100u), tie = (a & 1) ? hi : lo;
      o0 = (u4){hh, perm(tie, hi, 0x05040100u), ll, ll}; o1 = (u4){hh, perm(lo, tie, 0x05040100u), ll, ll};
    } else if (wq == 2) {
      const unsigned* t = (const unsigned*)(tb + 2 * (256 + 2 * a));
      const unsigned v01 = t[0], v2 = t[1];
      const unsigned t11 = perm(v01, v01, 0x03020302u), t00 = perm(v01, v01, 0x01000100u);
      o0 = (u4){perm(v2, v2, 0x01000100u), t11, perm(v01, v01, 0x01000302u), t00}; o1 = (u4){perm(v01, v2, 0x07060100u), t11, t00, t00};
    } else if (a >= 32) {
      const unsigned* t = (const unsigned*)(tb + 2 * (128 + 4 * a));
      const unsigned vm = t[-1], v01 = t[0], v23 = t[1], v4 = t[2];
      const unsigned t22 = perm(v23, v23, 0x01000100u);
      o0 = (u4){perm(v23, v4, 0x07060100u), t22, perm(v01, v23, 0x07060100u), perm(v01, v01, 0x01000100u)};
      o1 = (u4){perm(v23, v23, 0x01000302u), t22, perm(v01, v01, 0x01000302u), perm(vm, v01, a == 32 ? 0x07060100u : 0x01000100u)};
    } else {
      const u4 v = *(const u4*)(tb + 16 * a);
      const unsigned vm = a ? *(const unsigned*)(tb + 16 * a - 4) : 0u;
      o0 = (u4){perm(v.w, v.w, 0x01000302u), perm(v.z, v.z, 0x01000302u), perm(v.y, v.y, 0x01000302u), perm(v.x, v.x, 0x01000302u)};
      o1 = (u4){perm(v.z, v.w, 0x07060100u), perm(v.y, v.z, 0x07060100u), perm(v.x, v.y, 0x07060100u), perm(vm, v.x, 0x07060100u)};
    }
    char* kcb = sm + KA_K + ch * 4 * KC_PITCH;
    char* kc = kcb + 16 * t8;
    *(u4*)kc = o0; *(u4*)(kc + KC_PITCH) = o1;
    unsigned* k2 = (unsigned*)(kc + 2 * KC_PITCH - 4);
    unsigned* k3 = (unsigned*)(kc + 3 * KC_PITCH - 4);
    k2[0] = o0.x; k2[1] = o0.y; k2[2] = o0.z; k2[3] = o0.w;
    k3[0] = o1.x; k3[1] = o1.y; k3[2] = o1.z; k3[3] = o1.w;
    if (wq == 0 && lane < 36) { const int s = lane / 9; *(unsigned*)(kcb + s * KC_PITCH + 4096 - (s >> 1) * 4 + (lane - 9 * s) * 4) = 0; }
  }
  ST(4);
  }
  ST(5);
  bar();
  ST(6);
  const int wq = w & 3;
  u2 xv[2];
  gld8s<0>(xv[0], (512 * wq + 16 * col + 4 * q) * 2, ws + OFF_XT + (size_t)d * 4096);
  gld8s<512>(xv[1], (512 * wq + 16 * col + 4 * q) * 2, ws + OFF_XT + (size_t)d * 4096);
  f4 acc[8];
#pragma unroll
  for (int n = 0; n < 8; ++n) acc[n] = (f4){0.f, 0.f, 0.f, 0.f};
  {
    const int s = (3 - col) & 3;
    const unsigned pa = lds_addr(sm) + KA_K + (4 * ch + s) * KC_PITCH + 2 * (2031 - s - col + 8 * q) - 64 * (2 * wq + 1) - 512 * 7;
    const unsigned pb = lds_addr(sm) + KA_U + ch * U_PITCH + 448 + 32 * col + 16 * q - 64 * (2 * wq + 1);
    u2 al[8], ah[8]; u4 bb[8];
    conv_ld<0, 0>(al, ah, bb, pa, pb);
    conv_mm<0>(al, ah, bb, acc);
    __builtin_amdgcn_sched_barrier(0);
    conv_ld<1, 0>(al, ah, bb, pa, pb);
    conv_mm<0>(al, ah, bb, acc);
  }
  ST(7);
  f4 tot[2];
  {
    f4* red = (f4*)(rc + wq * 6144) + lane;
#pragma unroll
    for (int v = 0; v < 4; ++v) {
      if (v == wq) { tot[0] = acc[2 * v]; tot[1] = acc[2 * v + 1]; }
      else { const int sl = v > wq ? v - 1 : v; red[sl * 128] = acc[2 * v]; red[sl * 128 + 64] = acc[2 * v + 1]; }
    }
  }
  chsync(fl, w & 3, 2);
  ST(8);
#pragma unroll
  for (int v = 0; v < 3; ++v) {
    const int src = v < wq ? v : v + 1;
    const f4* rp = (const f4*)(rc + src * 6144) + (wq > src ? wq - 1 : wq) * 128 + lane;
    tot[0] += rp[0]; tot[1] += rp[64];
  }
  const float sc = slot[0];
  asm volatile("s_waitcnt vmcnt(0)" : "+v"(xv[0]), "+v"(xv[1])::"memory");
#pragma unroll
  for (int nn = 0; nn < 2; ++nn) {
    const int t = 512 * wq + 256 * nn + 16 * col + 4 * q;
    const int pos = (t & ~31) + 16 * ((t >> 2) & 1) + 4 * ((t >> 3) & 3);
    const float h[4] = {__builtin_fmaf(tot[nn][0], sc, bflo(xv[nn].x)), __builtin_fmaf(tot[nn][1], sc, bfhi(xv[nn].x)),
                        __builtin_fmaf(tot[nn][2], sc, bflo(xv[nn].y)), __builtin_fmaf(tot[nn][3], sc, bfhi(xv[nn].y))};
    *(u2*)(ws + OFF_HT + (size_t)(pos >> 6) * 81920 + d * 128 + (pos & 63) * 2) = (u2){pk_bf(h[0], h[1]), pk_bf(h[2], h[3])};
  }
  ST_END(0);
}

DEVI void tile_of(int b, int& i, int& j) { const int g = b & 7, r = b >> 3; i = (g >> 1) * 8 + (r >> 2); j = (g & 1) * 4 + (r & 3); }
constexpr int KB_RED = 81920, KB_SLOT = 7168, KB_Z1 = KB_RED + 65536, KB_S2 = KB_Z1 + 14336, KB_S3 = KB_S2 + 512,
              KB_CNT = KB_S3 + 512, KB_LDS = KB_CNT + 16;
#define KSP 10
#define KFL 15
constexpr int KFLAG = KFL;
constexpr int KSPLIT = KSP;

#define OSF ""
#define POLL_GAP "4"
#define WO_LATE 25
#define TCH 0
#define STSET 0
#define STK(set, i) do { if constexpr (STSET == set) ST(i); } while (0)
template <bool Z> DEVI void mmv(f4& d, const u4& a, const u4& b) {
  if constexpr (Z) asm volatile("v_mfma_f32_16x16x32_bf16 %0, %1, %2, 0" : "=v"(d) : "v"(a), "v"(b));
  else asm volatile("v_mfma_f32_16x16x32_bf16 %0, %1, %2, %0" : "+v"(d) : "v"(a), "v"(b));
}
template <bool Z> DEVI void mmva(f4& d, const u4& a, const u4& b) {
  if constexpr (Z) asm volatile("v_mfma_f32_16x16x32_bf16 %0, %1, %2, 0" : "=v"(d) : "v"(a), "a"(b));
  else asm volatile("v_mfma_f32_16x16x32_bf16 %0, %1, %2, %0" : "+v"(d) : "v"(a), "a"(b));
}
template <bool Z> DEVI void mmav(f4& d, const u4& a, const u4& b) {
  if constexpr (Z) asm volatile("v_mfma_f32_16x16x32_bf16 %0, %1, %2, 0" : "=v"(d) : "a"(a), "v"(b));
  else asm volatile("v_mfma_f32_16x16x32_bf16 %0, %1, %2, %0" : "+v"(d) : "a"(a), "v"(b));
}
template <int IMM> DEVI void gld16a(u4& d, unsigned voff, const void* sb) { asm volatile("global_load_dwordx4 %0, %1, %2 offset:%3" : "=a"(d) : "v"(voff), "s"(sb), "i"(IMM)); }
template <int IMM> DEVI void rld16(u4& d, unsigned voff, const void* sb) { asm volatile("global_load_dwordx4 %0, %1, %2 offset:%3" : "+v"(d) : "v"(voff), "s"(sb), "i"(IMM)); }
template <int IMM> DEVI void rld16s(u4& d, unsigned voff, const void* sb) { asm volatile("global_load_dwordx4 %0, %1, %2 offset:%3 sc1" : "+v"(d) : "v"(voff), "s"(sb), "i"(IMM)); }
template <int IMM> DEVI void trd(u2& d, unsigned a) { asm volatile("ds_read_b64_tr_b16 %0, %1 offset:%2" : "=v"(d) : "v"(a), "i"(IMM)); }
template <int IMM> DEVI void ldsw16(unsigned a, const f4& v) { asm volatile("ds_write_b128 %0, %1 offset:%2" ::"v"(a), "v"(v), "i"(IMM) : "memory"); }
DEVI void dot2(float& acc, unsigned a, unsigned b) { asm volatile("v_dot2_f32_bf16 %0, %1, %2, %0" : "+v"(acc) : "v"(a), "v"(b)); }
DEVI void dot2s(float& acc, unsigned a, unsigned b) { asm volatile("v_dot2_f32_bf16 %0, %1, %2, %0" : "+v"(acc) : "v"(a), "s"(b)); }
DEVI void settle(f4& a, f4& b, f4& c, f4& d) { asm volatile("s_nop 7" : "+v"(a), "+v"(b), "+v"(c), "+v"(d)); }
DEVI void hold(f4& a, f4& b, f4& c, f4& d) { asm volatile("" : "+v"(a), "+v"(b), "+v"(c), "+v"(d)); }
DEVI u4 cat(const u2& a, const u2& b) { return (u4){a.x, a.y, b.x, b.y}; }
DEVI f2 gelu2(f2 x) {
  const f2 u = x * (x * x * -0.1029432397f + -2.3022081985f);
  const f2 d = (f2){__builtin_amdgcn_exp2f(u.x), __builtin_amdgcn_exp2f(u.y)} + 1.0f;
  return x * (f2){__builtin_amdgcn_rcpf(d.x), __builtin_amdgcn_rcpf(d.y)};
}
template <int CTRL, int H> DEVI void bfly(float (&v)[16], bool up) {
#pragma unroll
  for (int k = 0; k < H; ++k) { const float send = up ? v[k] : v[k + H], keep = up ? v[k + H] : v[k]; v[k] = keep + dpp<CTRL>(send); }
}
DEVI float rows_sum(float v) {
  auto a = __builtin_amdgcn_permlane32_swap(__float_as_uint(v), __float_as_uint(v), false, false);
  v = __uint_as_float(a[0]) + __uint_as_float(a[1]);
  auto b = __builtin_amdgcn_permlane16_swap(__float_as_uint(v), __float_as_uint(v), false, false);
  return __uint_as_float(b[0]) + __uint_as_float(b[1]);
}
DEVI int fresh_lane() {
  int l;
  asm volatile("v_mbcnt_lo_u32_b32 %0, -1, 0\n\tv_mbcnt_hi_u32_b32 %0, -1, %0" : "=v"(l));
  return l;
}
DEVI bool last_of_four(unsigned cnt_addr) {
  unsigned o = 0;
  if (fresh_lane() == 0) asm volatile("ds_add_rtn_u32 %0, %1, %2\n\ts_waitcnt lgkmcnt(0)" : "=v"(o) : "v"(cnt_addr), "v"(1u) : "memory");
  return __builtin_amdgcn_readfirstlane(o) == 3;
}
#define POLL1(R) "s_waitcnt vmcnt(2)\n\tv_cmp_ne_u32 vcc, 0, " R "\n\ts_cmp_eq_u64 vcc, -1\n\ts_cbranch_scc1 2f\n\tglobal_load_dword " R ", %4, %5 sc1\n\t"
DEVI void poll8(unsigned (&t)[3], unsigned voff, const void* sb) {
  unsigned n;
  asm volatile(
      "s_mov_b32 %3, 0x20000\n\tglobal_load_dword %0, %4, %5 sc1\n\ts_sleep " POLL_GAP "\n\tglobal_load_dword %1, %4, %5 sc1\n\ts_sleep " POLL_GAP "\n\t"
      "global_load_dword %2, %4, %5 sc1\n1:\n\t" POLL1("%0") POLL1("%1") POLL1("%2") "s_sub_u32 %3, %3, 1\n\ts_cbranch_scc0 1b\n2:"
      : "=&v"(t[0]), "=&v"(t[1]), "=&v"(t[2]), "=&s"(n) : "v"(voff), "s"(sb) : "vcc", "scc", "memory");
}

struct G1 {
  u2 al[2][4], ah[2][4];
  u4 ring[8][2], ones;
  unsigned touch[4], va[2], sb;
  const char* hsrc;
  f4 acc[2][4], sx, sq;
  unsigned pa[4], vb[2];
  const char *b2, *b1;
  u64* st_;
};
struct WS { unsigned vw[2], rd[2], ring; const char* src[4]; };
template <int C, int M> DEVI void w_dma(const WS& x) {
  asm volatile("s_mov_b32 m0, %0\n\ts_nop 0\n\tglobal_load_lds_dwordx4 %1, %3\n\tglobal_load_lds_dwordx4 %2, %3 offset:1024"
               ::"s"(x.ring + (C & 1) * 8192 + M * 2048), "v"(x.vw[0]), "v"(x.vw[1]), "s"(x.src[M] + C * 128) : "memory");
}
DEVI void w_dma_rt(const WS& x, int C, int M) {
  asm volatile("s_mov_b32 m0, %0\n\ts_nop 0\n\tglobal_load_lds_dwordx4 %1, %3\n\tglobal_load_lds_dwordx4 %2, %3 offset:1024"
               ::"s"(x.ring + (C & 1) * 8192 + M * 2048), "v"(x.vw[0]), "v"(x.vw[1]), "s"(x.src[M] + C * 128) : "memory");
}
template <int C, int M, int KH> DEVI void w_read(const WS& x, u4 (&W)[16][4]) {
  asm volatile("ds_read_b128 %0, %1 offset:%2" : "=a"(W[2 * C + KH][M]) : "v"(x.rd[KH]), "i"((C & 1) * 8192 + M * 2048));
}
template <int C> DEVI void w_dma_all(const WS& x) { w_dma<C, 0>(x); w_dma<C, 1>(x); w_dma<C, 2>(x); w_dma<C, 3>(x); }
template <int C> DEVI void w_read_all(const WS& x, u4 (&W)[16][4]) {
  w_read<C, 0, 0>(x, W); w_read<C, 0, 1>(x, W); w_read<C, 1, 0>(x, W); w_read<C, 1, 1>(x, W);
  w_read<C, 2, 0>(x, W); w_read<C, 2, 1>(x, W); w_read<C, 3, 0>(x, W); w_read<C, 3, 1>(x, W);
}
DEVI void w_setup(WS& x, int lane, int w, int i, unsigned sb, const u16* wsp) {
  const int c = lane & 15, g4 = lane >> 4;
  x.ring = sb + KB_RED + w * 16384;
#pragma unroll
  for (int o = 0; o < 2; ++o) {
    x.vw[o] = (unsigned)(16 * o + 8 * (lane >> 5) + ((lane >> 3) & 3)) * 4096 + (((lane & 7) ^ ((4 * o + g4) & 7)) * 16) - 1024 * o;
    x.rd[o] = x.ring + c * 128 + (((4 * o + g4) ^ ((c >> 1) & 7)) * 16);
  }
#pragma unroll
  for (int m = 0; m < 4; ++m) x.src[m] = (const char*)wsp + (size_t)(64 * i + 32 * ((m ^ w) >> 1) + 4 * ((m ^ w) & 1)) * 4096 + w * 1024;
}
template <int KS> DEVI const char* wf_base(const char* b) { return b + (KS >> 3) * 8192 + 4096; }
template <int KS, int X> DEVI void ld_new(G1& g, u4& d, const char* b) { gld16<(KS & 7) * 1024 - 4096>(d, g.vb[X], wf_base<KS>(b)); }
template <int KS, int X> DEVI void ld_ring(G1& g, u4& d, const char* b) { rld16<(KS & 7) * 1024 - 4096>(d, g.vb[X], wf_base<KS>(b)); }
template <int S, int M> DEVI void rd_lo(G1& g) { trd<(S & 15) * 4096>(g.al[S & 1][M], g.pa[M]); }
template <int S, int M> DEVI void rd_hi(G1& g) { trd<(S & 15) * 4096 + 512>(g.ah[S & 1][M], g.pa[M]); }
template <int N, int LG> DEVI void g1_wait(G1& g, int c, int m) {
  asm volatile("s_waitcnt vmcnt(%4) lgkmcnt(%5)" : "+v"(g.al[c][m]), "+v"(g.ah[c][m]), "+v"(g.al[c][m + 1]), "+v"(g.ah[c][m + 1]) : "i"(N), "i"(LG) : "memory");
}
template <int S> DEVI void pa_step(G1& g) {
  if constexpr (S == 16) { for (int m = 0; m < 4; ++m) g.pa[m] += 65536; }
  if constexpr (S == 20) { for (int m = 0; m < 4; ++m) g.pa[m] -= 65536; }
}
template <int KS, int H> DEVI void a_dma(const G1& g) {
  asm volatile("s_mov_b32 m0, %0\n\ts_nop 0\n\tglobal_load_lds_dwordx4 %1, %3\n\tglobal_load_lds_dwordx4 %2, %3 offset:1024"
               ::"s"(g.sb + KS * 4096 + H * 2048), "v"(g.va[0]), "v"(g.va[1]), "s"(g.hsrc + KS * 4096 + H * 2048) : "memory");
}
template <int G, int X> DEVI void b_next(G1& g) {
  if constexpr (G + 8 < 20) ld_ring<G + 8, X>(g, g.ring[G & 7][X], g.b2);
  else if constexpr (G + 8 < 40) ld_ring<G + 8 - 20, X>(g, g.ring[G & 7][X], g.b1);
}
constexpr bool w_rd_at(int s) { return (s >= 0 && s < 8) || (s >= 10 && s < 18); }
constexpr bool w_dma_at(int s) { return w_rd_at(s - 1); }
constexpr int w_tile(int s) { return s < 8 ? s : s - 2; }
constexpr int ops_at(int nb, int k) { return (k < 32 ? nb : 0) + (nb == 1 && k < 10 ? 4 : 0) + (nb == 1 && k == 20 ? 3 : 0) + (w_dma_at(k - 20) ? 2 : 0); }
constexpr int ops_in(int nb, int a, int b) { int n = 0; for (int k = a; k < b; ++k) n += ops_at(nb, k); return n; }
constexpr int imin(int a, int b) { return a < b ? a : b; }
constexpr int g1_vm(int nb, int G) {
  int n = G < 4 ? nb * (7 - G) + 6 + ops_in(nb, 0, G) : G < 8 ? nb * (7 - G) + ops_in(nb, 0, G) : ops_in(nb, G - 7, G);
  if (G == 3) n = imin(n, 4 * nb + ops_in(nb, 0, 3));
  if (nb == 1 && G == 9) n = imin(n, 1 + ops_in(1, 5, 9));
  if (nb == 1 && G == 14) n = imin(n, 1 + ops_in(1, 10, 14));
  if (G >= 20 && G < 28) n = 2 * (27 - G) + ops_in(nb, 20, G);
  if (G >= 30 && G < 38) n = imin(n, (G - 9 < 32 ? nb - 1 : 0) + ops_in(nb, G - 8, G));
  return imin(n, 63);
}
template <int NB, int S> DEVI void g1_first(G1& g) {
  if constexpr (S < 20) {
    constexpr int c = S & 1, nx = (S + 1) % 20;
    constexpr bool Z = S == 0;
    g1_wait<g1_vm(NB, S), 4>(g, c, 0);
    if constexpr (S == 3 || S == 9 || S == 14) asm volatile("s_barrier" ::: "memory");
    { u64* st_ = g.st_; if constexpr (S == 1) STK(1, 4); if constexpr (S == 5) STK(1, 5); if constexpr (S == 10) STK(1, 6); if constexpr (S == 15) STK(1, 7); if constexpr (S == 19) STK(1, 8); }
    pa_step<S + 1>(g);
    const u4& b0 = g.ring[S & 7][0]; const u4& b1 = g.ring[S & 7][1];
    const u4 a0 = cat(g.al[c][0], g.ah[c][0]), a1 = cat(g.al[c][1], g.ah[c][1]);
    mmv<Z>(g.acc[0][0], a0, b0); rd_lo<nx, 0>(g);
    if constexpr (NB == 2) mmv<Z>(g.acc[1][0], a0, b1);
    rd_hi<nx, 0>(g);
    mmv<Z>(g.sx, a0, g.ones);
    if constexpr (NB == 1 && S < 10) a_dma<S + 10, 0>(g);
    mmv<Z>(g.acc[0][1], a1, b0); rd_lo<nx, 1>(g);
    if constexpr (NB == 2) mmv<Z>(g.acc[1][1], a1, b1);
    rd_hi<nx, 1>(g);
    mmv<Z>(g.sq, a0, a0);
    if constexpr (NB == 1 && S < 10) a_dma<S + 10, 1>(g);
    g1_wait<63, 4>(g, c, 2);
    const u4 a2 = cat(g.al[c][2], g.ah[c][2]), a3 = cat(g.al[c][3], g.ah[c][3]);
    mmv<Z>(g.acc[0][2], a2, b0); rd_lo<nx, 2>(g);
    if constexpr (NB == 2) mmv<Z>(g.acc[1][2], a2, b1);
    rd_hi<nx, 2>(g);
    mmv<Z>(g.acc[0][3], a3, b0); rd_lo<nx, 3>(g);
    b_next<S, 0>(g);
    if constexpr (NB == 2) mmv<Z>(g.acc[1][3], a3, b1);
    rd_hi<nx, 3>(g);
    if constexpr (NB == 2) b_next<S, 1>(g);
    g1_first<NB, S + 1>(g);
  }
}
template <int NB, int S, int X, int M> DEVI void mm1(G1& g) {
  if constexpr (X < NB) mmv<S == 0>(g.acc[X][M], g.ring[(S + 20) & 7][X], cat(g.al[S & 1][M], g.ah[S & 1][M]));
}
template <int NB, int S, int E> DEVI void g1_second(G1& g, const WS& x, u4 (&W)[16][4], unsigned tv, const char* tb) {
  if constexpr (S < E) {
    constexpr bool R = S < 19, WR = w_rd_at(S), WP = w_rd_at(S - 1);
    constexpr int T = w_tile(S), TD = w_tile(S - 1) + 8;
    g1_wait<g1_vm(NB, S + 20), (WP ? 5 : 4)>(g, S & 1, 0);
    { u64* st_ = g.st_; if constexpr (S == 0) STK(2, 1); if constexpr (S == 5) STK(2, 2); if constexpr (S == 10) STK(2, 5); if constexpr (S == 15) STK(2, 6); if constexpr (S == 19) STK(2, 7); }
    if constexpr (R) pa_step<S + 1>(g);
    if constexpr (NB == 1 && S == 0) { gld4<0>(g.touch[0], tv, tb); gld4<0>(g.touch[1], tv, tb + 45 * 128); gld4<0>(g.touch[2], tv, tb + 90 * 128); }
    mm1<NB, S, 0, 0>(g); if constexpr (R) rd_lo<S + 1, 0>(g);
    mm1<NB, S, 1, 0>(g); if constexpr (R) rd_hi<S + 1, 0>(g);
    if constexpr (WR) w_read<(T >> 2), (T & 3), 0>(x, W);
    mm1<NB, S, 0, 1>(g); if constexpr (R) rd_lo<S + 1, 1>(g);
    mm1<NB, S, 1, 1>(g); if constexpr (R) rd_hi<S + 1, 1>(g);
    g1_wait<63, (R ? (WR ? 5 : 4) : 0)>(g, S & 1, 2);
    mm1<NB, S, 0, 2>(g); if constexpr (R) rd_lo<S + 1, 2>(g);
    mm1<NB, S, 1, 2>(g); if constexpr (R) rd_hi<S + 1, 2>(g);
    if constexpr (WR) w_read<(T >> 2), (T & 3), 1>(x, W);
    mm1<NB, S, 0, 3>(g); if constexpr (R) rd_lo<S + 1, 3>(g);
    b_next<S + 20, 0>(g);
    if constexpr (w_dma_at(S)) w_dma<(TD >> 2), (TD & 3)>(x);
    mm1<NB, S, 1, 3>(g); if constexpr (R) rd_hi<S + 1, 3>(g);
    if constexpr (NB == 2) b_next<S + 20, 1>(g);
    g1_second<NB, S + 1, E>(g, x, W, tv, tb);
  }
}
template <int NB, int KS, int E> DEVI void g1_pro(G1& g) {
  if constexpr (KS < E) { ld_new<KS, 0>(g, g.ring[KS][0], g.b2); if constexpr (NB == 2) ld_new<KS, 1>(g, g.ring[KS][1], g.b2); g1_pro<NB, KS + 1, E>(g); }
}
#define FOLD2(CT, MA, MB, H) \
  "v_add_f32_dpp %0, %0, %0 " CT " row_mask:0xf bank_mask:" MA "\n\tv_add_f32_dpp %1, %1, %1 " CT " row_mask:0xf bank_mask:" MA "\n\t" \
  "v_add_f32_dpp %2, %2, %2 " CT " row_mask:0xf bank_mask:" MA "\n\tv_add_f32_dpp %3, %3, %3 " CT " row_mask:0xf bank_mask:" MA "\n\t" \
  "v_add_f32_dpp %0, %4, %4 " CT " row_mask:0xf bank_mask:" MB "\n\tv_add_f32_dpp %1, %5, %5 " CT " row_mask:0xf bank_mask:" MB "\n\t" \
  "v_add_f32_dpp %2, %6, %6 " CT " row_mask:0xf bank_mask:" MB "\n\tv_add_f32_dpp %3, %7, %7 " CT " row_mask:0xf bank_mask:" MB
DEVI void fold16(float (&v)[16]) {
  asm volatile("s_nop 1\n\t" FOLD2("row_mirror", "0x3", "0xc", 8) : "+v"(v[0]), "+v"(v[1]), "+v"(v[2]), "+v"(v[3]) : "v"(v[8]), "v"(v[9]), "v"(v[10]), "v"(v[11]));
  asm volatile("s_nop 1\n\t" FOLD2("row_mirror", "0x3", "0xc", 8) : "+v"(v[4]), "+v"(v[5]), "+v"(v[6]), "+v"(v[7]) : "v"(v[12]), "v"(v[13]), "v"(v[14]), "v"(v[15]));
  asm volatile(FOLD2("row_half_mirror", "0x5", "0xa", 4) "\n\t"
               "v_add_f32_dpp %0, %0, %0 quad_perm:[2,3,0,1] row_mask:0xf bank_mask:0xf\n\tv_add_f32_dpp %1, %1, %1 quad_perm:[2,3,0,1] row_mask:0xf bank_mask:0xf\n\t"
               "v_add_f32_dpp %2, %2, %2 quad_perm:[2,3,0,1] row_mask:0xf bank_mask:0xf\n\tv_add_f32_dpp %3, %3, %3 quad_perm:[2,3,0,1] row_mask:0xf bank_mask:0xf\n\t"
               "v_add_f32_dpp %0, %0, %0 quad_perm:[1,0,3,2] row_mask:0xf bank_mask:0xf\n\tv_add_f32_dpp %1, %1, %1 quad_perm:[1,0,3,2] row_mask:0xf bank_mask:0xf\n\t"
               "v_add_f32_dpp %2, %2, %2 quad_perm:[1,0,3,2] row_mask:0xf bank_mask:0xf\n\tv_add_f32_dpp %3, %3, %3 quad_perm:[1,0,3,2] row_mask:0xf bank_mask:0xf"
               : "+v"(v[0]), "+v"(v[1]), "+v"(v[2]), "+v"(v[3]) : "v"(v[4]), "v"(v[5]), "v"(v[6]), "v"(v[7]));
}

struct G2 {
  u4 z[4][7];
  f4 acc[7][4];
  unsigned vz, vz6, slot[3];
  const char *zsrc, *wo, *wo6;
};
template <int S, int FB> DEVI void ld_z(G2& g) {
  const char* sb = g.zsrc + (size_t)S * (NGB * 1024);
  if constexpr (S < 4) gld16s<(FB - 3) * 1024>(g.z[S][FB], FB == 6 ? g.vz6 : g.vz, sb);
  else rld16s<(FB - 3) * 1024>(g.z[S & 3][FB], FB == 6 ? g.vz6 : g.vz, sb);
}
template <int S, int FB> DEVI void ld_z_step(G2& g) { ld_z<S, FB>(g); if constexpr (FB < 6) ld_z_step<S, FB + 1>(g); }
template <int N> DEVI void ld_wo(G2& g, u4 (&WO)[7][5]) {
  constexpr int KS = N / 5, DB = N % 5;
  if constexpr (KS < 6) gld16a<(KS - 3) * 1024>(WO[KS][DB], g.vz, g.wo + DB * (NKS3 * 1024));
  else gld16a<0>(WO[KS][DB], g.vz, g.wo6 == nullptr ? g.wo + DB * (NKS3 * 1024) + 3072 : g.wo6);
}
template <int S, int FB> DEVI void g2_main(G2& g, u4 (&W)[16][4], u4 (&WO)[7][5], u64* st_) {
  if constexpr (S < 14) {
    if constexpr (S == 12 && FB == 0) asm volatile("s_waitcnt lgkmcnt(0)" ::: "memory");
    waitv<27>();
    if constexpr (FB == 0) { if constexpr (S == 0) { STK(0, 5); STK(4, 1); } if constexpr (S == 2) STK(4, 2); if constexpr (S == 4) STK(4, 4); if constexpr (S == 6) STK(4, 5); if constexpr (S == 8) STK(4, 6); if constexpr (S == 10) STK(4, 7); if constexpr (S == 13) STK(4, 8); }
    mmva<S == 0>(g.acc[FB][0], g.z[S & 3][FB], W[S][0]); mmva<S == 0>(g.acc[FB][1], g.z[S & 3][FB], W[S][1]);
    mmva<S == 0>(g.acc[FB][2], g.z[S & 3][FB], W[S][2]); mmva<S == 0>(g.acc[FB][3], g.z[S & 3][FB], W[S][3]);
    if constexpr (S < 12) ld_z<S + 4, FB>(g); else ld_wo<7 * (S - 12) + FB>(g, WO);
    if constexpr (FB < 6) g2_main<S, FB + 1>(g, W, WO, st_); else g2_main<S + 1, 0>(g, W, WO, st_);
  }
}
template <int GI, int K> DEVI void g2_tail(G2& g, u4 (&W)[16][4], u4 (&WO)[7][5]) {
  if constexpr (GI < 4) {
    constexpr int m = (GI + 1) & 3, s = 14 + K / 7, fb = K % 7;
    if constexpr (GI == 0 && K == 0) waitv<21>();
    if constexpr (GI == 0 && K == 7) waitv<14>();
    mmva<false>(g.acc[fb][m], g.z[s & 3][fb], W[s][m]);
    if constexpr (GI > 0 && K >= 1 && K < 8) ldsw16<(K - 1) * 1024>(g.slot[GI - 1], g.acc[K - 1][GI]);
    if constexpr (K >= 8 && 14 + 6 * GI + K - 8 < WO_LATE) ld_wo<14 + 6 * GI + K - 8>(g, WO);
    if constexpr (K < 13) g2_tail<GI, K + 1>(g, W, WO); else g2_tail<GI + 1, 0>(g, W, WO);
  }
}
template <int N> DEVI void ld_wo_rest(G2& g, u4 (&WO)[7][5]) { if constexpr (N < 35) { ld_wo<N>(g, WO); ld_wo_rest<N + 1>(g, WO); } }
struct G3 { u4 v[4][7]; f4 acc[5][4]; };
constexpr int V_PRE = 14;
template <int N, int E> DEVI void ld_v(G3& g, unsigned vo, unsigned vo6, const char* const (&vb)[4]) {
  if constexpr (N < E && N < 28) { gld16s<(N % 7 - 3) * 1024>(g.v[N / 7][N % 7], N % 7 == 6 ? vo6 : vo, vb[(N / 7 + 1) & 3]); ld_v<N + 1, E>(g, vo, vo6, vb); }
}
template <int N> DEVI void g3_loop(G3& g, u4 (&WO)[7][5], const unsigned (&slot)[3], unsigned vo, unsigned vo6, const char* const (&vb)[4], u64* st_) {
  if constexpr (N < 28) {
    constexpr int GI = N / 7, s = N % 7, m = (GI + 1) & 3;
    waitv<imin(V_PRE + 2 * N, 28) - 1 - N>();
    if constexpr (N == 0) STK(0, 8);
    mmav<s == 0>(g.acc[0][m], WO[s][0], g.v[GI][s]); mmav<s == 0>(g.acc[1][m], WO[s][1], g.v[GI][s]);
    ld_v<V_PRE + 2 * N, V_PRE + 2 * N + 1>(g, vo, vo6, vb);
    mmav<s == 0>(g.acc[2][m], WO[s][2], g.v[GI][s]); mmav<s == 0>(g.acc[3][m], WO[s][3], g.v[GI][s]);
    ld_v<V_PRE + 2 * N + 1, V_PRE + 2 * N + 2>(g, vo, vo6, vb);
    mmav<s == 0>(g.acc[4][m], WO[s][4], g.v[GI][s]);
    if constexpr (GI > 0 && s >= 1 && s < 6) ldsw16<(s - 1) * 1024>(slot[GI - 1], g.acc[s - 1][GI]);
    g3_loop<N + 1>(g, WO, slot, vo, vo6, vb, st_);
  }
}

struct Late { f4 acc[4]; float r[4], n[4]; unsigned o[4], rd[2], t[3]; u4 cs; };
DEVI void z1_tile(const Late& t, char* sm, int m) {
  const f2 c0 = {__uint_as_float(t.cs.x), __uint_as_float(t.cs.y)}, c1 = {__uint_as_float(t.cs.z), __uint_as_float(t.cs.w)};
  const f2 y0 = gelu2((f2){t.acc[m][0], t.acc[m][1]} * t.r[m] + c0 * t.n[m]), y1 = gelu2((f2){t.acc[m][2], t.acc[m][3]} * t.r[m] + c1 * t.n[m]);
  *(u2*)(sm + t.o[m]) = (u2){pk_bf(y0.x, y0.y), pk_bf(y1.x, y1.y)};
}
template <int NB> DEVI void phase1(char* sm, char* ws, const int i, const int j, const int w, const int gs, const int cnt, u4 (&W)[16][4], Late& late, const u16* wsp, u64 ta, u64 tb_, u64* st_) {
  const int lane = fresh_lane();
  const int c = lane & 15, g4 = lane >> 4;
  const bool liveA = 2 * w < cnt;
  const int ga = gs + (liveA ? 2 * w : cnt - 1), gb = ga + 1;
  const unsigned sb = lds_addr(sm);
  G1 g;
  g.st_ = st_;
  g.vb[0] = (unsigned)ga * (NKS1 * 1024) + lane * 16; g.vb[1] = g.vb[0] + NKS1 * 1024;
  g.b2 = ws + OFF_WF; g.b1 = ws + OFF_WF + (size_t)NGB * NKS1 * 1024;
  unsigned cs2[2]; u4 cs1[2];
  {
    const int r8 = lane >> 3, sg = (r8 >> 1) & 1;
    g.va[0] = r8 * 128 + (((lane & 7) ^ (2 * sg)) * 16); g.va[1] = r8 * 128 + (((lane & 7) ^ (2 * sg + 4)) * 16);
    g.sb = sb; g.hsrc = ws + OFF_HT + (size_t)i * 81920;
    const unsigned vo = w & 1 ? g.va[1] : g.va[0];
#pragma unroll
    for (int n = 0; n < 4; ++n) dma16(sb + (w + 4 * n) * 1024, vo, g.hsrc + (w + 4 * n) * 1024);
#pragma unroll
    for (int x = 0; x < NB; ++x) { gld4<0>(cs2[x], (FH + 16 * (ga + x) + c) * 4, ws + OFF_CS); gld16<0>(cs1[x], (16 * (ga + x) + 4 * g4) * 4, ws + OFF_CS); }
    g1_pro<NB, 0, 4>(g);
    STK(5, 1);
#pragma unroll
    for (int n = 4; n < 10; ++n) dma16(sb + (w + 4 * n) * 1024, vo, g.hsrc + (w + 4 * n) * 1024);
    g1_pro<NB, 4, 8>(g);
    if constexpr (NB == 1) {
      ta += ((lane >> 3) & 3) * (NKS3 * 1024) + (lane & 7) * 128; tb_ += (lane & 31) * (4 * D);
      const u64 tp = TCH == 1 ? ta : tb_ + ((ta - tb_) & (0ull - (lane < 32)));
      if (TCH != 2) asm volatile("global_load_dword %0, %1, off sc1" : "=v"(g.touch[3]) : "v"(tp)); else g.touch[3] = 0;
    }
  }
  STK(5, 4);
  WS x;
  w_setup(x, lane, w, i, sb, wsp);
  {
    const int qq = c >> 2, p = c & 3, sh = (qq >> 1) | ((g4 & 1) << 1);
#pragma unroll
    for (int m = 0; m < 4; ++m) {
      g.pa[m] = sb + (8 * g4 + qq) * 128 + (p >> 1) * 16 + 8 * (p & 1) + (m ^ w ^ sh) * 32;
    }
  }
  g.ones = (u4){0x3f803f80u, 0x3f803f80u, 0x3f803f80u, 0x3f803f80u};
  unsigned aq[4], al[4], zo[2], z1o[4], po, so;
#pragma unroll
  for (int m = 0; m < 4; ++m) { aq[m] = KB_S2 + (m ^ w) * 64 + g4 * 16; al[m] = KB_S2 + (m ^ w) * 64 + c * 4; z1o[m] = KB_Z1 + ((m ^ w) * 7 + 2 * w) * 512 + lane * 8; }
  zo[0] = (unsigned)(w >> 1) * (NGB * 1024) + lane * 16; zo[1] = (unsigned)(1 ^ (w >> 1)) * (NGB * 1024) + lane * 16;
  po = KB_Z1 + (w * 64 + 16 * ((c >> 2) ^ w) + 4 * g4) * 4; so = KB_S2 + (16 * w + c) * 4;
  const char* zb = ws + OFF_Z2 + ((size_t)2 * i * NGB + ga) * 1024;
  u64* st = (u64*)(ws + OFF_ST) + (size_t)i * 512;
  asm volatile("" : "+v"(aq[0]), "+v"(aq[1]), "+v"(aq[2]), "+v"(aq[3]), "+v"(al[0]), "+v"(al[1]), "+v"(al[2]), "+v"(al[3]), "+v"(zo[0]), "+v"(zo[1]),
               "+v"(z1o[0]), "+v"(z1o[1]), "+v"(z1o[2]), "+v"(z1o[3]), "+v"(po), "+v"(so), "+v"(x.vw[0]), "+v"(x.vw[1]), "+v"(x.rd[0]), "+v"(x.rd[1]));
  asm volatile("" : "+s"(x.src[0]), "+s"(x.src[1]), "+s"(x.src[2]), "+s"(x.src[3]), "+s"(x.ring), "+s"(zb), "+s"(st));
  if (w == 0 && lane < 4) *(unsigned*)(sm + KB_CNT + 4 * lane) = 0;
  if constexpr (NB == 2) asm volatile("s_waitcnt vmcnt(22)" : "+v"(cs2[0]), "+v"(cs2[1]), "+v"(cs1[0]), "+v"(cs1[1]), "+v"(g.ones)::"memory");
  else asm volatile("s_waitcnt vmcnt(14)" : "+v"(cs2[0]), "+v"(cs1[0]), "+v"(g.ones)::"memory");
  STK(5, 5);
  bar();
  STK(0, 1); STK(1, 1); STK(5, 6);
  rd_lo<0, 0>(g); rd_hi<0, 0>(g); rd_lo<0, 1>(g); rd_hi<0, 1>(g); rd_lo<0, 2>(g); rd_hi<0, 2>(g); rd_lo<0, 3>(g); rd_hi<0, 3>(g);
  g1_first<NB, 0>(g);
  asm volatile("s_nop 1" : "+v"(g.sx), "+v"(g.sq));
  {
    const float sx = c & 2 ? (c & 1 ? g.sx[3] : g.sx[2]) : (c & 1 ? g.sx[1] : g.sx[0]);
    const float sq = c & 2 ? (c & 1 ? g.sq[3] : g.sq[2]) : (c & 1 ? g.sq[1] : g.sq[0]);
    const f2 st = ln_stats(sx, sq, 1.0f / D);
    if ((c >> 2) == g4) { *(float*)(sm + so) = st.y; *(float*)(sm + so + 256) = -st.x * st.y; }
  }
  bar();
  hold(g.acc[0][0], g.acc[0][1], g.acc[0][2], g.acc[0][3]);
  if constexpr (NB == 2) hold(g.acc[1][0], g.acc[1][1], g.acc[1][2], g.acc[1][3]);
  STK(0, 2); STK(1, 2);
  f2 z[2][8];
  float r1[4], n1[4];
  float* ps = (float*)(sm + KB_Z1); float* pq = ps + 256;
  {
    float sv[16], qv[16];
    f4 r4[4], n4[4];
#pragma unroll
    for (int m = 0; m < 4; ++m) { r4[m] = *(const f4*)(sm + aq[m]); n4[m] = *(const f4*)(sm + aq[m] + 256); }
#pragma unroll
    for (int m = 0; m < 4; ++m) { r1[m] = *(const float*)(sm + al[m]); n1[m] = *(const float*)(sm + al[m] + 256); }
#pragma unroll
    for (int m = 0; m < 4; ++m) {
#pragma unroll
      for (int e = 0; e < 2; ++e) {
        const f2 r = {r4[m][2 * e], r4[m][2 * e + 1]}, n = {n4[m][2 * e], n4[m][2 * e + 1]};
#pragma unroll
        for (int y = 0; y < NB; ++y) z[y][2 * m + e] = gelu2((f2){g.acc[y][m][2 * e], g.acc[y][m][2 * e + 1]} * r + n * __uint_as_float(cs2[y]));
        f2 s2 = z[0][2 * m + e], q2 = s2 * s2;
        if constexpr (NB == 2) { s2 += z[1][2 * m + e]; q2 += z[1][2 * m + e] * z[1][2 * m + e]; }
        sv[4 * m + 2 * e] = s2.x; sv[4 * m + 2 * e + 1] = s2.y; qv[4 * m + 2 * e] = q2.x; qv[4 * m + 2 * e + 1] = q2.y;
        __builtin_amdgcn_sched_barrier(0);
        w_dma_rt(x, (2 * m + e) >> 2, (2 * m + e) & 3);
        __builtin_amdgcn_sched_barrier(0);
      }
    }
    fold16(sv); fold16(qv);
    if ((c & 3) == 0) {
      *(f4*)(sm + po) = liveA ? (f4){sv[0], sv[1], sv[2], sv[3]} : (f4){0.f, 0.f, 0.f, 0.f};
      *(f4*)(sm + po + 1024) = liveA ? (f4){qv[0], qv[1], qv[2], qv[3]} : (f4){0.f, 0.f, 0.f, 0.f};
    }
    if (last_of_four(sb + KB_CNT)) {
      const float ts = (ps[lane] + ps[64 + lane]) + (ps[128 + lane] + ps[192 + lane]), tq = (pq[lane] + pq[64 + lane]) + (pq[128 + lane] + pq[192 + lane]);
      __hip_atomic_store(st + j * 64 + lane, ((u64)(__float_as_uint(tq) | 0x80000000u) << 32) | __float_as_uint(ts), __ATOMIC_RELAXED, __HIP_MEMORY_SCOPE_AGENT);
    }
  }
  asm volatile("" : "+v"(r1[0]), "+v"(r1[1]), "+v"(r1[2]), "+v"(r1[3]), "+v"(n1[0]), "+v"(n1[1]), "+v"(n1[2]), "+v"(n1[3]));
  ST(3);
  const unsigned tv = (lane < 45 ? lane : 44) * 128;
  const char* tb = ws + OFF_WO + (size_t)5 * j * NKS3 * 1024 + (i & 7) * 135 * 128;
  g1_second<NB, 0, KSPLIT>(g, x, W, tv, tb);
  if constexpr (NB == 1) asm volatile("" : "+v"(g.touch[0]), "+v"(g.touch[1]), "+v"(g.touch[2]), "+v"(g.touch[3]));
  float* rs3 = (float*)(sm + KB_S3); float* nm3 = rs3 + 64;
  STK(2, 8);
  if constexpr (NB == 1) {
    u2 gv[8]; unsigned n;
    asm volatile(
        "s_mov_b32 %8, 0x40000\n"
        "1:\n\tglobal_load_dwordx2 %0, %9, %10 sc1\n\tglobal_load_dwordx2 %1, %9, %10 offset:512 sc1\n\tglobal_load_dwordx2 %2, %9, %10 offset:1024 sc1\n\t"
        "global_load_dwordx2 %3, %9, %10 offset:1536 sc1\n\tglobal_load_dwordx2 %4, %9, %10 offset:2048 sc1\n\tglobal_load_dwordx2 %5, %9, %10 offset:2560 sc1\n\t"
        "global_load_dwordx2 %6, %9, %10 offset:3072 sc1\n\tglobal_load_dwordx2 %7, %9, %10 offset:3584 sc1\n\ts_waitcnt vmcnt(0)\n\t"
        "v_cmp_gt_i64 vcc, 0, %0\n\tv_cmp_gt_i64 s[44:45], 0, %1\n\tv_cmp_gt_i64 s[46:47], 0, %2\n\tv_cmp_gt_i64 s[48:49], 0, %3\n\t"
        "s_and_b64 vcc, vcc, s[44:45]\n\ts_and_b64 s[46:47], s[46:47], s[48:49]\n\ts_and_b64 s[50:51], vcc, s[46:47]\n\t"
        "v_cmp_gt_i64 vcc, 0, %4\n\tv_cmp_gt_i64 s[44:45], 0, %5\n\tv_cmp_gt_i64 s[46:47], 0, %6\n\tv_cmp_gt_i64 s[48:49], 0, %7\n\t"
        "s_and_b64 vcc, vcc, s[44:45]\n\ts_and_b64 s[46:47], s[46:47], s[48:49]\n\ts_and_b64 vcc, vcc, s[46:47]\n\ts_and_b64 vcc, vcc, s[50:51]\n\t"
        "s_sub_u32 %8, %8, 1\n\ts_cbranch_scc1 2f\n\ts_cmp_eq_u64 vcc, -1\n\ts_cbranch_scc1 2f\n\ts_sleep 1\n\ts_branch 1b\n2:"
        : "=&v"(gv[0]), "=&v"(gv[1]), "=&v"(gv[2]), "=&v"(gv[3]), "=&v"(gv[4]), "=&v"(gv[5]), "=&v"(gv[6]), "=&v"(gv[7]), "=&s"(n)
        : "v"(lane * 8), "s"(st)
        : "vcc", "scc", "s44", "s45", "s46", "s47", "s48", "s49", "s50", "s51", "memory");
    float ts = 0.f, tq = 0.f;
#pragma unroll
    for (int v = 0; v < 8; ++v) { ts += __uint_as_float(gv[v].x); tq += __uint_as_float(gv[v].y & 0x7fffffffu); }
    const f2 s3 = ln_stats(ts, tq, 1.0f / FH);
    rs3[lane] = s3.y; nm3[lane] = -s3.x * s3.y;
  }
  bar();
  STK(0, 4); STK(2, 4); STK(3, 4);
  if (liveA) {
    unsigned o[2][8];
#pragma unroll
    for (int m = 0; m < 4; ++m) {
      const f4 r4 = *(const f4*)(sm + aq[m] + 512), n4 = *(const f4*)(sm + aq[m] + 768);
#pragma unroll
      for (int e = 0; e < 2; ++e) {
        const f2 r = {r4[2 * e], r4[2 * e + 1]}, n = {n4[2 * e], n4[2 * e + 1]};
#pragma unroll
        for (int x = 0; x < NB; ++x) { const f2 a = z[x][2 * m + e] * r + n; o[x][2 * m + e] = pk_bf(a.x, a.y); }
      }
    }
    const unsigned r0 = zo[0], r1 = zo[1];
    if (w & 1) {
#pragma unroll
      for (int x = 0; x < NB; ++x) { gst16s(r0, (u4){o[x][2], o[x][3], o[x][0], o[x][1]}, zb + x * 1024); gst16s(r1, (u4){o[x][6], o[x][7], o[x][4], o[x][5]}, zb + x * 1024); }
    } else {
#pragma unroll
      for (int x = 0; x < NB; ++x) { gst16s(r0, (u4){o[x][0], o[x][1], o[x][2], o[x][3]}, zb + x * 1024); gst16s(r1, (u4){o[x][4], o[x][5], o[x][6], o[x][7]}, zb + x * 1024); }
    }
  }
  __builtin_amdgcn_sched_barrier(0);
  STK(3, 1);
  g1_second<NB, KSPLIT, KFLAG>(g, x, W, tv, tb);
  waitv<ops_in(NB, KSPLIT + 20, KFLAG + 20)>();
  if (last_of_four(sb + KB_CNT + 4)) {
    if (lane == 0) __hip_atomic_store((unsigned*)(ws + OFF_F2) + (32 * j + i) * 32, 1u, __ATOMIC_RELAXED, __HIP_MEMORY_SCOPE_AGENT);
  }
  STK(3, 2);
  g1_second<NB, KFLAG, 20>(g, x, W, tv, tb);
  STK(3, 5);
  settle(g.acc[0][0], g.acc[0][1], g.acc[0][2], g.acc[0][3]);
#pragma unroll
  for (int m = 0; m < 4; ++m) { late.acc[m] = g.acc[0][m]; late.r[m] = r1[m]; late.n[m] = n1[m]; late.o[m] = z1o[m]; }
  late.cs = cs1[0];
  if constexpr (NB == 2) {
#pragma unroll
    for (int m = 0; m < 4; ++m) z1_tile(late, sm, m);
    hold(g.acc[1][0], g.acc[1][1], g.acc[1][2], g.acc[1][3]);
#pragma unroll
    for (int m = 0; m < 4; ++m) { late.acc[m] = g.acc[1][m]; late.o[m] += 512; }
    late.cs = cs1[1];
  }
  __builtin_amdgcn_sched_barrier(0);
  STK(3, 6);
  waitv<0>();
  w_read_all<4>(x, W); w_read_all<5>(x, W);
  asm volatile("s_waitcnt lgkmcnt(8)" ::: "memory");
  w_dma_all<6>(x);
  asm volatile("s_waitcnt lgkmcnt(0)" ::: "memory");
  w_dma_all<7>(x);
  late.rd[0] = x.rd[0]; late.rd[1] = x.rd[1];
  STK(3, 7);
  poll8(late.t, (lane & 7) * 128, ws + OFF_F2 + (32 * j + 8 * w) * 128);
  STK(3, 8);
}

__global__ __launch_bounds__(256, 1) void kb(const u16* __restrict__ wsp, const u16* __restrict__ bsp, char* __restrict__ ws, u16* __restrict__ out) {
  __shared__ __attribute__((aligned(16))) char sm[KB_LDS];
  const int w = __builtin_amdgcn_readfirstlane(threadIdx.x >> 6);
  int i, j; tile_of(blockIdx.x, i, j);
  const int gs = j < 7 ? 7 * j : 48, cnt = j < 6 ? 7 : 6;
  const unsigned sb = lds_addr(sm);
  const int hz = j < 4 ? 40960 : 0;
  ST_DECL
  u4 W[16][4];
  Late late;
  const char* vst = ws + OFF_V + ((size_t)4 * i * NKS3 + (gs >> 1)) * 1024;
  u64 ta = (u64)vst + (0x15 >> j & 1) * 3072, tb = (u64)(out + 64 * i * D + 80 * j);
  asm volatile("" : "+s"(ta), "+s"(tb));
  if (w < 3) phase1<2>(sm, ws, i, j, w, gs, cnt, W, late, wsp, ta, tb, st_); else phase1<1>(sm, ws, i, j, w, gs, cnt, W, late, wsp, ta, tb, st_);
  G2 h;
  unsigned bias;
  {
    const int ln = fresh_lane(), c = ln & 15;
    h.vz = ln * 16 + 3072; h.vz6 = cnt == 7 ? h.vz : h.vz - 1024;
    gld2(bias, (64 * i + 32 * (w >> 1) + 8 * (c >> 2) + 4 * (w & 1) + (c & 3)) * 2, bsp);
  }
  h.zsrc = ws + OFF_Z2 + ((size_t)16 * w * NGB + gs) * 1024;
  h.wo = ws + OFF_WO + ((size_t)5 * j * NKS3 + 7 * w) * 1024;
  h.wo6 = w == 3 ? ws + OFF_ZE - 3072 : nullptr;
  ld_z_step<0, 0>(h);
  {
    WS x;
    x.rd[0] = late.rd[0]; x.rd[1] = late.rd[1];
    w_read_all<6>(x, W); w_read_all<7>(x, W);
  }
  __builtin_amdgcn_sched_barrier(0);
  z1_tile(late, sm, 0);
  __builtin_amdgcn_sched_barrier(0);
  ld_z_step<1, 0>(h);
  __builtin_amdgcn_sched_barrier(0);
  z1_tile(late, sm, 1);
  __builtin_amdgcn_sched_barrier(0);
  ld_z_step<2, 0>(h);
  __builtin_amdgcn_sched_barrier(0);
  z1_tile(late, sm, 2); z1_tile(late, sm, 3);
  __builtin_amdgcn_sched_barrier(0);
  ld_z_step<3, 0>(h);
  __builtin_amdgcn_sched_barrier(0);
  u4 WO[7][5];
  g2_main<0, 0>(h, W, WO, st_);
  asm volatile("" : "+v"(late.t[0]), "+v"(late.t[1]), "+v"(late.t[2]));
  const int l2 = fresh_lane();
  const char* rd[3];
#pragma unroll
  for (int m = 1; m < 4; ++m) {
    h.slot[m - 1] = sb + (m < 3 ? KB_RED + w * 16384 + (m - 1) * KB_SLOT : hz + w * KB_SLOT) + l2 * 16;
    rd[m - 1] = sm + (m < 3 ? KB_RED + (w ^ m) * 16384 + (m - 1) * KB_SLOT : hz + (w ^ m) * KB_SLOT) + l2 * 16;
  }
  bar();
  g2_tail<0, 0>(h, W, WO);
  asm volatile("s_nop 7" : "+v"(h.acc[0][0]), "+v"(h.acc[1][0]), "+v"(h.acc[2][0]), "+v"(h.acc[3][0]), "+v"(h.acc[4][0]), "+v"(h.acc[5][0]), "+v"(h.acc[6][0]));
  bar();
  STK(0, 6);
  {
    pin(bias);
    const float bb = bflo(bias);
    const unsigned vl = l2 * 16;
    u2 vv[7], zz[7];
    f4 p[3][7];
#pragma unroll
    for (int fb = 0; fb < 7; ++fb) {
      p[0][fb] = *(const f4*)(rd[0] + fb * 1024); p[1][fb] = *(const f4*)(rd[1] + fb * 1024); p[2][fb] = *(const f4*)(rd[2] + fb * 1024);
      zz[fb] = *(const u2*)(sm + KB_Z1 + (w * 7 + fb) * 512 + l2 * 8);
    }
    __builtin_amdgcn_sched_barrier(0);
#pragma unroll
    for (int fb = 0; fb < 7; ++fb) {
      const f4 sum = (h.acc[fb][0] + p[0][fb]) + (p[1][fb] + p[2][fb]);
      const u2 z1 = zz[fb];
      const unsigned s0 = pk_bf(sum[0], sum[1]), s1 = pk_bf(sum[2], sum[3]);
      const unsigned g0 = pk_bf(bflo(s0) + bb, bfhi(s0) + bb), g1 = pk_bf(bflo(s1) + bb, bfhi(s1) + bb);
      vv[fb] = (u2){pk_bf(bflo(g0) * bflo(z1.x), bfhi(g0) * bfhi(z1.x)), pk_bf(bflo(g1) * bflo(z1.y), bfhi(g1) * bfhi(z1.y))};
    }
    const char* vb = vst + w * (NKS3 * 1024);
    if (gs & 1) {
      asm volatile("global_store_dwordx2 %0, %1, %2 offset:8 sc1" ::"v"(vl), "v"(vv[0]), "s"(vb) : "memory");
#pragma unroll
      for (int p = 0; p < 3; ++p) gst16s(vl + 1024 * (p + 1), cat(vv[2 * p + 1], vv[2 * p + 2]), vb);
    } else {
#pragma unroll
      for (int p = 0; p < 3; ++p) gst16s(vl + 1024 * p, cat(vv[2 * p], vv[2 * p + 1]), vb);
      if (cnt == 7) asm volatile("global_store_dwordx2 %0, %1, %2 offset:3072 sc1" ::"v"(vl), "v"(vv[6]), "s"(vb) : "memory");
    }
  }
  ld_wo_rest<WO_LATE>(h, WO);
  waitv<35 - WO_LATE>();
  if (last_of_four(sb + KB_CNT + 8)) {
    if (l2 == 0) __hip_atomic_store((unsigned*)(ws + OFF_F3) + (8 * i + j) * 32, 1u, __ATOMIC_RELAXED, __HIP_MEMORY_SCOPE_AGENT);
  }
  STK(0, 7);
  G3 o;
  const char* vb[4];
#pragma unroll
  for (int m = 0; m < 4; ++m) vb[m] = ws + OFF_V + ((size_t)(4 * i + (m ^ w)) * NKS3 + 7 * w) * 1024;
  unsigned pt[3];
  poll8(pt, (l2 & 7) * 128, ws + OFF_F3 + i * 1024);
  const unsigned vz6 = w == 3 ? h.vz - 1024 : h.vz;
  ld_v<0, V_PRE>(o, h.vz, vz6, vb);
  g3_loop<0>(o, WO, h.slot, h.vz, vz6, vb, st_);
  asm volatile("" : "+v"(pt[0]), "+v"(pt[1]), "+v"(pt[2]));
  asm volatile("s_nop 7" : "+v"(o.acc[0][0]), "+v"(o.acc[1][0]), "+v"(o.acc[2][0]), "+v"(o.acc[3][0]), "+v"(o.acc[4][0]));
  bar();
  const int c3 = l2 & 15, g3 = l2 >> 4;
  const int trow3 = 64 * i + 32 * (w >> 1) + 8 * (c3 >> 2) + 4 * (w & 1) + (c3 & 3);
  const char* hp = sm + (80 * j + 4 * g3 + (c3 >> 2)) * 128 + (((2 * w + ((c3 >> 1) & 1)) ^ (2 * (((c3 >> 3) & 1) | ((g3 >> 1) << 1)))) * 16) + 8 * (c3 & 1);
  u2 y[5];
  {
    f4 p[3][5]; u2 hv[5];
#pragma unroll
    for (int db = 0; db < 5; ++db) {
      p[0][db] = *(const f4*)(rd[0] + db * 1024); p[1][db] = *(const f4*)(rd[1] + db * 1024); p[2][db] = *(const f4*)(rd[2] + db * 1024);
      hv[db] = __builtin_bit_cast(u2, __builtin_amdgcn_ds_read_tr16_b64_v4i16((LDSP s4*)(hp + db * 2048)));
    }
    __builtin_amdgcn_sched_barrier(0);
#pragma unroll
    for (int db = 0; db < 5; ++db) {
      const f4 sum = (o.acc[db][0] + p[0][db]) + (p[1][db] + p[2][db]);
      const unsigned s0 = pk_bf(sum[0], sum[1]), s1 = pk_bf(sum[2], sum[3]);
      y[db] = (u2){pk_bf(bflo(hv[db].x) + bflo(s0), bfhi(hv[db].x) + bfhi(s0)), pk_bf(bflo(hv[db].y) + bflo(s1), bfhi(hv[db].y) + bfhi(s1))};
    }
  }
  const unsigned oo = (unsigned)trow3 * (2 * D) + 160 * j;
#pragma unroll
  for (int p = 0; p < 2; ++p) {
    const auto a = __builtin_amdgcn_permlane16_swap(y[2 * p].x, y[2 * p + 1].x, false, false), b = __builtin_amdgcn_permlane16_swap(y[2 * p].y, y[2 * p + 1].y, false, false);
    asm volatile("global_store_dwordx4 %0, %1, %2 offset:%3" OSF "\n\ts_nop 1" ::"v"(oo + 32 * (g3 & 1) + 16 * (g3 >> 1)), "v"((u4){a[0], b[0], a[1], b[1]}), "s"(out), "i"(64 * p) : "memory");
  }
  asm volatile("global_store_dwordx2 %0, %1, %2 offset:128" OSF ::"v"(oo + 8 * g3), "v"(y[4]), "s"(out) : "memory");
  ST_END(1);
}

extern "C" void kernel_launch(void* const* d_in, const int* in_sizes, int n_in, void* d_out, int out_size, void* d_ws, size_t ws_size,
                              hipStream_t stream) {
  char* ws = (char*)d_ws;
  ka<<<256, 1024, 0, stream>>>((const u16*)d_in[0], (const u16*)d_in[1], (const u16*)d_in[2], (const u16*)d_in[3], (const u16*)d_in[4],
                              (const unsigned*)d_in[5], (const unsigned*)d_in[6], (const u16*)d_in[8], (const u16*)d_in[9], (const u16*)d_in[11], ws);
  kb<<<256, 256, 0, stream>>>((const u16*)d_in[9], (const u16*)d_in[10], ws, (u16*)d_out);
}
```

```cpp
#include <hip/hip_runtime.h>

typedef unsigned short u16;
typedef unsigned long long u64;
typedef __attribute__((ext_vector_type(8))) __bf16 bfx8;
typedef __attribute__((ext_vector_type(2))) __bf16 bf2;
typedef __attribute__((ext_vector_type(8))) _Float16 h8;
typedef __attribute__((ext_vector_type(2))) _Float16 h2;
typedef __attribute__((ext_vector_type(4))) float f4;
typedef __attribute__((ext_vector_type(2))) float f2;
typedef __attribute__((ext_vector_type(4))) short s4;
typedef __attribute__((ext_vector_type(4))) unsigned u4;
typedef __attribute__((ext_vector_type(2))) unsigned u2;
#define LDSP __attribute__((address_space(3)))
#define DEVI __device__ __forceinline__

constexpr int L = 2048, D = 640, NM = 128, F = 1728, FH = 864;
constexpr int NGB = 54;
constexpr int NKS1 = 20;
constexpr int NKS3 = 27;

constexpr size_t OFF_UT = 0;
constexpr size_t OFF_XT = OFF_UT + (size_t)D * L * 2;
constexpr size_t OFF_KR = OFF_XT + (size_t)D * L * 2;
constexpr size_t OFF_HT = OFF_KR + (size_t)D * L * 4;
constexpr size_t OFF_WF = OFF_HT + (size_t)D * L * 2;
constexpr size_t OFF_WO = OFF_WF + (size_t)2 * NGB * NKS1 * 1024;
constexpr size_t OFF_Z1 = OFF_WO + (size_t)40 * NKS3 * 1024;
constexpr size_t OFF_Z2 = OFF_Z1 + (size_t)128 * NGB * 512;
constexpr size_t OFF_V = OFF_Z2 + (size_t)64 * NGB * 1024;
constexpr size_t OFF_ST = OFF_V + (size_t)128 * NKS3 * 1024;
constexpr size_t OFF_CSP = OFF_ST + (size_t)32 * 8 * 64 * 8;
constexpr size_t OFF_CS = OFF_CSP + (size_t)5 * F * 4;
constexpr size_t OFF_KSC = OFF_CS + (size_t)F * 4;
constexpr size_t OFF_G1 = 32u << 20;
constexpr size_t OFF_F1 = OFF_G1 + 32768;
constexpr size_t OFF_EP = OFF_G1 + 65536;
constexpr size_t OFF_F2 = OFF_G1 + 131072;
constexpr size_t OFF_F3 = OFF_F2 + 32768;
constexpr size_t OFF_ZE = OFF_F3 + 32768;

DEVI float bflo(unsigned v) { return __uint_as_float(v << 16); }
DEVI float bfhi(unsigned v) { return __uint_as_float(v & 0xffff0000u); }
DEVI unsigned pk_bf(float a, float b) { f2 p = {a, b}; return __builtin_bit_cast(unsigned, __builtin_convertvector(p, bf2)); }
DEVI unsigned pk_h(float a, float b) { f2 p = {a, b}; return __builtin_bit_cast(unsigned, __builtin_convertvector(p, h2)); }
DEVI float rbf(float a) { return bflo(pk_bf(a, 0.f)); }
DEVI float hlo(unsigned v) { return (float)__builtin_bit_cast(h2, v).x; }
DEVI float hhi(unsigned v) { return (float)__builtin_bit_cast(h2, v).y; }
DEVI f4 mfma_bf(u4 a, u4 b, f4 c) { return __builtin_amdgcn_mfma_f32_16x16x32_bf16(__builtin_bit_cast(bfx8, a), __builtin_bit_cast(bfx8, b), c, 0, 0, 0); }
DEVI f4 mfma_h(u4 a, u4 b, f4 c) { return __builtin_amdgcn_mfma_f32_16x16x32_f16(__builtin_bit_cast(h8, a), __builtin_bit_cast(h8, b), c, 0, 0, 0); }
template <int CTRL> DEVI float dpp(float v) { return __int_as_float(__builtin_amdgcn_update_dpp(0, __float_as_int(v), CTRL, 0xf, 0xf, true)); }
constexpr int DPP_ROR1 = 0x121, DPP_ROR2 = 0x122, DPP_ROR4 = 0x124, DPP_ROR8 = 0x128, DPP_MIRROR = 0x140, DPP_HMIRROR = 0x141, DPP_Q3210 = 0x1B, DPP_Q1032 = 0xB1;

DEVI f2 ln_stats(float s, float q, float inv_n) {
  float m = s * inv_n;
  float v = fmaxf(q * inv_n - m * m, 0.f);
  float sd = rbf(__builtin_amdgcn_sqrtf(rbf(rbf(v) + 1e-5f)));
  f2 r = {rbf(m), __builtin_amdgcn_rcpf(sd)};
  return r;
}
DEVI float gelu_t(float x) {
  float t = __builtin_fmaf(x * x, -0.1029432397f, -2.3022081985f);
  float e = __builtin_amdgcn_exp2f(x * t);
  return x * __builtin_amdgcn_rcpf(1.0f + e);
}

template <int IMM> DEVI void gld16(u4& d, unsigned voff, const void* sb) { asm volatile("global_load_dwordx4 %0, %1, %2 offset:%3" : "=v"(d) : "v"(voff), "s"(sb), "i"(IMM)); }
template <int IMM> DEVI void gld8(u2& d, unsigned voff, const void* sb) { asm volatile("global_load_dwordx2 %0, %1, %2 offset:%3" : "=v"(d) : "v"(voff), "s"(sb), "i"(IMM)); }
template <int IMM> DEVI void gld4(unsigned& d, unsigned voff, const void* sb) { asm volatile("global_load_dword %0, %1, %2 offset:%3" : "=v"(d) : "v"(voff), "s"(sb), "i"(IMM)); }
DEVI void gld2(unsigned& d, unsigned voff, const void* sb) { asm volatile("global_load_ushort %0, %1, %2" : "=v"(d) : "v"(voff), "s"(sb)); }
DEVI void dma16(unsigned ldsdst, unsigned voff, const void* sb) {
  asm volatile("s_mov_b32 m0, %0\n\ts_nop 0\n\tglobal_load_lds_dwordx4 %1, %2" ::"s"(ldsdst), "v"(voff), "s"(sb) : "memory");
}
template <int N> DEVI void waitv() { asm volatile("s_waitcnt vmcnt(%0)" ::"i"(N) : "memory"); }
template <int N> DEVI void waitv(u4& a) { asm volatile("s_waitcnt vmcnt(%1)" : "+v"(a) : "i"(N) : "memory"); }
DEVI void pin(u4& a) { asm volatile("" : "+v"(a)); }
DEVI void pin(u2& a) { asm volatile("" : "+v"(a)); }
DEVI void pin(unsigned& a) { asm volatile("" : "+v"(a)); }
DEVI void bar() { asm volatile("s_waitcnt lgkmcnt(0)\n\ts_barrier" ::: "memory"); }
DEVI unsigned lds_addr(const void* p) { return (unsigned)(size_t)(LDSP const char*)p; }

DEVI void tr8x8(const u4 (&r)[8], u4 (&o)[8]) {
#pragma unroll
  for (int c2 = 0; c2 < 4; ++c2)
#pragma unroll
    for (int m = 0; m < 4; ++m) {
      o[2 * c2][m] = __builtin_amdgcn_perm(r[2 * m + 1][c2], r[2 * m][c2], 0x05040100u);
      o[2 * c2 + 1][m] = __builtin_amdgcn_perm(r[2 * m + 1][c2], r[2 * m][c2], 0x07060302u);
    }
}

constexpr int KG_PITCH = 528;
constexpr int KC_PITCH = 4160;
constexpr int RC_SZ = 30208;
constexpr int KA_K = 3 * RC_SZ, KA_U = KA_K + 12 * KC_PITCH, U_PITCH = 4608, KA_T = KA_U + 3 * U_PITCH, T_PITCH = 1536;
constexpr int KA_S = KA_T + 3 * T_PITCH, KA_LDS = KA_S + 96;

struct c2 { f2 x, y; };
DEVI c2 cmul(c2 a, c2 b) { c2 r; r.x = a.x * b.x - a.y * b.y; r.y = a.x * b.y + a.y * b.x; return r; }
DEVI c2 cexpp(f2 a2, f2 br, float p) {
  const f2 ea = a2 * p, ph = br * p;
  const f2 e = {__builtin_amdgcn_exp2f(ea.x), __builtin_amdgcn_exp2f(ea.y)};
  const float p0 = __builtin_amdgcn_fractf(ph.x), p1 = __builtin_amdgcn_fractf(ph.y);
  c2 r;
  r.x = e * (f2){__builtin_amdgcn_cosf(p0), __builtin_amdgcn_cosf(p1)};
  r.y = e * (f2){__builtin_amdgcn_sinf(p0), __builtin_amdgcn_sinf(p1)};
  return r;
}
DEVI unsigned pk_h2(f2 p) { return __builtin_bit_cast(unsigned, __builtin_convertvector(p, h2)); }
template <int CTRL, int RM> DEVI float dppm(float v) { return __int_as_float(__builtin_amdgcn_update_dpp(0, __float_as_int(v), CTRL, RM, 0xf, false)); }
DEVI float wave_sum63(float v) {
  v += dpp<DPP_ROR8>(v); v += dpp<DPP_ROR4>(v); v += dpp<DPP_ROR2>(v); v += dpp<DPP_ROR1>(v);
  v += dppm<0x142, 0xa>(v); v += dppm<0x143, 0xc>(v);
  return v;
}
template <int IMM> DEVI void lds8(u2& d, unsigned a) { asm volatile("ds_read_b64 %0, %1 offset:%2" : "=v"(d) : "v"(a), "i"(IMM)); }
template <int IMM> DEVI void lds16(u4& d, unsigned a) { asm volatile("ds_read_b128 %0, %1 offset:%2" : "=v"(d) : "v"(a), "i"(IMM)); }
template <int N> DEVI void waitl(u2& a, u2& b, u4& c) { asm volatile("s_waitcnt lgkmcnt(%3)" : "+v"(a), "+v"(b), "+v"(c) : "i"(N) : "memory"); }
template <int IMM> DEVI void gld16s(u4& d, unsigned voff, const void* sb) { asm volatile("global_load_dwordx4 %0, %1, %2 offset:%3 sc1" : "=v"(d) : "v"(voff), "s"(sb), "i"(IMM)); }
DEVI void gld4s(unsigned& d, unsigned voff, const void* sb) { asm volatile("global_load_dword %0, %1, %2 sc1" : "=v"(d) : "v"(voff), "s"(sb)); }
DEVI unsigned perm(unsigned hi, unsigned lo, unsigned sel) { return __builtin_amdgcn_perm(hi, lo, sel); }

template <int RR, int P> DEVI void conv_ld(u2 (&al)[8], u2 (&ah)[8], u4 (&bb)[8], unsigned pa, unsigned pb) {
  if constexpr (P < 8) {
    lds8<64 * (1 - RR) + 512 * (7 - P)>(al[P], pa);
    lds8<64 * (1 - RR) + 512 * (7 - P) + 8>(ah[P], pa);
    lds16<64 * (1 - RR) + 512 * P>(bb[P], pb);
    conv_ld<RR, P + 1>(al, ah, bb, pa, pb);
  }
}
template <int S> DEVI void conv_mm(u2 (&al)[8], u2 (&ah)[8], u4 (&bb)[8], f4 (&acc)[8]) {
  if constexpr (S < 8) {
    waitl<(3 * (7 - S) > 15 ? 15 : 3 * (7 - S))>(al[S], ah[S], bb[S]);
#pragma unroll
    for (int p = 0; p <= S; ++p) acc[S] = mfma_h((u4){al[p].x, al[p].y, ah[p].x, ah[p].y}, bb[S - p], acc[S]);
    __builtin_amdgcn_sched_barrier(0);
    conv_mm<S + 1>(al, ah, bb, acc);
  }
}
template <int IMM> DEVI void gld8s(u2& d, unsigned voff, const void* sb) { asm volatile("global_load_dwordx2 %0, %1, %2 offset:%3 sc1" : "=v"(d) : "v"(voff), "s"(sb), "i"(IMM)); }
DEVI void gst16s(unsigned voff, u4 v, const void* sb) { asm volatile("global_store_dwordx4 %0, %1, %2 sc1\n\ts_nop 1" ::"v"(voff), "v"(v), "s"(sb) : "memory"); }
DEVI u4 tr2(const char* p, int step) {
  const u2 a = __builtin_bit_cast(u2, __builtin_amdgcn_ds_read_tr16_b64_v4i16((LDSP s4*)p));
  const u2 b = __builtin_bit_cast(u2, __builtin_amdgcn_ds_read_tr16_b64_v4i16((LDSP s4*)(p + step)));
  return (u4){a.x, a.y, b.x, b.y};
}
template <bool TWO, int K> DEVI void tbl_ld(u4 (&a)[8], u4 (&c)[8], u4 (&e)[8], unsigned pa, unsigned pb) {
  if constexpr (K < 8) {
    lds16<64 * K>(a[K], pa); lds16<64 * K>(c[K], pb);
    if constexpr (TWO) lds16<64 * K + 16 * KG_PITCH>(e[K], pb);
    tbl_ld<TWO, K + 1>(a, c, e, pa, pb);
  }
}
template <bool TWO, int K> DEVI void tbl_mm(u4 (&a)[8], u4 (&c)[8], u4 (&e)[8], f4& acc, f4& ac0) {
  if constexpr (K < 8) {
    constexpr int N = (TWO ? 3 : 2) * (7 - K);
    if constexpr (TWO) asm volatile("s_waitcnt lgkmcnt(%3)" : "+v"(a[K]), "+v"(c[K]), "+v"(e[K]) : "i"(N > 15 ? 15 : N) : "memory");
    else asm volatile("s_waitcnt lgkmcnt(%2)" : "+v"(a[K]), "+v"(c[K]) : "i"(N) : "memory");
    acc = mfma_h(a[K], c[K], acc);
    if constexpr (TWO) ac0 = mfma_h(e[K], c[K], ac0);
    __builtin_amdgcn_sched_barrier(0);
    tbl_mm<TWO, K + 1>(a, c, e, acc, ac0);
  }
}
template <bool TWO> DEVI void tbl_tiles(unsigned pa, unsigned pb, f4& acc, f4& ac0) {
  u4 a[8], c[8], e[8];
  tbl_ld<TWO, 0>(a, c, e, pa, pb);
  tbl_mm<TWO, 0>(a, c, e, acc, ac0);
}
DEVI void chsync(unsigned fl, int wq, unsigned phase) {
  asm volatile("s_waitcnt lgkmcnt(0)\n\tds_write_b32 %0, %1" ::"v"(fl + 4 * wq), "v"(phase) : "memory");
  for (;;) {
    u4 v;
    asm volatile("ds_read_b128 %0, %1\n\ts_waitcnt lgkmcnt(0)" : "=v"(v) : "v"(fl) : "memory");
    if (__builtin_amdgcn_readfirstlane(min(min(v.x, v.y), min(v.z, v.w))) >= phase) break;
    __builtin_amdgcn_s_sleep(1);
  }
}
DEVI void rawbar() { asm volatile("s_barrier" ::: "memory"); }
template <int I> DEVI void poll_ld(u4 (&g)[4], unsigned go, const char* base) {
  if constexpr (I < 4) { gld16s<16 * I>(g[I], go, base); poll_ld<I + 1>(g, go, base); }
}
DEVI void wait4(u4 (&g)[4]) { asm volatile("s_waitcnt vmcnt(0)" : "+v"(g[0]), "+v"(g[1]), "+v"(g[2]), "+v"(g[3])::"memory"); }

constexpr int KA_CODE_LINES = 118, KB_CODE_LINES = 290;
__global__ __launch_bounds__(1024, 4) void ka(const u16* __restrict__ x, const u16* __restrict__ logA, const u16* __restrict__ Aim,
                                          const u16* __restrict__ Cre, const u16* __restrict__ Cim, const unsigned* __restrict__ logdt,
                                          const unsigned* __restrict__ dskip, const u16* __restrict__ w_in, const u16* __restrict__ wsp,
                                          const u16* __restrict__ w_out, char* __restrict__ ws) {
  __shared__ __attribute__((aligned(16))) char sm[KA_LDS];
  const int tid = threadIdx.x, lane = tid & 63;
  const int w = __builtin_amdgcn_readfirstlane(tid >> 6);
  const int b = blockIdx.x;
  const int g = b & 7, m = b >> 3;
  const bool isconv = m < 27;
  const int d0 = 80 * g + (3 * m < 77 ? 3 * m : 77);
  if (tid < 16) ((int*)(sm + KA_S + 32))[tid] = 0;
  unsigned tag = 0;
  if (w < 4) {
    u4 r0, r1; u2 r2;
    unsigned ep;
    const int row = 256 * g + 8 * m + 2 * w + (lane >> 5), tm = 4 * g + (m >> 3), tg = m & 7;
    const unsigned ro = (unsigned)row * 1280 + (lane & 31) * 16;
    gld4s(ep, 0, ws + OFF_EP);
    gld16<0>(r0, ro, x); gld16<512>(r1, ro, x); gld8<1024>(r2, ro - (lane & 31) * 8, x);
    bar();
    {
      const char* tsrc = (const char*)x + (size_t)tm * 64 * 1280 + tg * 160;
      const unsigned dst = lds_addr(sm) + KA_U + w * 1024;
#pragma unroll
      for (int i = 0; i < 3; ++i) { const unsigned pc = tid + 256 * i < 640 ? tid + 256 * i : 639; dma16(dst + i * 4096, (pc / 10) * 1280 + (pc % 10) * 16, tsrc); }
    }
    __builtin_amdgcn_s_setprio(2);
    asm volatile("s_waitcnt vmcnt(3)" : "+v"(ep), "+v"(r0), "+v"(r1), "+v"(r2)::"memory");
    tag = ep + 0x9E3779B9u;
    f2 s0 = {bflo(r2.x), bfhi(r2.x)}, s1 = {bflo(r2.y), bfhi(r2.y)}, q0 = s0 * s0, q1 = s1 * s1;
#pragma unroll
    for (int c = 0; c < 4; ++c) {
      const unsigned v = r0[c], v2 = r1[c];
      const f2 a = {bflo(v), bfhi(v)}, a2 = {bflo(v2), bfhi(v2)};
      s0 += a; q0 += a * a; s1 += a2; q1 += a2 * a2;
    }
    s0 += s1; q0 += q1;
    float ss = s0.x + s0.y, qq = q0.x + q0.y;
    ss += dpp<DPP_ROR8>(ss); qq += dpp<DPP_ROR8>(qq); ss += dpp<DPP_ROR4>(ss); qq += dpp<DPP_ROR4>(qq);
    ss += dpp<DPP_ROR2>(ss); qq += dpp<DPP_ROR2>(qq); ss += dpp<DPP_ROR1>(ss); qq += dpp<DPP_ROR1>(qq);
    ss += dppm<0x142, 0xa>(ss); qq += dppm<0x142, 0xa>(qq);
    const float mean = ss * (1.0f / D);
    const float var = fmaxf(qq * (1.0f / D) - mean * mean, 0.f);
    unsigned pay = pk_bf(mean, __builtin_amdgcn_sqrtf(rbf(rbf(var) + 1e-5f)));
    pin(pay);
    u4 gv = {(unsigned)__builtin_amdgcn_readlane(pay, 31), tag, pay, tag};
    pin(gv);
    waitv<0>();
    if (lane == 63) gst16s((row - 1) * 8, gv, ws + OFF_G1);
    chsync(lds_addr(sm) + KA_S + 80, w, 1);
#pragma unroll 1
    for (int bl = w; bl < 5; bl += 4) {
      const int l = lane & 15, gm = lane >> 4;
      const char* tp = sm + KA_U + (8 * gm + (l >> 2)) * 160 + bl * 32 + (l & 3) * 8;
      const u4 A = tr2(tp, 640), B = tr2(tp + 32 * 160, 640);
      u4 s1v, s2v;
#pragma unroll
      for (int e = 0; e < 4; ++e) {
        const unsigned ae = A[e], be = B[e];
        s1v[e] = __builtin_amdgcn_update_dpp(ae, be, DPP_ROR8, 0xf, 0xc, false);
        s2v[e] = __builtin_amdgcn_update_dpp(be, ae, DPP_ROR8, 0xf, 0x3, false);
      }
      const unsigned vo = (unsigned)(80 * tg + 16 * bl + (l & 7)) * 4096 + tm * 128 + (gm + 4 * (l >> 3)) * 16;
      gst16s(vo, s1v, ws + OFF_XT);
      gst16s(vo + 8 * 4096, s2v, ws + OFF_XT);
    }
    waitv<0>();
    chsync(lds_addr(sm) + KA_S + 80, w, 2);
    if (tid == 0) __hip_atomic_store((unsigned*)(ws + OFF_F1) + (32 * tg + tm) * 32, tag, __ATOMIC_RELAXED, __HIP_MEMORY_SCOPE_AGENT);
    if (isconv) {
      u4 gr[4], xr[3];
      poll_ld<0>(gr, tid * 64, ws + OFF_G1);
      if (w == 0) {
#pragma unroll 1
        for (int spin = 0; spin < (1 << 16); ++spin) {
          unsigned v;
          asm volatile("global_load_dword %0, %1, %2 sc1\n\ts_waitcnt vmcnt(0)" : "=v"(v) : "v"((lane & 31) * 128), "s"(ws + OFF_F1 + g * 4096) : "memory");
          if (__all(v == tag)) break;
          __builtin_amdgcn_s_sleep(2);
        }
      }
      wait4(gr);
      chsync(lds_addr(sm) + KA_S + 80, w, 3);
      const char* xsrc = ws + OFF_XT + (size_t)d0 * 4096;
      gld16s<0>(xr[0], tid * 16, xsrc); gld16s<0>(xr[1], tid * 16, xsrc + 4096); gld16s<0>(xr[2], tid * 16, xsrc + 8192);
#pragma unroll 1
      for (int spin = 0; spin < (1 << 16); ++spin) {
        bool ok = true;
#pragma unroll
        for (int i = 0; i < 4; ++i) ok = ok && gr[i].y == tag && gr[i].w == tag;
        if (__all(ok)) break;
        poll_ld<0>(gr, tid * 64, ws + OFF_G1);
        wait4(gr);
      }
      f2 mm[4], rs[4];
#pragma unroll
      for (int i = 0; i < 4; ++i) { mm[i] = (f2){bflo(gr[i].x), bflo(gr[i].z)}; rs[i] = (f2){__builtin_amdgcn_rcpf(bfhi(gr[i].x)), __builtin_amdgcn_rcpf(bfhi(gr[i].z))}; }
      if (tid < 90) *(u4*)(sm + KA_U + (tid / 30) * U_PITCH + (tid % 30) * 16) = (u4){0, 0, 0, 0};
      asm volatile("s_waitcnt vmcnt(0)" : "+v"(xr[0]), "+v"(xr[1]), "+v"(xr[2])::"memory");
#pragma unroll
      for (int c = 0; c < 3; ++c) {
        u4 o;
#pragma unroll
        for (int i = 0; i < 4; ++i) { const unsigned v = xr[c][i]; o[i] = pk_h2(((f2){bflo(v), bfhi(v)} - mm[i]) * rs[i]); }
        *(u4*)(sm + KA_U + c * U_PITCH + 480 + tid * 16) = o;
      }
      __builtin_amdgcn_s_setprio(0);
      bar();
      if (b == 0 && tid == 0) __hip_atomic_store((unsigned*)(ws + OFF_EP), tag, __ATOMIC_RELAXED, __HIP_MEMORY_SCOPE_AGENT);
      __builtin_amdgcn_s_sleep(4);
      if (w != 0) {
        unsigned pw[4];
#pragma unroll
        for (int i = 0; i < 4; ++i) {
          const unsigned ln = (i * 81 + m * 3 + w - 1) * 64 + lane;
          gld4<0>(pw[i], (ln < 16384 ? ln : 16383) * 128, (const char*)wsp + (size_t)(g >> 1) * (16384 * 128));
        }
        if (m < 2) {
          const unsigned ln = (m * 3 + w - 1) * 64 + lane;
          const char* code;
          unsigned pc;
          asm volatile("s_getpc_b64 s[44:45]\n\ts_add_u32 s44, s44, _Z2kbPKtS0_PcPt@rel32@lo+4\n\ts_addc_u32 s45, s45, _Z2kbPKtS0_PcPt@rel32@hi+12\n\ts_mov_b64 %0, s[44:45]"
                       : "=s"(code)::"s44", "s45", "scc");
          gld4<0>(pc, (ln < KB_CODE_LINES ? ln : KB_CODE_LINES - 1) * 128, code);
          asm volatile("" : "+v"(pc), "+v"(pw[0]), "+v"(pw[1]), "+v"(pw[2]), "+v"(pw[3]));
        }
        asm volatile("" : "+v"(pw[0]), "+v"(pw[1]), "+v"(pw[2]), "+v"(pw[3]));
      } else {
      const int item = b * 40 + lane;
      if (lane < 40) {
        const int oc = item % 80, kq = item / 80, kap = kq >> 2, qq = kq & 3;
        u4 r[8], o[8];
#pragma unroll
        for (int e = 0; e < 8; ++e) r[e] = *(const u4*)(w_out + (size_t)(32 * kap + 16 * (e >> 2) + 4 * qq + (e & 3)) * D + oc * 8);
        tr8x8(r, o);
        u4* dst = (u4*)(ws + OFF_WO + ((size_t)(oc >> 1) * NKS3 + kap) * 1024) + qq * 16 + (oc & 1) * 8;
#pragma unroll
        for (int c = 0; c < 8; ++c) dst[c] = o[c];
      }
      }
      return;
    }
    __builtin_amdgcn_s_setprio(0);
  }
  if (!isconv) {
    unsigned pc = 0;
    if (w == 4) {
      const unsigned ln = (m - 27) * 64 + lane;
      const char* code;
      asm volatile("s_getpc_b64 s[44:45]\n\ts_add_u32 s44, s44, _Z2kaPKtS0_S0_S0_S0_PKjS2_S0_S0_S0_Pc@rel32@lo+4\n\t"
                   "s_addc_u32 s45, s45, _Z2kaPKtS0_S0_S0_S0_PKjS2_S0_S0_S0_Pc@rel32@hi+12\n\ts_mov_b64 %0, s[44:45]" : "=s"(code)::"s44", "s45", "scc");
      gld4<0>(pc, (ln < KA_CODE_LINES ? ln : KA_CODE_LINES - 1) * 128, code);
    }
    if (w >= 4) rawbar();
    rawbar();
    if (w == 4) asm volatile("s_waitcnt vmcnt(0)" : "+v"(pc)::"memory");
    const int si = b - 216;
    if (si < 27) {
      const int tt = tid - 384, co = tt & 7, ko = tt >> 3, c0 = 64 * si + 8 * co, k0 = 8 * ko;
      float* part = (float*)(sm + 98304);
      if (w >= 6) {
        u4 r[8], o[8];
#pragma unroll
        for (int e = 0; e < 8; ++e) r[e] = *(const u4*)(w_in + (size_t)(k0 + e) * F + c0);
        float cs[8];
#pragma unroll
        for (int c = 0; c < 4; ++c) {
          f2 s = {0.f, 0.f};
#pragma unroll
          for (int e = 0; e < 8; ++e) { const unsigned v = r[e][c]; s += (f2){bflo(v), bfhi(v)}; }
          cs[2 * c] = s.x + dpp<DPP_ROR8>(s.x); cs[2 * c + 1] = s.y + dpp<DPP_ROR8>(s.y);
        }
        if (!(lane & 8)) {
          f4* p = (f4*)(part + ((w - 6) * 4 + (lane >> 4)) * 64 + co * 8);
          p[0] = (f4){cs[0], cs[1], cs[2], cs[3]}; p[1] = (f4){cs[4], cs[5], cs[6], cs[7]};
        }
        tr8x8(r, o);
#pragma unroll
        for (int c = 0; c < 8; ++c) *(u4*)(sm + tt * 144 + c * 16) = o[c];
      }
      __syncthreads();
      if (w < 8) {
        float s = 0.f;
#pragma unroll
        for (int e = 0; e < 5; ++e) s += part[(w * 5 + e) * 64 + lane];
        part[2560 + w * 64 + lane] = s;
      }
#pragma unroll
      for (int i = 0; i < 5; ++i) {
        const int f = w * 5 + i, blk = f / 20, ks = f % 20, gc = 4 * si + blk;
        const u4 v = *(const u4*)(sm + ((4 * ks + (lane >> 4)) * 8 + 2 * blk + ((lane >> 3) & 1)) * 144 + (lane & 7) * 16);
        *(u4*)(ws + OFF_WF + ((size_t)(gc >= NGB ? gc - NGB : gc + NGB) * NKS1 + ks) * 1024 + lane * 16) = v;
      }
      __syncthreads();
      if (w == 0) {
        float s = 0.f;
#pragma unroll
        for (int e = 0; e < 8; ++e) s += part[2560 + e * 64 + lane];
        ((float*)(ws + OFF_CS))[64 * si + lane] = s;
      }
    } else if (si < 35) {
      ((u4*)(ws + OFF_ST))[(si - 27) * 1024 + tid] = (u4){0, 0, 0, 0};
    } else if (si == 35) {
      if (tid < 576) *(u4*)(ws + OFF_F2 + (tid < 512 ? tid * 128 : 65536 + (tid - 512) * 16)) = (u4){0, 0, 0, 0};
    }
    return;
  }
  const int ch = w < 4 ? 0 : (w >> 2) - 1, role = (w + ch) & 3;
  const int d = d0 + ch;
  const int col = lane & 15, q = lane >> 4;
  char* rc = sm + ch * RC_SZ;
  float* slot = (float*)(sm + KA_S) + 2 * ch;
  const unsigned fl = lds_addr(sm) + KA_S + 32 + 16 * ch;
  if (w >= 4) {
    unsigned la, ai, cr, ci, ldw, dw;
    gld4<0>(ldw, 0, logdt + (d >> 1));
    gld4<0>(dw, 0, dskip + (d >> 1));
    gld4<0>(la, lane * 4, logA + d * NM);
    gld4<0>(ai, lane * 4, Aim + d * NM);
    gld4<0>(cr, lane * 4, Cre + d * NM);
    gld4<0>(ci, lane * 4, Cim + d * NM);
    rawbar();
    asm volatile("s_waitcnt vmcnt(0)" : "+v"(la), "+v"(ai), "+v"(cr), "+v"(ci), "+v"(ldw), "+v"(dw)::"memory");
    const float dt = rbf(__expf((d & 1) ? bfhi(ldw) : bflo(ldw)));
    const unsigned ea = pk_bf(__expf(bflo(la)), __expf(bfhi(la)));
    const f2 a2 = (f2){bflo(ea), bfhi(ea)} * (-1.4426950408889634f * dt);
    f2 br = (f2){bflo(ai), bfhi(ai)} * (dt * 0.15915494309189535f);
    char* base = rc + lane * 8;
    auto put = [&](int row, c2 v) { *(u2*)(base + row * KG_PITCH) = (u2){pk_h2(v.x), pk_h2(v.y)}; };
    if (role == 0) {
      c2 v = {{bflo(cr), bfhi(cr)}, {bflo(ci), bfhi(ci)}};
      float mx = fmaxf(fmaxf(fabsf(v.x.x), fabsf(v.x.y)), fmaxf(fabsf(v.y.x), fabsf(v.y.y)));
      mx = fmaxf(mx, dpp<DPP_ROR8>(mx)); mx = fmaxf(mx, dpp<DPP_ROR4>(mx)); mx = fmaxf(mx, dpp<DPP_ROR2>(mx)); mx = fmaxf(mx, dpp<DPP_ROR1>(mx));
      mx = fmaxf(mx, dppm<0x142, 0xa>(mx)); mx = fmaxf(mx, dppm<0x143, 0xc>(mx));
      mx = __int_as_float(__builtin_amdgcn_readlane(__float_as_int(mx), 63));
      const float dsk = (d & 1) ? bfhi(dw) : bflo(dw);
      const float sc = fmaxf(fmaxf(2.0f * dt * mx, fabsf(dsk) * (1.0f / 16384.0f)), 1e-30f);
      const float isc = 1.0f / sc;
      if (lane == 0) { slot[0] = sc; slot[1] = dsk * isc; }
      v.x *= 2.0f * dt * isc; v.y *= 2.0f * dt * isc;
      const c2 s = cexpp(a2, br, 8.f);
#pragma unroll
      for (int j = 0; j < 16; ++j) { put(j, v); if (j < 15) v = cmul(v, s); }
    } else {
      br = -br;
      const c2 h = cexpp(a2, br, 128.f);
      if (role == 1) {
        const c2 s = cexpp(a2, br, 1.f);
        put(16, (c2){{1.f, 1.f}, {0.f, 0.f}}); put(24, h);
        c2 v = s;
#pragma unroll
        for (int c = 1; c < 8; ++c) { put(16 + c, v); put(24 + c, cmul(v, h)); if (c < 7) v = cmul(v, s); }
      } else if (role == 2) {
        const c2 s = cexpp(a2, br, 2.f);
        c2 v = cmul(h, h);
        const c2 q = cmul(s, s), y = cmul(v, v), z = cmul(y, h);
        put(40, y); put(41, cmul(y, q)); put(42, z); put(43, cmul(z, q));
#pragma unroll
        for (int c = 0; c < 4; ++c) { put(32 + c, v); put(36 + c, cmul(v, h)); if (c < 3) v = cmul(v, s); }
      } else {
        const c2 q = cexpp(a2, br, 4.f);
        c2 v = cexpp(a2, br, 768.f);
        put(44, v); put(45, cmul(v, q)); v = cmul(v, h);
        put(46, v); put(47, cmul(v, q));
#pragma unroll
        for (int i = 0; i < 9; ++i) { v = cmul(v, h); put(48 + i, v); }
      }
    }
  chsync(fl, w & 3, 1);
  char* tb = sm + KA_T + ch * T_PITCH;
  {
    const int wq = w & 3;
    const unsigned pb = lds_addr(rc) + col * KG_PITCH + q * 16;
    f4 acc = {0.f, 0.f, 0.f, 0.f}, ac0 = {0.f, 0.f, 0.f, 0.f};
    const float dadd = slot[1];
    if (wq == 3) tbl_tiles<true>(pb + 32 * KG_PITCH, pb, acc, ac0); else tbl_tiles<false>(pb + (wq < 2 ? 48 : 40) * KG_PITCH, pb, acc, ac0);
    const unsigned p0 = pk_h(acc[0], acc[1]), p1 = pk_h(acc[2], acc[3]);
    if (wq < 2) {
      u16* t = (u16*)tb + 512 + 64 * q + col;
      t[0] = (u16)p0; t[16] = (u16)(p0 >> 16); t[32] = (u16)p1; t[48] = (u16)(p1 >> 16);
    } else if (wq == 2) {
      if (q < 2) { *(unsigned*)(tb + 2 * (384 + 64 * q + 2 * col)) = p0; *(unsigned*)(tb + 2 * (416 + 64 * q + 2 * col)) = p1; }
      else if (lane == 32) ((u16*)tb)[512] = (u16)p0;
    } else {
      if (q < 2) *(u2*)(tb + 2 * (256 + 64 * q + 4 * col)) = (u2){p0, p1};
      else if (lane == 32) ((u16*)tb)[384] = (u16)p0;
      if (lane == 0) ac0[0] += dadd;
      *(u2*)(tb + 2 * (128 * (q >> 1) + 4 * (q & 1) + 8 * col)) = (u2){pk_h(ac0[0], ac0[1]), pk_h(ac0[2], ac0[3])};
    }
  }
  asm volatile("s_waitcnt lgkmcnt(0)" ::: "memory");
  {
    const int t8 = tid & 255, a = 255 - t8, wq = w & 3;
    u4 o0, o1;
    if (wq < 2) {
      const u16* t = (const u16*)tb + 384 + a;
      const unsigned lo = t[0], hi = t[1];
      const unsigned hh = perm(hi, hi, 0x01000100u), ll = perm(lo, lo, 0x01000100u), tie = (a & 1) ? hi : lo;
      o0 = (u4){hh, perm(tie, hi, 0x05040100u), ll, ll}; o1 = (u4){hh, perm(lo, tie, 0x05040100u), ll, ll};
    } else if (wq == 2) {
      const unsigned* t = (const unsigned*)(tb + 2 * (256 + 2 * a));
      const unsigned v01 = t[0], v2 = t[1];
      const unsigned t11 = perm(v01, v01, 0x03020302u), t00 = perm(v01, v01, 0x01000100u);
      o0 = (u4){perm(v2, v2, 0x01000100u), t11, perm(v01, v01, 0x01000302u), t00}; o1 = (u4){perm(v01, v2, 0x07060100u), t11, t00, t00};
    } else if (a >= 32) {
      const unsigned* t = (const unsigned*)(tb + 2 * (128 + 4 * a));
      const unsigned vm = t[-1], v01 = t[0], v23 = t[1], v4 = t[2];
      const unsigned t22 = perm(v23, v23, 0x01000100u);
      o0 = (u4){perm(v23, v4, 0x07060100u), t22, perm(v01, v23, 0x07060100u), perm(v01, v01, 0x01000100u)};
      o1 = (u4){perm(v23, v23, 0x01000302u), t22, perm(v01, v01, 0x01000302u), perm(vm, v01, a == 32 ? 0x07060100u : 0x01000100u)};
    } else {
      const u4 v = *(const u4*)(tb + 16 * a);
      const unsigned vm = a ? *(const unsigned*)(tb + 16 * a - 4) : 0u;
      o0 = (u4){perm(v.w, v.w, 0x01000302u), perm(v.z, v.z, 0x01000302u), perm(v.y, v.y, 0x01000302u), perm(v.x, v.x, 0x01000302u)};
      o1 = (u4){perm(v.z, v.w, 0x07060100u), perm(v.y, v.z, 0x07060100u), perm(v.x, v.y, 0x07060100u), perm(vm, v.x, 0x07060100u)};
    }
    char* kcb = sm + KA_K + ch * 4 * KC_PITCH;
    char* kc = kcb + 16 * t8;
    *(u4*)kc = o0; *(u4*)(kc + KC_PITCH) = o1;
    unsigned* k2 = (unsigned*)(kc + 2 * KC_PITCH - 4);
    unsigned* k3 = (unsigned*)(kc + 3 * KC_PITCH - 4);
    k2[0] = o0.x; k2[1] = o0.y; k2[2] = o0.z; k2[3] = o0.w;
    k3[0] = o1.x; k3[1] = o1.y; k3[2] = o1.z; k3[3] = o1.w;
    if (wq == 0 && lane < 36) { const int s = lane / 9; *(unsigned*)(kcb + s * KC_PITCH + 4096 - (s >> 1) * 4 + (lane - 9 * s) * 4) = 0; }
  }
  }
  bar();
  const int wq = w & 3;
  u2 xv[2];
  gld8s<0>(xv[0], (512 * wq + 16 * col + 4 * q) * 2, ws + OFF_XT + (size_t)d * 4096);
  gld8s<512>(xv[1], (512 * wq + 16 * col + 4 * q) * 2, ws + OFF_XT + (size_t)d * 4096);
  f4 acc[8];
#pragma unroll
  for (int n = 0; n < 8; ++n) acc[n] = (f4){0.f, 0.f, 0.f, 0.f};
  {
    const int s = (3 - col) & 3;
    const unsigned pa = lds_addr(sm) + KA_K + (4 * ch + s) * KC_PITCH + 2 * (2031 - s - col + 8 * q) - 64 * (2 * wq + 1) - 512 * 7;
    const unsigned pb = lds_addr(sm) + KA_U + ch * U_PITCH + 448 + 32 * col + 16 * q - 64 * (2 * wq + 1);
    u2 al[8], ah[8]; u4 bb[8];
    conv_ld<0, 0>(al, ah, bb, pa, pb);
    conv_mm<0>(al, ah, bb, acc);
    __builtin_amdgcn_sched_barrier(0);
    conv_ld<1, 0>(al, ah, bb, pa, pb);
    conv_mm<0>(al, ah, bb, acc);
  }
  f4 tot[2];
  {
    f4* red = (f4*)(rc + wq * 6144) + lane;
#pragma unroll
    for (int v = 0; v < 4; ++v) {
      if (v == wq) { tot[0] = acc[2 * v]; tot[1] = acc[2 * v + 1]; }
      else { const int sl = v > wq ? v - 1 : v; red[sl * 128] = acc[2 * v]; red[sl * 128 + 64] = acc[2 * v + 1]; }
    }
  }
  chsync(fl, w & 3, 2);
#pragma unroll
  for (int v = 0; v < 3; ++v) {
    const int src = v < wq ? v : v + 1;
    const f4* rp = (const f4*)(rc + src * 6144) + (wq > src ? wq - 1 : wq) * 128 + lane;
    tot[0] += rp[0]; tot[1] += rp[64];
  }
  const float sc = slot[0];
  asm volatile("s_waitcnt vmcnt(0)" : "+v"(xv[0]), "+v"(xv[1])::"memory");
#pragma unroll
  for (int nn = 0; nn < 2; ++nn) {
    const int t = 512 * wq + 256 * nn + 16 * col + 4 * q;
    const int pos = (t & ~31) + 16 * ((t >> 2) & 1) + 4 * ((t >> 3) & 3);
    const float h[4] = {__builtin_fmaf(tot[nn][0], sc, bflo(xv[nn].x)), __builtin_fmaf(tot[nn][1], sc, bfhi(xv[nn].x)),
                        __builtin_fmaf(tot[nn][2], sc, bflo(xv[nn].y)), __builtin_fmaf(tot[nn][3], sc, bfhi(xv[nn].y))};
    *(u2*)(ws + OFF_HT + (size_t)(pos >> 6) * 81920 + d * 128 + (pos & 63) * 2) = (u2){pk_bf(h[0], h[1]), pk_bf(h[2], h[3])};
  }
}

DEVI void tile_of(int b, int& i, int& j) { const int g = b & 7, r = b >> 3; i = (g >> 1) * 8 + (r >> 2); j = (g & 1) * 4 + (r & 3); }
constexpr int KB_RED = 81920, KB_SLOT = 7168, KB_Z1 = KB_RED + 65536, KB_S2 = KB_Z1 + 14336, KB_S3 = KB_S2 + 512,
              KB_CNT = KB_S3 + 512, KB_LDS = KB_CNT + 16;
constexpr int KSPLIT = 10;
constexpr int KFLAG = 15;
constexpr int WO_LATE = 25;
#define POLL_GAP "4"

template <bool Z> DEVI void mmv(f4& d, const u4& a, const u4& b) {
  if constexpr (Z) asm volatile("v_mfma_f32_16x16x32_bf16 %0, %1, %2, 0" : "=v"(d) : "v"(a), "v"(b));
  else asm volatile("v_mfma_f32_16x16x32_bf16 %0, %1, %2, %0" : "+v"(d) : "v"(a), "v"(b));
}
template <bool Z> DEVI void mmva(f4& d, const u4& a, const u4& b) {
  if constexpr (Z) asm volatile("v_mfma_f32_16x16x32_bf16 %0, %1, %2, 0" : "=v"(d) : "v"(a), "a"(b));
  else asm volatile("v_mfma_f32_16x16x32_bf16 %0, %1, %2, %0" : "+v"(d) : "v"(a), "a"(b));
}
template <bool Z> DEVI void mmav(f4& d, const u4& a, const u4& b) {
  if constexpr (Z) asm volatile("v_mfma_f32_16x16x32_bf16 %0, %1, %2, 0" : "=v"(d) : "a"(a), "v"(b));
  else asm volatile("v_mfma_f32_16x16x32_bf16 %0, %1, %2, %0" : "+v"(d) : "a"(a), "v"(b));
}
template <int IMM> DEVI void gld16a(u4& d, unsigned voff, const void* sb) { asm volatile("global_load_dwordx4 %0, %1, %2 offset:%3" : "=a"(d) : "v"(voff), "s"(sb), "i"(IMM)); }
template <int IMM> DEVI void rld16(u4& d, unsigned voff, const void* sb) { asm volatile("global_load_dwordx4 %0, %1, %2 offset:%3" : "+v"(d) : "v"(voff), "s"(sb), "i"(IMM)); }
template <int IMM> DEVI void rld16s(u4& d, unsigned voff, const void* sb) { asm volatile("global_load_dwordx4 %0, %1, %2 offset:%3 sc1" : "+v"(d) : "v"(voff), "s"(sb), "i"(IMM)); }
template <int IMM> DEVI void trd(u2& d, unsigned a) { asm volatile("ds_read_b64_tr_b16 %0, %1 offset:%2" : "=v"(d) : "v"(a), "i"(IMM)); }
template <int IMM> DEVI void ldsw16(unsigned a, const f4& v) { asm volatile("ds_write_b128 %0, %1 offset:%2" ::"v"(a), "v"(v), "i"(IMM) : "memory"); }
DEVI void settle(f4& a, f4& b, f4& c, f4& d) { asm volatile("s_nop 7" : "+v"(a), "+v"(b), "+v"(c), "+v"(d)); }
DEVI void hold(f4& a, f4& b, f4& c, f4& d) { asm volatile("" : "+v"(a), "+v"(b), "+v"(c), "+v"(d)); }
DEVI u4 cat(const u2& a, const u2& b) { return (u4){a.x, a.y, b.x, b.y}; }
DEVI f2 gelu2(f2 x) {
  const f2 u = x * (x * x * -0.1029432397f + -2.3022081985f);
  const f2 d = (f2){__builtin_amdgcn_exp2f(u.x), __builtin_amdgcn_exp2f(u.y)} + 1.0f;
  return x * (f2){__builtin_amdgcn_rcpf(d.x), __builtin_amdgcn_rcpf(d.y)};
}
DEVI int fresh_lane() {
  int l;
  asm volatile("v_mbcnt_lo_u32_b32 %0, -1, 0\n\tv_mbcnt_hi_u32_b32 %0, -1, %0" : "=v"(l));
  return l;
}
DEVI bool last_of_four(unsigned cnt_addr) {
  unsigned o = 0;
  if (fresh_lane() == 0) asm volatile("ds_add_rtn_u32 %0, %1, %2\n\ts_waitcnt lgkmcnt(0)" : "=v"(o) : "v"(cnt_addr), "v"(1u) : "memory");
  return __builtin_amdgcn_readfirstlane(o) == 3;
}
#define POLL1(R) "s_waitcnt vmcnt(2)\n\tv_cmp_ne_u32 vcc, 0, " R "\n\ts_cmp_eq_u64 vcc, -1\n\ts_cbranch_scc1 2f\n\tglobal_load_dword " R ", %4, %5 sc1\n\t"
DEVI void poll8(unsigned (&t)[3], unsigned voff, const void* sb) {
  unsigned n;
  asm volatile(
      "s_mov_b32 %3, 0x20000\n\tglobal_load_dword %0, %4, %5 sc1\n\ts_sleep " POLL_GAP "\n\tglobal_load_dword %1, %4, %5 sc1\n\ts_sleep " POLL_GAP "\n\t"
      "global_load_dword %2, %4, %5 sc1\n1:\n\t" POLL1("%0") POLL1("%1") POLL1("%2") "s_sub_u32 %3, %3, 1\n\ts_cbranch_scc0 1b\n2:"
      : "=&v"(t[0]), "=&v"(t[1]), "=&v"(t[2]), "=&s"(n) : "v"(voff), "s"(sb) : "vcc", "scc", "memory");
}

struct G1 {
  u2 al[2][4], ah[2][4];
  u4 ring[8][2], ones;
  unsigned touch[4], va[2], sb;
  const char* hsrc;
  f4 acc[2][4], sx, sq;
  unsigned pa[4], vb[2];
  const char *b2, *b1;
};
struct WS { unsigned vw[2], rd[2], ring; const char* src[4]; };
template <int C, int M> DEVI void w_dma(const WS& x) {
  asm volatile("s_mov_b32 m0, %0\n\ts_nop 0\n\tglobal_load_lds_dwordx4 %1, %3\n\tglobal_load_lds_dwordx4 %2, %3 offset:1024"
               ::"s"(x.ring + (C & 1) * 8192 + M * 2048), "v"(x.vw[0]), "v"(x.vw[1]), "s"(x.src[M] + C * 128) : "memory");
}
DEVI void w_dma_rt(const WS& x, int C, int M) {
  asm volatile("s_mov_b32 m0, %0\n\ts_nop 0\n\tglobal_load_lds_dwordx4 %1, %3\n\tglobal_load_lds_dwordx4 %2, %3 offset:1024"
               ::"s"(x.ring + (C & 1) * 8192 + M * 2048), "v"(x.vw[0]), "v"(x.vw[1]), "s"(x.src[M] + C * 128) : "memory");
}
template <int C, int M, int KH> DEVI void w_read(const WS& x, u4 (&W)[16][4]) {
  asm volatile("ds_read_b128 %0, %1 offset:%2" : "=a"(W[2 * C + KH][M]) : "v"(x.rd[KH]), "i"((C & 1) * 8192 + M * 2048));
}
template <int C> DEVI void w_dma_all(const WS& x) { w_dma<C, 0>(x); w_dma<C, 1>(x); w_dma<C, 2>(x); w_dma<C, 3>(x); }
template <int C> DEVI void w_read_all(const WS& x, u4 (&W)[16][4]) {
  w_read<C, 0, 0>(x, W); w_read<C, 0, 1>(x, W); w_read<C, 1, 0>(x, W); w_read<C, 1, 1>(x, W);
  w_read<C, 2, 0>(x, W); w_read<C, 2, 1>(x, W); w_read<C, 3, 0>(x, W); w_read<C, 3, 1>(x, W);
}
DEVI void w_setup(WS& x, int lane, int w, int i, unsigned sb, const u16* wsp) {
  const int c = lane & 15, g4 = lane >> 4;
  x.ring = sb + KB_RED + w * 16384;
#pragma unroll
  for (int o = 0; o < 2; ++o) {
    x.vw[o] = (unsigned)(16 * o + 8 * (lane >> 5) + ((lane >> 3) & 3)) * 4096 + (((lane & 7) ^ ((4 * o + g4) & 7)) * 16) - 1024 * o;
    x.rd[o] = x.ring + c * 128 + (((4 * o + g4) ^ ((c >> 1) & 7)) * 16);
  }
#pragma unroll
  for (int m = 0; m < 4; ++m) x.src[m] = (const char*)wsp + (size_t)(64 * i + 32 * ((m ^ w) >> 1) + 4 * ((m ^ w) & 1)) * 4096 + w * 1024;
}
template <int KS> DEVI const char* wf_base(const char* b) { return b + (KS >> 3) * 8192 + 4096; }
template <int KS, int X> DEVI void ld_new(G1& g, u4& d, const char* b) { gld16<(KS & 7) * 1024 - 4096>(d, g.vb[X], wf_base<KS>(b)); }
template <int KS, int X> DEVI void ld_ring(G1& g, u4& d, const char* b) { rld16<(KS & 7) * 1024 - 4096>(d, g.vb[X], wf_base<KS>(b)); }
template <int S, int M> DEVI void rd_lo(G1& g) { trd<(S & 15) * 4096>(g.al[S & 1][M], g.pa[M]); }
template <int S, int M> DEVI void rd_hi(G1& g) { trd<(S & 15) * 4096 + 512>(g.ah[S & 1][M], g.pa[M]); }
template <int N, int LG> DEVI void g1_wait(G1& g, int c, int m) {
  asm volatile("s_waitcnt vmcnt(%4) lgkmcnt(%5)" : "+v"(g.al[c][m]), "+v"(g.ah[c][m]), "+v"(g.al[c][m + 1]), "+v"(g.ah[c][m + 1]) : "i"(N), "i"(LG) : "memory");
}
template <int S> DEVI void pa_step(G1& g) {
  if constexpr (S == 16) { for (int m = 0; m < 4; ++m) g.pa[m] += 65536; }
  if constexpr (S == 20) { for (int m = 0; m < 4; ++m) g.pa[m] -= 65536; }
}
template <int KS, int H> DEVI void a_dma(const G1& g) {
  asm volatile("s_mov_b32 m0, %0\n\ts_nop 0\n\tglobal_load_lds_dwordx4 %1, %3\n\tglobal_load_lds_dwordx4 %2, %3 offset:1024"
               ::"s"(g.sb + KS * 4096 + H * 2048), "v"(g.va[0]), "v"(g.va[1]), "s"(g.hsrc + KS * 4096 + H * 2048) : "memory");
}
template <int G, int X> DEVI void b_next(G1& g) {
  if constexpr (G + 8 < 20) ld_ring<G + 8, X>(g, g.ring[G & 7][X], g.b2);
  else if constexpr (G + 8 < 40) ld_ring<G + 8 - 20, X>(g, g.ring[G & 7][X], g.b1);
}
constexpr bool w_rd_at(int s) { return (s >= 0 && s < 8) || (s >= 10 && s < 18); }
constexpr bool w_dma_at(int s) { return w_rd_at(s - 1); }
constexpr int w_tile(int s) { return s < 8 ? s : s - 2; }
constexpr int ops_at(int nb, int k) { return (k < 32 ? nb : 0) + (nb == 1 && k < 10 ? 4 : 0) + (nb == 1 && k == 20 ? 3 : 0) + (w_dma_at(k - 20) ? 2 : 0); }
constexpr int ops_in(int nb, int a, int b) { int n = 0; for (int k = a; k < b; ++k) n += ops_at(nb, k); return n; }
constexpr int imin(int a, int b) { return a < b ? a : b; }
constexpr int g1_vm(int nb, int G) {
  int n = G < 4 ? nb * (7 - G) + 6 + ops_in(nb, 0, G) : G < 8 ? nb * (7 - G) + ops_in(nb, 0, G) : ops_in(nb, G - 7, G);
  if (G == 3) n = imin(n, 4 * nb + ops_in(nb, 0, 3));
  if (nb == 1 && G == 9) n = imin(n, 1 + ops_in(1, 5, 9));
  if (nb == 1 && G == 14) n = imin(n, 1 + ops_in(1, 10, 14));
  if (G >= 20 && G < 28) n = 2 * (27 - G) + ops_in(nb, 20, G);
  if (G >= 30 && G < 38) n = imin(n, (G - 9 < 32 ? nb - 1 : 0) + ops_in(nb, G - 8, G));
  return imin(n, 63);
}
template <int NB, int S> DEVI void g1_first(G1& g) {
  if constexpr (S < 20) {
    constexpr int c = S & 1, nx = (S + 1) % 20;
    constexpr bool Z = S == 0;
    g1_wait<g1_vm(NB, S), 4>(g, c, 0);
    if constexpr (S == 3 || S == 9 || S == 14) asm volatile("s_barrier" ::: "memory");
    pa_step<S + 1>(g);
    const u4& b0 = g.ring[S & 7][0]; const u4& b1 = g.ring[S & 7][1];
    const u4 a0 = cat(g.al[c][0], g.ah[c][0]), a1 = cat(g.al[c][1], g.ah[c][1]);
    mmv<Z>(g.acc[0][0], a0, b0); rd_lo<nx, 0>(g);
    if constexpr (NB == 2) mmv<Z>(g.acc[1][0], a0, b1);
    rd_hi<nx, 0>(g);
    mmv<Z>(g.sx, a0, g.ones);
    if constexpr (NB == 1 && S < 10) a_dma<S + 10, 0>(g);
    mmv<Z>(g.acc[0][1], a1, b0); rd_lo<nx, 1>(g);
    if constexpr (NB == 2) mmv<Z>(g.acc[1][1], a1, b1);
    rd_hi<nx, 1>(g);
    mmv<Z>(g.sq, a0, a0);
    if constexpr (NB == 1 && S < 10) a_dma<S + 10, 1>(g);
    g1_wait<63, 4>(g, c, 2);
    const u4 a2 = cat(g.al[c][2], g.ah[c][2]), a3 = cat(g.al[c][3], g.ah[c][3]);
    mmv<Z>(g.acc[0][2], a2, b0); rd_lo<nx, 2>(g);
    if constexpr (NB == 2) mmv<Z>(g.acc[1][2], a2, b1);
    rd_hi<nx, 2>(g);
    mmv<Z>(g.acc[0][3], a3, b0); rd_lo<nx, 3>(g);
    b_next<S, 0>(g);
    if constexpr (NB == 2) mmv<Z>(g.acc[1][3], a3, b1);
    rd_hi<nx, 3>(g);
    if constexpr (NB == 2) b_next<S, 1>(g);
    g1_first<NB, S + 1>(g);
  }
}
template <int NB, int S, int X, int M> DEVI void mm1(G1& g) {
  if constexpr (X < NB) mmv<S == 0>(g.acc[X][M], g.ring[(S + 20) & 7][X], cat(g.al[S & 1][M], g.ah[S & 1][M]));
}
template <int NB, int S, int E> DEVI void g1_second(G1& g, const WS& x, u4 (&W)[16][4], unsigned tv, const char* tb) {
  if constexpr (S < E) {
    constexpr bool R = S < 19, WR = w_rd_at(S), WP = w_rd_at(S - 1);
    constexpr int T = w_tile(S), TD = w_tile(S - 1) + 8;
    g1_wait<g1_vm(NB, S + 20), (WP ? 5 : 4)>(g, S & 1, 0);
    if constexpr (R) pa_step<S + 1>(g);
    if constexpr (NB == 1 && S == 0) { gld4<0>(g.touch[0], tv, tb); gld4<0>(g.touch[1], tv, tb + 45 * 128); gld4<0>(g.touch[2], tv, tb + 90 * 128); }
    mm1<NB, S, 0, 0>(g); if constexpr (R) rd_lo<S + 1, 0>(g);
    mm1<NB, S, 1, 0>(g); if constexpr (R) rd_hi<S + 1, 0>(g);
    if constexpr (WR) w_read<(T >> 2), (T & 3), 0>(x, W);
    mm1<NB, S, 0, 1>(g); if constexpr (R) rd_lo<S + 1, 1>(g);
    mm1<NB, S, 1, 1>(g); if constexpr (R) rd_hi<S + 1, 1>(g);
    g1_wait<63, (R ? (WR ? 5 : 4) : 0)>(g, S & 1, 2);
    mm1<NB, S, 0, 2>(g); if constexpr (R) rd_lo<S + 1, 2>(g);
    mm1<NB, S, 1, 2>(g); if constexpr (R) rd_hi<S + 1, 2>(g);
    if constexpr (WR) w_read<(T >> 2), (T & 3), 1>(x, W);
    mm1<NB, S, 0, 3>(g); if constexpr (R) rd_lo<S + 1, 3>(g);
    b_next<S + 20, 0>(g);
    if constexpr (w_dma_at(S)) w_dma<(TD >> 2), (TD & 3)>(x);
    mm1<NB, S, 1, 3>(g); if constexpr (R) rd_hi<S + 1, 3>(g);
    if constexpr (NB == 2) b_next<S + 20, 1>(g);
    g1_second<NB, S + 1, E>(g, x, W, tv, tb);
  }
}
template <int NB, int KS, int E> DEVI void g1_pro(G1& g) {
  if constexpr (KS < E) { ld_new<KS, 0>(g, g.ring[KS][0], g.b2); if constexpr (NB == 2) ld_new<KS, 1>(g, g.ring[KS][1], g.b2); g1_pro<NB, KS + 1, E>(g); }
}
#define FOLD2(CT, MA, MB, H) \
  "v_add_f32_dpp %0, %0, %0 " CT " row_mask:0xf bank_mask:" MA "\n\tv_add_f32_dpp %1, %1, %1 " CT " row_mask:0xf bank_mask:" MA "\n\t" \
  "v_add_f32_dpp %2, %2, %2 " CT " row_mask:0xf bank_mask:" MA "\n\tv_add_f32_dpp %3, %3, %3 " CT " row_mask:0xf bank_mask:" MA "\n\t" \
  "v_add_f32_dpp %0, %4, %4 " CT " row_mask:0xf bank_mask:" MB "\n\tv_add_f32_dpp %1, %5, %5 " CT " row_mask:0xf bank_mask:" MB "\n\t" \
  "v_add_f32_dpp %2, %6, %6 " CT " row_mask:0xf bank_mask:" MB "\n\tv_add_f32_dpp %3, %7, %7 " CT " row_mask:0xf bank_mask:" MB
DEVI void fold16(float (&v)[16]) {
  asm volatile("s_nop 1\n\t" FOLD2("row_mirror", "0x3", "0xc", 8) : "+v"(v[0]), "+v"(v[1]), "+v"(v[2]), "+v"(v[3]) : "v"(v[8]), "v"(v[9]), "v"(v[10]), "v"(v[11]));
  asm volatile("s_nop 1\n\t" FOLD2("row_mirror", "0x3", "0xc", 8) : "+v"(v[4]), "+v"(v[5]), "+v"(v[6]), "+v"(v[7]) : "v"(v[12]), "v"(v[13]), "v"(v[14]), "v"(v[15]));
  asm volatile(FOLD2("row_half_mirror", "0x5", "0xa", 4) "\n\t"
               "v_add_f32_dpp %0, %0, %0 quad_perm:[2,3,0,1] row_mask:0xf bank_mask:0xf\n\tv_add_f32_dpp %1, %1, %1 quad_perm:[2,3,0,1] row_mask:0xf bank_mask:0xf\n\t"
               "v_add_f32_dpp %2, %2, %2 quad_perm:[2,3,0,1] row_mask:0xf bank_mask:0xf\n\tv_add_f32_dpp %3, %3, %3 quad_perm:[2,3,0,1] row_mask:0xf bank_mask:0xf\n\t"
               "v_add_f32_dpp %0, %0, %0 quad_perm:[1,0,3,2] row_mask:0xf bank_mask:0xf\n\tv_add_f32_dpp %1, %1, %1 quad_perm:[1,0,3,2] row_mask:0xf bank_mask:0xf\n\t"
               "v_add_f32_dpp %2, %2, %2 quad_perm:[1,0,3,2] row_mask:0xf bank_mask:0xf\n\tv_add_f32_dpp %3, %3, %3 quad_perm:[1,0,3,2] row_mask:0xf bank_mask:0xf"
               : "+v"(v[0]), "+v"(v[1]), "+v"(v[2]), "+v"(v[3]) : "v"(v[4]), "v"(v[5]), "v"(v[6]), "v"(v[7]));
}

struct G2 {
  u4 z[4][7];
  f4 acc[7][4];
  unsigned vz, vz6, slot[3];
  const char *zsrc, *wo, *wo6;
};
template <int S, int FB> DEVI void ld_z(G2& g) {
  const char* sb = g.zsrc + (size_t)S * (NGB * 1024);
  if constexpr (S < 4) gld16s<(FB - 3) * 1024>(g.z[S][FB], FB == 6 ? g.vz6 : g.vz, sb);
  else rld16s<(FB - 3) * 1024>(g.z[S & 3][FB], FB == 6 ? g.vz6 : g.vz, sb);
}
template <int S, int FB> DEVI void ld_z_step(G2& g) { ld_z<S, FB>(g); if constexpr (FB < 6) ld_z_step<S, FB + 1>(g); }
template <int N> DEVI void ld_wo(G2& g, u4 (&WO)[7][5]) {
  constexpr int KS = N / 5, DB = N % 5;
  if constexpr (KS < 6) gld16a<(KS - 3) * 1024>(WO[KS][DB], g.vz, g.wo + DB * (NKS3 * 1024));
  else gld16a<0>(WO[KS][DB], g.vz, g.wo6 == nullptr ? g.wo + DB * (NKS3 * 1024) + 3072 : g.wo6);
}
template <int S, int FB> DEVI void g2_main(G2& g, u4 (&W)[16][4], u4 (&WO)[7][5]) {
  if constexpr (S < 14) {
    if constexpr (S == 12 && FB == 0) asm volatile("s_waitcnt lgkmcnt(0)" ::: "memory");
    waitv<27>();
    mmva<S == 0>(g.acc[FB][0], g.z[S & 3][FB], W[S][0]); mmva<S == 0>(g.acc[FB][1], g.z[S & 3][FB], W[S][1]);
    mmva<S == 0>(g.acc[FB][2], g.z[S & 3][FB], W[S][2]); mmva<S == 0>(g.acc[FB][3], g.z[S & 3][FB], W[S][3]);
    if constexpr (S < 12) ld_z<S + 4, FB>(g); else ld_wo<7 * (S - 12) + FB>(g, WO);
    if constexpr (FB < 6) g2_main<S, FB + 1>(g, W, WO); else g2_main<S + 1, 0>(g, W, WO);
  }
}
template <int GI, int K> DEVI void g2_tail(G2& g, u4 (&W)[16][4], u4 (&WO)[7][5]) {
  if constexpr (GI < 4) {
    constexpr int m = (GI + 1) & 3, s = 14 + K / 7, fb = K % 7;
    if constexpr (GI == 0 && K == 0) waitv<21>();
    if constexpr (GI == 0 && K == 7) waitv<14>();
    mmva<false>(g.acc[fb][m], g.z[s & 3][fb], W[s][m]);
    if constexpr (GI > 0 && K >= 1 && K < 8) ldsw16<(K - 1) * 1024>(g.slot[GI - 1], g.acc[K - 1][GI]);
    if constexpr (K >= 8 && 14 + 6 * GI + K - 8 < WO_LATE) ld_wo<14 + 6 * GI + K - 8>(g, WO);
    if constexpr (K < 13) g2_tail<GI, K + 1>(g, W, WO); else g2_tail<GI + 1, 0>(g, W, WO);
  }
}
template <int N> DEVI void ld_wo_rest(G2& g, u4 (&WO)[7][5]) { if constexpr (N < 35) { ld_wo<N>(g, WO); ld_wo_rest<N + 1>(g, WO); } }
struct G3 { u4 v[4][7]; f4 acc[5][4]; };
constexpr int V_PRE = 14;
template <int N, int E> DEVI void ld_v(G3& g, unsigned vo, unsigned vo6, const char* const (&vb)[4]) {
  if constexpr (N < E && N < 28) { gld16s<(N % 7 - 3) * 1024>(g.v[N / 7][N % 7], N % 7 == 6 ? vo6 : vo, vb[(N / 7 + 1) & 3]); ld_v<N + 1, E>(g, vo, vo6, vb); }
}
template <int N> DEVI void g3_loop(G3& g, u4 (&WO)[7][5], const unsigned (&slot)[3], unsigned vo, unsigned vo6, const char* const (&vb)[4]) {
  if constexpr (N < 28) {
    constexpr int GI = N / 7, s = N % 7, m = (GI + 1) & 3;
    waitv<imin(V_PRE + 2 * N, 28) - 1 - N>();
    mmav<s == 0>(g.acc[0][m], WO[s][0], g.v[GI][s]); mmav<s == 0>(g.acc[1][m], WO[s][1], g.v[GI][s]);
    ld_v<V_PRE + 2 * N, V_PRE + 2 * N + 1>(g, vo, vo6, vb);
    mmav<s == 0>(g.acc[2][m], WO[s][2], g.v[GI][s]); mmav<s == 0>(g.acc[3][m], WO[s][3], g.v[GI][s]);
    ld_v<V_PRE + 2 * N + 1, V_PRE + 2 * N + 2>(g, vo, vo6, vb);
    mmav<s == 0>(g.acc[4][m], WO[s][4], g.v[GI][s]);
    if constexpr (GI > 0 && s >= 1 && s < 6) ldsw16<(s - 1) * 1024>(slot[GI - 1], g.acc[s - 1][GI]);
    g3_loop<N + 1>(g, WO, slot, vo, vo6, vb);
  }
}

struct Late { f4 acc[4]; float r[4], n[4]; unsigned o[4], rd[2], t[3]; u4 cs; };
DEVI void z1_tile(const Late& t, char* sm, int m) {
  const f2 c0 = {__uint_as_float(t.cs.x), __uint_as_float(t.cs.y)}, c1 = {__uint_as_float(t.cs.z), __uint_as_float(t.cs.w)};
  const f2 y0 = gelu2((f2){t.acc[m][0], t.acc[m][1]} * t.r[m] + c0 * t.n[m]), y1 = gelu2((f2){t.acc[m][2], t.acc[m][3]} * t.r[m] + c1 * t.n[m]);
  *(u2*)(sm + t.o[m]) = (u2){pk_bf(y0.x, y0.y), pk_bf(y1.x, y1.y)};
}
template <int NB> DEVI void phase1(char* sm, char* ws, const int i, const int j, const int w, const int gs, const int cnt, u4 (&W)[16][4], Late& late, const u16* wsp, u64 ta, u64 tb_) {
  const int lane = fresh_lane();
  const int c = lane & 15, g4 = lane >> 4;
  const bool liveA = 2 * w < cnt;
  const int ga = gs + (liveA ? 2 * w : cnt - 1), gb = ga + 1;
  const unsigned sb = lds_addr(sm);
  G1 g;
  g.vb[0] = (unsigned)ga * (NKS1 * 1024) + lane * 16; g.vb[1] = g.vb[0] + NKS1 * 1024;
  g.b2 = ws + OFF_WF; g.b1 = ws + OFF_WF + (size_t)NGB * NKS1 * 1024;
  unsigned cs2[2]; u4 cs1[2];
  {
    const int r8 = lane >> 3, sg = (r8 >> 1) & 1;
    g.va[0] = r8 * 128 + (((lane & 7) ^ (2 * sg)) * 16); g.va[1] = r8 * 128 + (((lane & 7) ^ (2 * sg + 4)) * 16);
    g.sb = sb; g.hsrc = ws + OFF_HT + (size_t)i * 81920;
    const unsigned vo = w & 1 ? g.va[1] : g.va[0];
#pragma unroll
    for (int n = 0; n < 4; ++n) dma16(sb + (w + 4 * n) * 1024, vo, g.hsrc + (w + 4 * n) * 1024);
#pragma unroll
    for (int x = 0; x < NB; ++x) { gld4<0>(cs2[x], (FH + 16 * (ga + x) + c) * 4, ws + OFF_CS); gld16<0>(cs1[x], (16 * (ga + x) + 4 * g4) * 4, ws + OFF_CS); }
    g1_pro<NB, 0, 4>(g);
#pragma unroll
    for (int n = 4; n < 10; ++n) dma16(sb + (w + 4 * n) * 1024, vo, g.hsrc + (w + 4 * n) * 1024);
    g1_pro<NB, 4, 8>(g);
    if constexpr (NB == 1) {
      ta += ((lane >> 3) & 3) * (NKS3 * 1024) + (lane & 7) * 128; tb_ += (lane & 31) * (4 * D);
      const u64 tp = tb_ + ((ta - tb_) & (0ull - (lane < 32)));
      asm volatile("global_load_dword %0, %1, off sc1" : "=v"(g.touch[3]) : "v"(tp));
    }
  }
  WS x;
  w_setup(x, lane, w, i, sb, wsp);
  {
    const int qq = c >> 2, p = c & 3, sh = (qq >> 1) | ((g4 & 1) << 1);
#pragma unroll
    for (int m = 0; m < 4; ++m) {
      g.pa[m] = sb + (8 * g4 + qq) * 128 + (p >> 1) * 16 + 8 * (p & 1) + (m ^ w ^ sh) * 32;
    }
  }
  g.ones = (u4){0x3f803f80u, 0x3f803f80u, 0x3f803f80u, 0x3f803f80u};
  unsigned aq[4], al[4], zo[2], z1o[4], po, so;
#pragma unroll
  for (int m = 0; m < 4; ++m) { aq[m] = KB_S2 + (m ^ w) * 64 + g4 * 16; al[m] = KB_S2 + (m ^ w) * 64 + c * 4; z1o[m] = KB_Z1 + ((m ^ w) * 7 + 2 * w) * 512 + lane * 8; }
  zo[0] = (unsigned)(w >> 1) * (NGB * 1024) + lane * 16; zo[1] = (unsigned)(1 ^ (w >> 1)) * (NGB * 1024) + lane * 16;
  po = KB_Z1 + (w * 64 + 16 * ((c >> 2) ^ w) + 4 * g4) * 4; so = KB_S2 + (16 * w + c) * 4;
  const char* zb = ws + OFF_Z2 + ((size_t)2 * i * NGB + ga) * 1024;
  u64* st = (u64*)(ws + OFF_ST) + (size_t)i * 512;
  asm volatile("" : "+v"(aq[0]), "+v"(aq[1]), "+v"(aq[2]), "+v"(aq[3]), "+v"(al[0]), "+v"(al[1]), "+v"(al[2]), "+v"(al[3]), "+v"(zo[0]), "+v"(zo[1]),
               "+v"(z1o[0]), "+v"(z1o[1]), "+v"(z1o[2]), "+v"(z1o[3]), "+v"(po), "+v"(so), "+v"(x.vw[0]), "+v"(x.vw[1]), "+v"(x.rd[0]), "+v"(x.rd[1]));
  asm volatile("" : "+s"(x.src[0]), "+s"(x.src[1]), "+s"(x.src[2]), "+s"(x.src[3]), "+s"(x.ring), "+s"(zb), "+s"(st));
  if (w == 0 && lane < 4) *(unsigned*)(sm + KB_CNT + 4 * lane) = 0;
  if constexpr (NB == 2) asm volatile("s_waitcnt vmcnt(22)" : "+v"(cs2[0]), "+v"(cs2[1]), "+v"(cs1[0]), "+v"(cs1[1]), "+v"(g.ones)::"memory");
  else asm volatile("s_waitcnt vmcnt(14)" : "+v"(cs2[0]), "+v"(cs1[0]), "+v"(g.ones)::"memory");
  bar();
  rd_lo<0, 0>(g); rd_hi<0, 0>(g); rd_lo<0, 1>(g); rd_hi<0, 1>(g); rd_lo<0, 2>(g); rd_hi<0, 2>(g); rd_lo<0, 3>(g); rd_hi<0, 3>(g);
  g1_first<NB, 0>(g);
  asm volatile("s_nop 1" : "+v"(g.sx), "+v"(g.sq));
  {
    const float sx = c & 2 ? (c & 1 ? g.sx[3] : g.sx[2]) : (c & 1 ? g.sx[1] : g.sx[0]);
    const float sq = c & 2 ? (c & 1 ? g.sq[3] : g.sq[2]) : (c & 1 ? g.sq[1] : g.sq[0]);
    const f2 st = ln_stats(sx, sq, 1.0f / D);
    if ((c >> 2) == g4) { *(float*)(sm + so) = st.y; *(float*)(sm + so + 256) = -st.x * st.y; }
  }
  bar();
  hold(g.acc[0][0], g.acc[0][1], g.acc[0][2], g.acc[0][3]);
  if constexpr (NB == 2) hold(g.acc[1][0], g.acc[1][1], g.acc[1][2], g.acc[1][3]);
  f2 z[2][8];
  float r1[4], n1[4];
  float* ps = (float*)(sm + KB_Z1); float* pq = ps + 256;
  {
    float sv[16], qv[16];
    f4 r4[4], n4[4];
#pragma unroll
    for (int m = 0; m < 4; ++m) { r4[m] = *(const f4*)(sm + aq[m]); n4[m] = *(const f4*)(sm + aq[m] + 256); }
#pragma unroll
    for (int m = 0; m < 4; ++m) { r1[m] = *(const float*)(sm + al[m]); n1[m] = *(const float*)(sm + al[m] + 256); }
#pragma unroll
    for (int m = 0; m < 4; ++m) {
#pragma unroll
      for (int e = 0; e < 2; ++e) {
        const f2 r = {r4[m][2 * e], r4[m][2 * e + 1]}, n = {n4[m][2 * e], n4[m][2 * e + 1]};
#pragma unroll
        for (int y = 0; y < NB; ++y) z[y][2 * m + e] = gelu2((f2){g.acc[y][m][2 * e], g.acc[y][m][2 * e + 1]} * r + n * __uint_as_float(cs2[y]));
        f2 s2 = z[0][2 * m + e], q2 = s2 * s2;
        if constexpr (NB == 2) { s2 += z[1][2 * m + e]; q2 += z[1][2 * m + e] * z[1][2 * m + e]; }
        sv[4 * m + 2 * e] = s2.x; sv[4 * m + 2 * e + 1] = s2.y; qv[4 * m + 2 * e] = q2.x; qv[4 * m + 2 * e + 1] = q2.y;
        __builtin_amdgcn_sched_barrier(0);
        w_dma_rt(x, (2 * m + e) >> 2, (2 * m + e) & 3);
        __builtin_amdgcn_sched_barrier(0);
      }
    }
    fold16(sv); fold16(qv);
    if ((c & 3) == 0) {
      *(f4*)(sm + po) = liveA ? (f4){sv[0], sv[1], sv[2], sv[3]} : (f4){0.f, 0.f, 0.f, 0.f};
      *(f4*)(sm + po + 1024) = liveA ? (f4){qv[0], qv[1], qv[2], qv[3]} : (f4){0.f, 0.f, 0.f, 0.f};
    }
    if (last_of_four(sb + KB_CNT)) {
      const float ts = (ps[lane] + ps[64 + lane]) + (ps[128 + lane] + ps[192 + lane]), tq = (pq[lane] + pq[64 + lane]) + (pq[128 + lane] + pq[192 + lane]);
      __hip_atomic_store(st + j * 64 + lane, ((u64)(__float_as_uint(tq) | 0x80000000u) << 32) | __float_as_uint(ts), __ATOMIC_RELAXED, __HIP_MEMORY_SCOPE_AGENT);
    }
  }
  asm volatile("" : "+v"(r1[0]), "+v"(r1[1]), "+v"(r1[2]), "+v"(r1[3]), "+v"(n1[0]), "+v"(n1[1]), "+v"(n1[2]), "+v"(n1[3]));
  const unsigned tv = (lane < 45 ? lane : 44) * 128;
  const char* tb = ws + OFF_WO + (size_t)5 * j * NKS3 * 1024 + (i & 7) * 135 * 128;
  g1_second<NB, 0, KSPLIT>(g, x, W, tv, tb);
  if constexpr (NB == 1) asm volatile("" : "+v"(g.touch[0]), "+v"(g.touch[1]), "+v"(g.touch[2]), "+v"(g.touch[3]));
  float* rs3 = (float*)(sm + KB_S3); float* nm3 = rs3 + 64;
  if constexpr (NB == 1) {
    u2 gv[8]; unsigned n;
    asm volatile(
        "s_mov_b32 %8, 0x40000\n"
        "1:\n\tglobal_load_dwordx2 %0, %9, %10 sc1\n\tglobal_load_dwordx2 %1, %9, %10 offset:512 sc1\n\tglobal_load_dwordx2 %2, %9, %10 offset:1024 sc1\n\t"
        "global_load_dwordx2 %3, %9, %10 offset:1536 sc1\n\tglobal_load_dwordx2 %4, %9, %10 offset:2048 sc1\n\tglobal_load_dwordx2 %5, %9, %10 offset:2560 sc1\n\t"
        "global_load_dwordx2 %6, %9, %10 offset:3072 sc1\n\tglobal_load_dwordx2 %7, %9, %10 offset:3584 sc1\n\ts_waitcnt vmcnt(0)\n\t"
        "v_cmp_gt_i64 vcc, 0, %0\n\tv_cmp_gt_i64 s[44:45], 0, %1\n\tv_cmp_gt_i64 s[46:47], 0, %2\n\tv_cmp_gt_i64 s[48:49], 0, %3\n\t"
        "s_and_b64 vcc, vcc, s[44:45]\n\ts_and_b64 s[46:47], s[46:47], s[48:49]\n\ts_and_b64 s[50:51], vcc, s[46:47]\n\t"
        "v_cmp_gt_i64 vcc, 0, %4\n\tv_cmp_gt_i64 s[44:45], 0, %5\n\tv_cmp_gt_i64 s[46:47], 0, %6\n\tv_cmp_gt_i64 s[48:49], 0, %7\n\t"
        "s_and_b64 vcc, vcc, s[44:45]\n\ts_and_b64 s[46:47], s[46:47], s[48:49]\n\ts_and_b64 vcc, vcc, s[46:47]\n\ts_and_b64 vcc, vcc, s[50:51]\n\t"
        "s_sub_u32 %8, %8, 1\n\ts_cbranch_scc1 2f\n\ts_cmp_eq_u64 vcc, -1\n\ts_cbranch_scc1 2f\n\ts_sleep 1\n\ts_branch 1b\n2:"
        : "=&v"(gv[0]), "=&v"(gv[1]), "=&v"(gv[2]), "=&v"(gv[3]), "=&v"(gv[4]), "=&v"(gv[5]), "=&v"(gv[6]), "=&v"(gv[7]), "=&s"(n)
        : "v"(lane * 8), "s"(st)
        : "vcc", "scc", "s44", "s45", "s46", "s47", "s48", "s49", "s50", "s51", "memory");
    float ts = 0.f, tq = 0.f;
#pragma unroll
    for (int v = 0; v < 8; ++v) { ts += __uint_as_float(gv[v].x); tq += __uint_as_float(gv[v].y & 0x7fffffffu); }
    const f2 s3 = ln_stats(ts, tq, 1.0f / FH);
    rs3[lane] = s3.y; nm3[lane] = -s3.x * s3.y;
  }
  bar();
  if (liveA) {
    unsigned o[2][8];
#pragma unroll
    for (int m = 0; m < 4; ++m) {
      const f4 r4 = *(const f4*)(sm + aq[m] + 512), n4 = *(const f4*)(sm + aq[m] + 768);
#pragma unroll
      for (int e = 0; e < 2; ++e) {
        const f2 r = {r4[2 * e], r4[2 * e + 1]}, n = {n4[2 * e], n4[2 * e + 1]};
#pragma unroll
        for (int x = 0; x < NB; ++x) { const f2 a = z[x][2 * m + e] * r + n; o[x][2 * m + e] = pk_bf(a.x, a.y); }
      }
    }
    const unsigned r0 = zo[0], r1 = zo[1];
    if (w & 1) {
#pragma unroll
      for (int x = 0; x < NB; ++x) { gst16s(r0, (u4){o[x][2], o[x][3], o[x][0], o[x][1]}, zb + x * 1024); gst16s(r1, (u4){o[x][6], o[x][7], o[x][4], o[x][5]}, zb + x * 1024); }
    } else {
#pragma unroll
      for (int x = 0; x < NB; ++x) { gst16s(r0, (u4){o[x][0], o[x][1], o[x][2], o[x][3]}, zb + x * 1024); gst16s(r1, (u4){o[x][4], o[x][5], o[x][6], o[x][7]}, zb + x * 1024); }
    }
  }
  __builtin_amdgcn_sched_barrier(0);
  g1_second<NB, KSPLIT, KFLAG>(g, x, W, tv, tb);
  waitv<ops_in(NB, KSPLIT + 20, KFLAG + 20)>();
  if (last_of_four(sb + KB_CNT + 4)) {
    if (lane == 0) __hip_atomic_store((unsigned*)(ws + OFF_F2) + (32 * j + i) * 32, 1u, __ATOMIC_RELAXED, __HIP_MEMORY_SCOPE_AGENT);
  }
  g1_second<NB, KFLAG, 20>(g, x, W, tv, tb);
  settle(g.acc[0][0], g.acc[0][1], g.acc[0][2], g.acc[0][3]);
#pragma unroll
  for (int m = 0; m < 4; ++m) { late.acc[m] = g.acc[0][m]; late.r[m] = r1[m]; late.n[m] = n1[m]; late.o[m] = z1o[m]; }
  late.cs = cs1[0];
  if constexpr (NB == 2) {
#pragma unroll
    for (int m = 0; m < 4; ++m) z1_tile(late, sm, m);
    hold(g.acc[1][0], g.acc[1][1], g.acc[1][2], g.acc[1][3]);
#pragma unroll
    for (int m = 0; m < 4; ++m) { late.acc[m] = g.acc[1][m]; late.o[m] += 512; }
    late.cs = cs1[1];
  }
  __builtin_amdgcn_sched_barrier(0);
  waitv<0>();
  w_read_all<4>(x, W); w_read_all<5>(x, W);
  asm volatile("s_waitcnt lgkmcnt(8)" ::: "memory");
  w_dma_all<6>(x);
  asm volatile("s_waitcnt lgkmcnt(0)" ::: "memory");
  w_dma_all<7>(x);
  late.rd[0] = x.rd[0]; late.rd[1] = x.rd[1];
  poll8(late.t, (lane & 7) * 128, ws + OFF_F2 + (32 * j + 8 * w) * 128);
}

__global__ __launch_bounds__(256, 1) void kb(const u16* __restrict__ wsp, const u16* __restrict__ bsp, char* __restrict__ ws, u16* __restrict__ out) {
  __shared__ __attribute__((aligned(16))) char sm[KB_LDS];
  const int w = __builtin_amdgcn_readfirstlane(threadIdx.x >> 6);
  int i, j; tile_of(blockIdx.x, i, j);
  const int gs = j < 7 ? 7 * j : 48, cnt = j < 6 ? 7 : 6;
  const unsigned sb = lds_addr(sm);
  const int hz = j < 4 ? 40960 : 0;
  u4 W[16][4];
  Late late;
  const char* vst = ws + OFF_V + ((size_t)4 * i * NKS3 + (gs >> 1)) * 1024;
  u64 ta = (u64)vst + (0x15 >> j & 1) * 3072, tb = (u64)(out + 64 * i * D + 80 * j);
  asm volatile("" : "+s"(ta), "+s"(tb));
  if (w < 3) phase1<2>(sm, ws, i, j, w, gs, cnt, W, late, wsp, ta, tb); else phase1<1>(sm, ws, i, j, w, gs, cnt, W, late, wsp, ta, tb);
  G2 h;
  unsigned bias;
  {
    const int ln = fresh_lane(), c = ln & 15;
    h.vz = ln * 16 + 3072; h.vz6 = cnt == 7 ? h.vz : h.vz - 1024;
    gld2(bias, (64 * i + 32 * (w >> 1) + 8 * (c >> 2) + 4 * (w & 1) + (c & 3)) * 2, bsp);
  }
  h.zsrc = ws + OFF_Z2 + ((size_t)16 * w * NGB + gs) * 1024;
  h.wo = ws + OFF_WO + ((size_t)5 * j * NKS3 + 7 * w) * 1024;
  h.wo6 = w == 3 ? ws + OFF_ZE - 3072 : nullptr;
  ld_z_step<0, 0>(h);
  {
    WS x;
    x.rd[0] = late.rd[0]; x.rd[1] = late.rd[1];
    w_read_all<6>(x, W); w_read_all<7>(x, W);
  }
  __builtin_amdgcn_sched_barrier(0);
  z1_tile(late, sm, 0);
  __builtin_amdgcn_sched_barrier(0);
  ld_z_step<1, 0>(h);
  __builtin_amdgcn_sched_barrier(0);
  z1_tile(late, sm, 1);
  __builtin_amdgcn_sched_barrier(0);
  ld_z_step<2, 0>(h);
  __builtin_amdgcn_sched_barrier(0);
  z1_tile(late, sm, 2); z1_tile(late, sm, 3);
  __builtin_amdgcn_sched_barrier(0);
  ld_z_step<3, 0>(h);
  __builtin_amdgcn_sched_barrier(0);
  u4 WO[7][5];
  g2_main<0, 0>(h, W, WO);
  asm volatile("" : "+v"(late.t[0]), "+v"(late.t[1]), "+v"(late.t[2]));
  const int l2 = fresh_lane();
  const char* rd[3];
#pragma unroll
  for (int m = 1; m < 4; ++m) {
    h.slot[m - 1] = sb + (m < 3 ? KB_RED + w * 16384 + (m - 1) * KB_SLOT : hz + w * KB_SLOT) + l2 * 16;
    rd[m - 1] = sm + (m < 3 ? KB_RED + (w ^ m) * 16384 + (m - 1) * KB_SLOT : hz + (w ^ m) * KB_SLOT) + l2 * 16;
  }
  bar();
  g2_tail<0, 0>(h, W, WO);
  asm volatile("s_nop 7" : "+v"(h.acc[0][0]), "+v"(h.acc[1][0]), "+v"(h.acc[2][0]), "+v"(h.acc[3][0]), "+v"(h.acc[4][0]), "+v"(h.acc[5][0]), "+v"(h.acc[6][0]));
  bar();
  {
    pin(bias);
    const float bb = bflo(bias);
    const unsigned vl = l2 * 16;
    u2 vv[7], zz[7];
    f4 p[3][7];
#pragma unroll
    for (int fb = 0; fb < 7; ++fb) {
      p[0][fb] = *(const f4*)(rd[0] + fb * 1024); p[1][fb] = *(const f4*)(rd[1] + fb * 1024); p[2][fb] = *(const f4*)(rd[2] + fb * 1024);
      zz[fb] = *(const u2*)(sm + KB_Z1 + (w * 7 + fb) * 512 + l2 * 8);
    }
    __builtin_amdgcn_sched_barrier(0);
#pragma unroll
    for (int fb = 0; fb < 7; ++fb) {
      const f4 sum = (h.acc[fb][0] + p[0][fb]) + (p[1][fb] + p[2][fb]);
      const u2 z1 = zz[fb];
      const unsigned s0 = pk_bf(sum[0], sum[1]), s1 = pk_bf(sum[2], sum[3]);
      const unsigned g0 = pk_bf(bflo(s0) + bb, bfhi(s0) + bb), g1 = pk_bf(bflo(s1) + bb, bfhi(s1) + bb);
      vv[fb] = (u2){pk_bf(bflo(g0) * bflo(z1.x), bfhi(g0) * bfhi(z1.x)), pk_bf(bflo(g1) * bflo(z1.y), bfhi(g1) * bfhi(z1.y))};
    }
    const char* vb = vst + w * (NKS3 * 1024);
    if (gs & 1) {
      asm volatile("global_store_dwordx2 %0, %1, %2 offset:8 sc1" ::"v"(vl), "v"(vv[0]), "s"(vb) : "memory");
#pragma unroll
      for (int p = 0; p < 3; ++p) gst16s(vl + 1024 * (p + 1), cat(vv[2 * p + 1], vv[2 * p + 2]), vb);
    } else {
#pragma unroll
      for (int p = 0; p < 3; ++p) gst16s(vl + 1024 * p, cat(vv[2 * p], vv[2 * p + 1]), vb);
      if (cnt == 7) asm volatile("global_store_dwordx2 %0, %1, %2 offset:3072 sc1" ::"v"(vl), "v"(vv[6]), "s"(vb) : "memory");
    }
  }
  ld_wo_rest<WO_LATE>(h, WO);
  waitv<35 - WO_LATE>();
  if (last_of_four(sb + KB_CNT + 8)) {
    if (l2 == 0) __hip_atomic_store((unsigned*)(ws + OFF_F3) + (8 * i + j) * 32, 1u, __ATOMIC_RELAXED, __HIP_MEMORY_SCOPE_AGENT);
  }
  G3 o;
  const char* vb[4];
#pragma unroll
  for (int m = 0; m < 4; ++m) vb[m] = ws + OFF_V + ((size_t)(4 * i + (m ^ w)) * NKS3 + 7 * w) * 1024;
  unsigned pt[3];
  poll8(pt, (l2 & 7) * 128, ws + OFF_F3 + i * 1024);
  const unsigned vz6 = w == 3 ? h.vz - 1024 : h.vz;
  ld_v<0, V_PRE>(o, h.vz, vz6, vb);
  g3_loop<0>(o, WO, h.slot, h.vz, vz6, vb);
  asm volatile("" : "+v"(pt[0]), "+v"(pt[1]), "+v"(pt[2]));
  asm volatile("s_nop 7" : "+v"(o.acc[0][0]), "+v"(o.acc[1][0]), "+v"(o.acc[2][0]), "+v"(o.acc[3][0]), "+v"(o.acc[4][0]));
  bar();
  const int c3 = l2 & 15, g3 = l2 >> 4;
  const int trow3 = 64 * i + 32 * (w >> 1) + 8 * (c3 >> 2) + 4 * (w & 1) + (c3 & 3);
  const char* hp = sm + (80 * j + 4 * g3 + (c3 >> 2)) * 128 + (((2 * w + ((c3 >> 1) & 1)) ^ (2 * (((c3 >> 3) & 1) | ((g3 >> 1) << 1)))) * 16) + 8 * (c3 & 1);
  u2 y[5];
  {
    f4 p[3][5]; u2 hv[5];
#pragma unroll
    for (int db = 0; db < 5; ++db) {
      p[0][db] = *(const f4*)(rd[0] + db * 1024); p[1][db] = *(const f4*)(rd[1] + db * 1024); p[2][db] = *(const f4*)(rd[2] + db * 1024);
      hv[db] = __builtin_bit_cast(u2, __builtin_amdgcn_ds_read_tr16_b64_v4i16((LDSP s4*)(hp + db * 2048)));
    }
    __builtin_amdgcn_sched_barrier(0);
#pragma unroll
    for (int db = 0; db < 5; ++db) {
      const f4 sum = (o.acc[db][0] + p[0][db]) + (p[1][db] + p[2][db]);
      const unsigned s0 = pk_bf(sum[0], sum[1]), s1 = pk_bf(sum[2], sum[3]);
      y[db] = (u2){pk_bf(bflo(hv[db].x) + bflo(s0), bfhi(hv[db].x) + bfhi(s0)), pk_bf(bflo(hv[db].y) + bflo(s1), bfhi(hv[db].y) + bfhi(s1))};
    }
  }
  const unsigned oo = (unsigned)trow3 * (2 * D) + 160 * j;
#pragma unroll
  for (int p = 0; p < 2; ++p) {
    const auto a = __builtin_amdgcn_permlane16_swap(y[2 * p].x, y[2 * p + 1].x, false, false), b = __builtin_amdgcn_permlane16_swap(y[2 * p].y, y[2 * p + 1].y, false, false);
    asm volatile("global_store_dwordx4 %0, %1, %2 offset:%3\n\ts_nop 1" ::"v"(oo + 32 * (g3 & 1) + 16 * (g3 >> 1)), "v"((u4){a[0], b[0], a[1], b[1]}), "s"(out), "i"(64 * p) : "memory");
  }
  asm volatile("global_store_dwordx2 %0, %1, %2 offset:128" ::"v"(oo + 8 * g3), "v"(y[4]), "s"(out) : "memory");
}

extern "C" void kernel_launch(void* const* d_in, const int* in_sizes, int n_in, void* d_out, int out_size, void* d_ws, size_t ws_size,
                              hipStream_t stream) {
  char* ws = (char*)d_ws;
  ka<<<256, 1024, 0, stream>>>((const u16*)d_in[0], (const u16*)d_in[1], (const u16*)d_in[2], (const u16*)d_in[3], (const u16*)d_in[4],
                              (const unsigned*)d_in[5], (const unsigned*)d_in[6], (const u16*)d_in[8], (const u16*)d_in[9], (const u16*)d_in[11], ws);
  kb<<<256, 256, 0, stream>>>((const u16*)d_in[9], (const u16*)d_in[10], ws, (u16*)d_out);
}
```
